```python
import torch

def _softmax(x, axis=-1):
    return torch.softmax(x, dim=axis)
def _gelu(x):
    return torch.nn.functional.gelu(x, approximate="tanh")
def _silu(x):
    return torch.nn.functional.silu(x)
def _sigmoid(x):
    return torch.sigmoid(x)
def _layernorm(x, axis=-1, eps=1e-5):
    m = x.mean(dim=axis, keepdim=True)
    v = x.var(dim=axis, keepdim=True, unbiased=False)
    return (x - m) / torch.sqrt(v + eps)
def _scatter_add(tgt, idx, src, axis=0):
    return tgt.clone().index_add_(axis, idx, src)
def _segment_sum(src, seg, n):
    out = torch.zeros((n,) + src.shape[1:], dtype=src.dtype)
    return out.index_add_(0, seg, src)
def _pad(x, pw):
    flat = tuple(v for pair in reversed(pw) for v in pair)
    return torch.nn.functional.pad(x, flat)

kernel_name = 'sequential_preLN__spectral_fnet__geglu_ffn__prompt'

def setup_inputs():

    _w = (lambda a: (lambda t: t.to(torch.bfloat16) if t.is_floating_point() else t)(torch.as_tensor(a)))
    def _A_setup():
        B, S, D = 1, 2048, 640
        ns = torch.arange(S)[:, None] * torch.arange(S)[None, :]
        nd = torch.arange(D)[:, None] * torch.arange(D)[None, :]
        Ws_re = torch.cos(-2.0 * torch.pi * ns / S).to(float)
        Ws_im = torch.sin(-2.0 * torch.pi * ns / S).to(float)
        Wd_re = torch.cos(-2.0 * torch.pi * nd / D).to(float)
        Wd_im = torch.sin(-2.0 * torch.pi * nd / D).to(float)
        return {
            "x": _w((lambda *s: torch.randn(*s, dtype=torch.bfloat16) * 0.05)(B, S, D)),
            "dft_seq_re": _w(Ws_re),
            "dft_seq_im": _w(Ws_im),
            "dft_hid_re": _w(Wd_re),
            "dft_hid_im": _w(Wd_im),
        }
    _A_in = _A_setup()

    def _B_setup():
        B, S, D, F = 1, 2048, 640, 1536
        return {
            "x": _w((lambda *s: torch.randn(*s, dtype=torch.bfloat16) * 0.05)(B, S, D)),
            "w_gate": _w((lambda *s: torch.randn(*s, dtype=torch.bfloat16) * 0.2)(D, F)),
            "w_up": _w((lambda *s: torch.randn(*s, dtype=torch.bfloat16) * 0.2)(D, F)),
            "w_down": _w((lambda *s: torch.randn(*s, dtype=torch.bfloat16) * 0.2)(F, D)),
        }
    _B_in = _B_setup()

    return {
        "A_x": _A_in["x"],
        "A_dft_seq_re": _A_in["dft_seq_re"],
        "A_dft_seq_im": _A_in["dft_seq_im"],
        "A_dft_hid_re": _A_in["dft_hid_re"],
        "A_dft_hid_im": _A_in["dft_hid_im"],
        "B_x": _B_in["x"],
        "B_w_gate": _B_in["w_gate"],
        "B_w_up": _B_in["w_up"],
        "B_w_down": _B_in["w_down"],
    }

def reference(A_x, A_dft_seq_re, A_dft_seq_im, A_dft_hid_re, A_dft_hid_im, B_x, B_w_gate, B_w_up, B_w_down):
    def _A_ref(x, dft_seq_re, dft_seq_im, dft_hid_re, dft_hid_im):
        a = torch.einsum("bsd,ed->bse", x, dft_hid_re)
        b = torch.einsum("bsd,ed->bse", x, dft_hid_im)
        y = torch.einsum("ts,bse->bte", dft_seq_re, a) - torch.einsum("ts,bse->bte", dft_seq_im, b)
        return y

    def _B_ref(x, w_gate, w_up, w_down):
        return (_gelu(x @ w_gate) * (x @ w_up)) @ w_down

    x = A_x
    h = x + _A_ref(_layernorm(x, axis=-1), A_dft_seq_re, A_dft_seq_im, A_dft_hid_re, A_dft_hid_im)
    h2 = h + _B_ref(_layernorm(h, axis=-1), B_w_gate, B_w_up, B_w_down)
    return h2
```

```cpp
#include <hip/hip_runtime.h>
#include <hip/hip_bf16.h>
#include <stdint.h>
#include <type_traits>

typedef unsigned short u16;
typedef __attribute__((ext_vector_type(8))) short bf16x8;
typedef __attribute__((ext_vector_type(4))) float f32x4;
typedef __attribute__((ext_vector_type(2))) float f32x2;
typedef __attribute__((ext_vector_type(2))) __bf16 bf16x2;
typedef __attribute__((ext_vector_type(2))) unsigned u32x2;
typedef __attribute__((ext_vector_type(4))) unsigned u32x4;

#define DEVI __device__ __forceinline__
#define IC(x) std::integral_constant<int, (x)>{}
#define SB() __builtin_amdgcn_sched_barrier(0)

constexpr int SEQ = 2048, DM = 640, FF = 1536;

DEVI void glds16(unsigned voff, const char* sbase, unsigned ldsaddr) {
  asm volatile("s_mov_b32 m0, %2\n\ts_nop 0\n\tglobal_load_lds_dwordx4 %0, %1" ::"v"(voff), "s"(sbase), "s"(ldsaddr));
}
template <int N> DEVI void wait_bar() { asm volatile("s_waitcnt vmcnt(%0) lgkmcnt(0)\n\ts_barrier" ::"i"(N) : "memory"); }
DEVI unsigned pack2(float a, float b) { f32x2 p = {a, b}; return __builtin_bit_cast(unsigned, __builtin_convertvector(p, bf16x2)); }
DEVI float bflo(unsigned v) { return __builtin_bit_cast(float, v << 16); }
DEVI float bfhi(unsigned v) { return __builtin_bit_cast(float, v & 0xffff0000u); }
DEVI float dot2(unsigned a, unsigned b, float c) {
  return __builtin_amdgcn_fdot2_f32_bf16(__builtin_bit_cast(bf16x2, a), __builtin_bit_cast(bf16x2, b), c, false);
}
DEVI float sum8(bf16x8 f, float c) {
  u32x4 u = __builtin_bit_cast(u32x4, f);
  c = dot2(u.x, 0x3f803f80u, c); c = dot2(u.y, 0x3f803f80u, c); c = dot2(u.z, 0x3f803f80u, c); c = dot2(u.w, 0x3f803f80u, c);
  return c;
}
DEVI float sq8(bf16x8 f, float c) {
  u32x4 u = __builtin_bit_cast(u32x4, f);
  c = dot2(u.x, u.x, c); c = dot2(u.y, u.y, c); c = dot2(u.z, u.z, c); c = dot2(u.w, u.w, c);
  return c;
}
DEVI float redq(float v) { v += __shfl_xor(v, 16); v += __shfl_xor(v, 32); return v; }

template <int TA, int TB, int NWA, int NWB, int KS, int BK, int NST, int NK> struct Cfg {
  static constexpr int NW = NWA * NWB * KS, NSUB = BK / 64, PPS = (TA + TB) / 8, NP = NSUB * PPS, STG = NP * 1024, SUBB = PPS * 1024;
  static constexpr int WAF = TA / 16 / NWA, WBF = TB / 16 / NWB, KH = (BK / 32) / KS;
  static constexpr int GMAX = (NP + NW - 1) / NW, GREM = NP % NW;
  static constexpr int NF = WAF + WBF, NRD = KH * NF, NMF = KH * WAF * WBF;
  static constexpr int LDS = NST * STG;
};

template <int TA, int TB, int NWA, int NWB, int KS, int BK, int NST, int NK, class RowA, class RowB, class Ptrs, class Hook>
DEVI void gemm_core(f32x4 (&acc)[TA / 16 / NWA][TB / 16 / NWB], char* shm, int wid, int lane, RowA rowA, RowB rowB, Ptrs ptrs, Hook hook) {
  using C = Cfg<TA, TB, NWA, NWB, KS, BK, NST, NK>;
  constexpr int NW = C::NW, PPS = C::PPS, STG = C::STG, SUBB = C::SUBB, WAF = C::WAF, WBF = C::WBF, KH = C::KH;
  constexpr int GMAX = C::GMAX, GREM = C::GREM, NF = C::NF, NRD = C::NRD, NMF = C::NMF;
  const int kg = wid / (NWA * NWB), wab = wid % (NWA * NWB), wa = wab / NWB, wb = wab % NWB;
  unsigned voff[GMAX]; bool isA[GMAX];
#pragma unroll
  for (int i = 0; i < GMAX; ++i) {
    int p = wid + NW * i; if (p >= C::NP) p = C::NP - 1;
    int sub = p / PPS, pr = p % PPS, row = pr * 8 + (lane >> 3);
    int ch = (lane & 7) ^ ((row >> 1) & 7);
    isA[i] = pr < TA / 8;
    voff[i] = (isA[i] ? rowA(row) : rowB(row - TA)) + (unsigned)(ch * 16 + sub * 128);
  }
  const unsigned ldsbase = (unsigned)(size_t)(__attribute__((address_space(3))) char*)shm;
  const bool big = (GREM == 0) || (wid < GREM);
  int kiss = 0; unsigned islot = 0; const char* pa; const char* pb;
  ptrs(0, pa, pb);
  auto adv = [&]() { ++kiss; ptrs(kiss, pa, pb); islot += STG; if (islot == NST * STG) islot = 0; };
  auto issue1 = [&](int i) {
    if (i < GMAX - 1 || GREM == 0 || big) glds16(voff[i], isA[i] ? pa : pb, ldsbase + islot + (wid + NW * i) * 1024);
  };
#pragma unroll
  for (int j = 0; j < WAF; ++j)
#pragma unroll
    for (int b = 0; b < WBF; ++b) acc[j][b] = f32x4{0, 0, 0, 0};
  const int fr = lane & 15, fq = lane >> 4;
  const int c0 = fr * 128 + ((fq ^ (fr >> 1)) * 16);
  int rdo[KH];
#pragma unroll
  for (int h = 0; h < KH; ++h) { int ks = kg * KH + h; rdo[h] = (c0 ^ ((ks & 1) * 64)) + (ks >> 1) * SUBB; }
  const int waoff = wa * WAF * 2048, wboff = TA * 128 + wb * WBF * 2048;
  bf16x8 R[2][KH][NF];
  unsigned rslot = 0;
  auto rd1 = [&](bf16x8 (&Rn)[KH][NF], int r) {
    int h = r / NF, f = r % NF;
    const char* sp = shm + rslot + rdo[h];
    if (f < WAF) Rn[h][f] = *(const bf16x8*)(sp + waoff + f * 2048);
    else Rn[h][f] = *(const bf16x8*)(sp + wboff + (f - WAF) * 2048);
  };
  auto body = [&](bf16x8 (&Rc)[KH][NF], bf16x8 (&Rn)[KH][NF], bool doi, bool dor) {
    int ri = 0, gi = 0, hi = 0;
#pragma unroll
    for (int m = 0; m < NMF; ++m) {
      { int h = m / (WAF * WBF), q = m % (WAF * WBF), n = q / WAF, j = q % WAF;
        acc[j][n] = __builtin_amdgcn_mfma_f32_16x16x32_bf16(Rc[h][j], Rc[h][WAF + n], acc[j][n], 0, 0, 0); }
      int rt = ((m + 1) * NRD + NMF - 1) / NMF; if (rt > NRD) rt = NRD;
#pragma unroll
      for (; ri < rt; ++ri) if (dor) rd1(Rn, ri);
      int gt = ((m + 1) * GMAX + NMF - 1) / NMF; if (gt > GMAX) gt = GMAX;
#pragma unroll
      for (; gi < gt; ++gi) if (doi) issue1(gi);
#pragma unroll
      for (; hi < rt; ++hi) hook(hi % NF, Rc[hi / NF][hi % NF]);
      SB();
    }
    if (doi) adv();
    if (dor) { rslot += STG; if (rslot == NST * STG) rslot = 0; }
  };
  auto wait = [&](auto W) { constexpr int w = decltype(W)::value; if (big) wait_bar<w * GMAX>(); else wait_bar<w * (GMAX - 1)>(); };
#pragma unroll
  for (int s = 0; s < NST; ++s) {
#pragma unroll
    for (int i = 0; i < GMAX; ++i) issue1(i);
    adv();
  }
  wait(IC(NST - 1));
#pragma unroll
  for (int r = 0; r < NRD; ++r) rd1(R[0], r);
  rslot += STG;
  static_assert((NK - NST) % 2 == 0 && NK >= NST, "stage count");
  for (int kt = 0; kt < NK - NST; kt += 2) {
    wait(IC(NST - 2)); body(R[0], R[1], true, true);
    wait(IC(NST - 2)); body(R[1], R[0], true, true);
  }
  auto tail = [&](auto I) {
    constexpr int i = decltype(I)::value;
    if constexpr (i < NST - 1) wait(IC(NST - 2 - i));
    if constexpr (i % 2 == 0) body(R[0], R[1], false, i < NST - 1); else body(R[1], R[0], false, i < NST - 1);
  };
  tail(IC(0)); tail(IC(1));
  if constexpr (NST > 2) tail(IC(2));
  if constexpr (NST > 3) tail(IC(3));
  if constexpr (NST > 4) tail(IC(4));
  if constexpr (NST > 5) tail(IC(5));
  if constexpr (NST > 6) tail(IC(6));
  if constexpr (NST > 7) tail(IC(7));
}

template <int WAF, int WBF, int NWAB, int KS>
DEVI bool ksplit_reduce(f32x4 (&acc)[WAF][WBF], char* shm, int wid, int lane) {
  if constexpr (KS == 1) return true;
  const int kg = wid / NWAB, wab = wid % NWAB;
  f32x4* red = (f32x4*)shm;
  __syncthreads();
  if (kg > 0) {
#pragma unroll
    for (int j = 0; j < WAF; ++j)
#pragma unroll
      for (int n = 0; n < WBF; ++n) red[((((kg - 1) * NWAB + wab) * WAF + j) * WBF + n) * 64 + lane] = acc[j][n];
  }
  __syncthreads();
  if (kg > 0) return false;
#pragma unroll
  for (int g = 0; g < KS - 1; ++g)
#pragma unroll
    for (int j = 0; j < WAF; ++j)
#pragma unroll
      for (int n = 0; n < WBF; ++n) acc[j][n] += red[(((g * NWAB + wab) * WAF + j) * WBF + n) * 64 + lane];
  return true;
}

struct TrUnit { u32x2 v[8]; };
DEVI void tr_load(TrUnit& t, const u16* W, int N, int k0, int n0, int lane) {
  const u16* p = W + (size_t)(k0 + 8 * (lane >> 4)) * N + n0 + 4 * (lane & 15);
#pragma unroll
  for (int i = 0; i < 8; ++i) t.v[i] = *(const u32x2*)(p + (size_t)i * N);
}
DEVI void tr_store(const TrUnit& t, u16* WT, int K, int k0, int n0, int lane) {
  u16* p = WT + (size_t)(n0 + 4 * (lane & 15)) * K + k0 + 8 * (lane >> 4);
  u32x4 o;
#define TRP(W_, SEL) o.x = __builtin_amdgcn_perm(t.v[1].W_, t.v[0].W_, SEL); o.y = __builtin_amdgcn_perm(t.v[3].W_, t.v[2].W_, SEL); \
                     o.z = __builtin_amdgcn_perm(t.v[5].W_, t.v[4].W_, SEL); o.w = __builtin_amdgcn_perm(t.v[7].W_, t.v[6].W_, SEL);
  TRP(x, 0x05040100u) *(u32x4*)(p) = o;
  TRP(x, 0x07060302u) *(u32x4*)(p + K) = o;
  TRP(y, 0x05040100u) *(u32x4*)(p + 2 * K) = o;
  TRP(y, 0x07060302u) *(u32x4*)(p + 3 * K) = o;
#undef TRP
}

constexpr int K1_NST = 6;
using C1 = Cfg<128, 80, 4, 1, 1, 64, K1_NST, 10>;
__global__ __launch_bounds__(256, 1) void k1_hid(const u16* __restrict__ x, const u16* __restrict__ Wre, const u16* __restrict__ Wim,
                                                 const u16* __restrict__ Wg, const u16* __restrict__ Wu, u16* __restrict__ abT,
                                                 u16* __restrict__ WguT) {
  extern __shared__ __attribute__((aligned(16))) char shm[];
  const int bid = blockIdx.x, xcd = bid & 7, idx = bid >> 3;
  const int ts = (xcd >> 1) * 4 + (idx >> 3), te = (xcd & 1) * 8 + (idx & 7);
  const int wid = __builtin_amdgcn_readfirstlane(threadIdx.x >> 6), lane = threadIdx.x & 63;
  TrUnit tu; const int u = bid * 4 + wid; const bool dotr = u < 960;
  const int tgu = u >= 480, tu_i = u - tgu * 480, tk0 = (tu_i / 24) * 32, tn0 = (tu_i % 24) * 64;
  if (dotr) tr_load(tu, tgu ? Wu : Wg, FF, tk0, tn0, lane);

  f32x4 acc[2][5];
  float sx[2] = {0, 0}, sq[2] = {0, 0}, sw[5] = {0, 0, 0, 0, 0};
  const int half = te >> 3;
  const char* Wb = (const char*)(half ? Wim : Wre) + (size_t)(te & 7) * 80 * DM * 2;
  const char* xb = (const char*)x + (size_t)ts * 128 * DM * 2;
  gemm_core<128, 80, 4, 1, 1, 64, K1_NST, 10>(
      acc, shm, wid, lane,
      [&](int r) { int s = (r & ~31) + 8 * ((r >> 2) & 3) + 4 * ((r >> 4) & 1) + (r & 3); return (unsigned)(s * DM * 2); },
      [&](int r) { return (unsigned)(r * DM * 2); },
      [&](int k, const char*& pa, const char*& pb) { pa = xb + k * 128; pb = Wb + k * 128; },
      [&](int f, bf16x8 v) { if (f < 2) { sx[f] = sum8(v, sx[f]); sq[f] = sq8(v, sq[f]); } else sw[f - 2] = sum8(v, sw[f - 2]); });
  const int fr = lane & 15, fq = lane >> 4;
  float rs[2][4], mr[2][4];
#pragma unroll
  for (int a = 0; a < 2; ++a) {
    float s1 = redq(sx[a]), s2 = redq(sq[a]);
    float mean = s1 * (1.0f / DM);
    float var = fmaxf(s2 * (1.0f / DM) - mean * mean, 0.0f);
    float rstd = rsqrtf(var + 1e-5f);
    if (half) rstd = -rstd;
    float m2 = mean * rstd;
#pragma unroll
    for (int r = 0; r < 4; ++r) { rs[a][r] = __shfl(rstd, 4 * fq + r); mr[a][r] = __shfl(m2, 4 * fq + r); }
  }
#pragma unroll
  for (int b = 0; b < 5; ++b) {
    float w = redq(sw[b]);
    u32x4 o;
    o.x = pack2(acc[0][b][0] * rs[0][0] - w * mr[0][0], acc[0][b][1] * rs[0][1] - w * mr[0][1]);
    o.y = pack2(acc[0][b][2] * rs[0][2] - w * mr[0][2], acc[0][b][3] * rs[0][3] - w * mr[0][3]);
    o.z = pack2(acc[1][b][0] * rs[1][0] - w * mr[1][0], acc[1][b][1] * rs[1][1] - w * mr[1][1]);
    o.w = pack2(acc[1][b][2] * rs[1][2] - w * mr[1][2], acc[1][b][3] * rs[1][3] - w * mr[1][3]);
    int e = (te & 7) * 80 + b * 16 + fr;
    *(u32x4*)(abT + (size_t)e * 4096 + half * 2048 + ts * 128 + wid * 32 + fq * 8) = o;
  }
  if (dotr) tr_store(tu, WguT + (size_t)tgu * FF * DM, DM, tk0, tn0, lane);
}

DEVI int perm80(int r) {
  int j = r >> 4, q = (r >> 2) & 3, c = r & 3;
  return j < 4 ? 32 * (j >> 1) + 8 * q + 4 * (j & 1) + c : r;
}

constexpr int K2_NST = 4;
using C2 = Cfg<80, 64, 1, 2, 2, 128, K2_NST, 32>;
__global__ __launch_bounds__(256, 1) void k2_seq(const u16* __restrict__ x, const u16* __restrict__ Cr, const u16* __restrict__ Ci,
                                                 const u16* __restrict__ abT, const u16* __restrict__ Wd, u16* __restrict__ h,
                                                 f32x2* __restrict__ stats, u16* __restrict__ WdT) {
  extern __shared__ __attribute__((aligned(16))) char shm[];
  const int bid = blockIdx.x, xcd = bid & 7, idx = bid >> 3;
  const int tt = xcd * 4 + (idx >> 3), te = idx & 7;
  const int wid = __builtin_amdgcn_readfirstlane(threadIdx.x >> 6), lane = threadIdx.x & 63;
  TrUnit tu; const int u = bid * 2 + (wid - 2); const bool dotr = wid >= 2 && u < 480;
  const int tk0 = (u / 10) * 32, tn0 = (u % 10) * 64;
  if (dotr) tr_load(tu, Wd, DM, tk0, tn0, lane);

  f32x4 acc[5][2];
  const char* ab = (const char*)abT + (size_t)te * 80 * 8192;
  const char* cr = (const char*)Cr + (size_t)tt * 64 * 4096;
  const char* ci = (const char*)Ci + (size_t)tt * 64 * 4096;
  gemm_core<80, 64, 1, 2, 2, 128, K2_NST, 32>(
      acc, shm, wid, lane,
      [&](int r) { return (unsigned)(perm80(r) * 8192); },
      [&](int r) { return (unsigned)(r * 4096); },
      [&](int k, const char*& pa, const char*& pb) { pa = ab + k * 256; pb = k < 16 ? cr + k * 256 : ci + (k - 16) * 256; },
      [&](int, bf16x8) {});
  if (!ksplit_reduce<5, 2, 2, 2>(acc, shm, wid, lane)) {
    if (dotr) tr_store(tu, WdT, FF, tk0, tn0, lane);
    return;
  }
  const int fr = lane & 15, fq = lane >> 4;
#pragma unroll
  for (int n = 0; n < 2; ++n) {
    const int t = tt * 64 + (wid * 2 + n) * 16 + fr;
    const size_t ro = (size_t)t * DM + te * 80;
    u32x4 x0 = *(const u32x4*)(x + ro + 8 * fq), x1 = *(const u32x4*)(x + ro + 32 + 8 * fq);
    u32x2 x2 = *(const u32x2*)(x + ro + 64 + 4 * fq);
    u32x4 o0, o1; u32x2 o2;
    o0.x = pack2(acc[0][n][0] + bflo(x0.x), acc[0][n][1] + bfhi(x0.x)); o0.y = pack2(acc[0][n][2] + bflo(x0.y), acc[0][n][3] + bfhi(x0.y));
    o0.z = pack2(acc[1][n][0] + bflo(x0.z), acc[1][n][1] + bfhi(x0.z)); o0.w = pack2(acc[1][n][2] + bflo(x0.w), acc[1][n][3] + bfhi(x0.w));
    o1.x = pack2(acc[2][n][0] + bflo(x1.x), acc[2][n][1] + bfhi(x1.x)); o1.y = pack2(acc[2][n][2] + bflo(x1.y), acc[2][n][3] + bfhi(x1.y));
    o1.z = pack2(acc[3][n][0] + bflo(x1.z), acc[3][n][1] + bfhi(x1.z)); o1.w = pack2(acc[3][n][2] + bflo(x1.w), acc[3][n][3] + bfhi(x1.w));
    o2.x = pack2(acc[4][n][0] + bflo(x2.x), acc[4][n][1] + bfhi(x2.x)); o2.y = pack2(acc[4][n][2] + bflo(x2.y), acc[4][n][3] + bfhi(x2.y));
    *(u32x4*)(h + ro + 8 * fq) = o0; *(u32x4*)(h + ro + 32 + 8 * fq) = o1; *(u32x2*)(h + ro + 64 + 4 * fq) = o2;
    unsigned w[10] = {o0.x, o0.y, o0.z, o0.w, o1.x, o1.y, o1.z, o1.w, o2.x, o2.y};
    float s = 0;
#pragma unroll
    for (int i = 0; i < 10; ++i) s += bflo(w[i]) + bfhi(w[i]);
    s = redq(s);
    const float mu = s * (1.0f / 80);
    float m2 = 0;
#pragma unroll
    for (int i = 0; i < 10; ++i) { float a = bflo(w[i]) - mu, b = bfhi(w[i]) - mu; m2 += a * a + b * b; }
    m2 = redq(m2);
    if (fq == 0) stats[t * 8 + te] = f32x2{s, m2};
  }
}

constexpr int K3_NST = 4;
using C3 = Cfg<192, 128, 2, 2, 1, 64, K3_NST, 10>;
DEVI float geglu(float g, float u) {
  float z = 1.5957691216057308f * (g + 0.044715f * g * g * g);
  return g * u * __builtin_amdgcn_rcpf(1.0f + __expf(-z));
}
__global__ __launch_bounds__(256, 1) void k3_ffn1(const u16* __restrict__ h, const f32x2* __restrict__ stats, const u16* __restrict__ WguT,
                                                  u16* __restrict__ act) {
  extern __shared__ __attribute__((aligned(16))) char shm[];
  const int bid = blockIdx.x, xcd = bid & 7, idx = bid >> 3;
  const int tm = (xcd >> 2) * 8 + (idx >> 2), tn = (xcd & 3) * 4 + (idx & 3);
  const int wid = __builtin_amdgcn_readfirstlane(threadIdx.x >> 6), lane = threadIdx.x & 63;
  const int wa = wid >> 1, wb = wid & 1;
  f32x4 acc[6][4];
  float cs[6] = {0, 0, 0, 0, 0, 0};
  const char* wbp = (const char*)WguT + (size_t)tn * 96 * DM * 2;
  const char* hb = (const char*)h + (size_t)tm * 128 * DM * 2;
  gemm_core<192, 128, 2, 2, 1, 64, K3_NST, 10>(
      acc, shm, wid, lane,
      [&](int r) { int ha = r / 96, rr = r % 96, p = rr >> 5, gu = (rr >> 4) & 1, q = (rr >> 2) & 3, c = rr & 3;
                   return (unsigned)((gu * FF + 48 * ha + 12 * q + 4 * p + c) * DM * 2); },
      [&](int r) { return (unsigned)(r * DM * 2); },
      [&](int k, const char*& pa, const char*& pb) { pa = wbp + k * 128; pb = hb + k * 128; },
      [&](int f, bf16x8 v) { if (f < 6) cs[f] = sum8(v, cs[f]); });
  const int fr = lane & 15, fq = lane >> 4;
  float csr[6][4];
#pragma unroll
  for (int j = 0; j < 6; ++j) {
    float c = redq(cs[j]);
#pragma unroll
    for (int r = 0; r < 4; ++r) csr[j][r] = __shfl(c, 4 * fq + r);
  }
#pragma unroll
  for (int n = 0; n < 4; ++n) {
    const int m = tm * 128 + (wb * 4 + n) * 16 + fr;
    const f32x4* sp = (const f32x4*)(stats + m * 8);
    f32x4 s0 = sp[0], s1 = sp[1], s2 = sp[2], s3 = sp[3];
    float mean = (s0[0] + s0[2] + s1[0] + s1[2] + s2[0] + s2[2] + s3[0] + s3[2]) * (1.0f / DM);
    float m2 = s0[1] + s0[3] + s1[1] + s1[3] + s2[1] + s2[3] + s3[1] + s3[3];
    float d;
    d = s0[0] * (1.0f / 80) - mean; m2 += 80.0f * d * d; d = s0[2] * (1.0f / 80) - mean; m2 += 80.0f * d * d;
    d = s1[0] * (1.0f / 80) - mean; m2 += 80.0f * d * d; d = s1[2] * (1.0f / 80) - mean; m2 += 80.0f * d * d;
    d = s2[0] * (1.0f / 80) - mean; m2 += 80.0f * d * d; d = s2[2] * (1.0f / 80) - mean; m2 += 80.0f * d * d;
    d = s3[0] * (1.0f / 80) - mean; m2 += 80.0f * d * d; d = s3[2] * (1.0f / 80) - mean; m2 += 80.0f * d * d;
    const float rstd = rsqrtf(m2 * (1.0f / DM) + 1e-5f);
    u16* op = act + (size_t)m * FF + tn * 96 + wa * 48 + 12 * fq;
#pragma unroll
    for (int p = 0; p < 3; ++p) {
      float v[4];
#pragma unroll
      for (int r = 0; r < 4; ++r) {
        float g = rstd * (acc[2 * p][n][r] - mean * csr[2 * p][r]);
        float uu = rstd * (acc[2 * p + 1][n][r] - mean * csr[2 * p + 1][r]);
        v[r] = geglu(g, uu);
      }
      u32x2 o; o.x = pack2(v[0], v[1]); o.y = pack2(v[2], v[3]);
      *(u32x2*)(op + 4 * p) = o;
    }
  }
}

constexpr int K4_NST = 4;
using C4 = Cfg<80, 64, 1, 2, 2, 128, K4_NST, 12>;
__global__ __launch_bounds__(256, 1) void k4_ffn2(const u16* __restrict__ h, const u16* __restrict__ act, const u16* __restrict__ WdT,
                                                  u16* __restrict__ out) {
  extern __shared__ __attribute__((aligned(16))) char shm[];
  const int bid = blockIdx.x, xcd = bid & 7, idx = bid >> 3;
  const int tm = (xcd >> 1) * 8 + (idx >> 2), td = (xcd & 1) * 4 + (idx & 3);
  const int wid = __builtin_amdgcn_readfirstlane(threadIdx.x >> 6), lane = threadIdx.x & 63;
  f32x4 acc[5][2];
  const char* wp = (const char*)WdT + (size_t)td * 80 * FF * 2;
  const char* ap = (const char*)act + (size_t)tm * 64 * FF * 2;
  gemm_core<80, 64, 1, 2, 2, 128, K4_NST, 12>(
      acc, shm, wid, lane,
      [&](int r) { return (unsigned)(perm80(r) * FF * 2); },
      [&](int r) { return (unsigned)(r * FF * 2); },
      [&](int k, const char*& pa, const char*& pb) { pa = wp + k * 256; pb = ap + k * 256; },
      [&](int, bf16x8) {});
  if (!ksplit_reduce<5, 2, 2, 2>(acc, shm, wid, lane)) return;
  const int fr = lane & 15, fq = lane >> 4;
#pragma unroll
  for (int n = 0; n < 2; ++n) {
    const int m = tm * 64 + (wid * 2 + n) * 16 + fr;
    const size_t ro = (size_t)m * DM + td * 80;
    u32x4 x0 = *(const u32x4*)(h + ro + 8 * fq), x1 = *(const u32x4*)(h + ro + 32 + 8 * fq);
    u32x2 x2 = *(const u32x2*)(h + ro + 64 + 4 * fq);
    u32x4 o0, o1; u32x2 o2;
    o0.x = pack2(acc[0][n][0] + bflo(x0.x), acc[0][n][1] + bfhi(x0.x)); o0.y = pack2(acc[0][n][2] + bflo(x0.y), acc[0][n][3] + bfhi(x0.y));
    o0.z = pack2(acc[1][n][0] + bflo(x0.z), acc[1][n][1] + bfhi(x0.z)); o0.w = pack2(acc[1][n][2] + bflo(x0.w), acc[1][n][3] + bfhi(x0.w));
    o1.x = pack2(acc[2][n][0] + bflo(x1.x), acc[2][n][1] + bfhi(x1.x)); o1.y = pack2(acc[2][n][2] + bflo(x1.y), acc[2][n][3] + bfhi(x1.y));
    o1.z = pack2(acc[3][n][0] + bflo(x1.z), acc[3][n][1] + bfhi(x1.z)); o1.w = pack2(acc[3][n][2] + bflo(x1.w), acc[3][n][3] + bfhi(x1.w));
    o2.x = pack2(acc[4][n][0] + bflo(x2.x), acc[4][n][1] + bfhi(x2.x)); o2.y = pack2(acc[4][n][2] + bflo(x2.y), acc[4][n][3] + bfhi(x2.y));
    *(u32x4*)(out + ro + 8 * fq) = o0; *(u32x4*)(out + ro + 32 + 8 * fq) = o1; *(u32x2*)(out + ro + 64 + 4 * fq) = o2;
  }
}

extern "C" void kernel_launch(void* const* d_in, const int* in_sizes, int n_in, void* d_out, int out_size, void* d_ws, size_t ws_size,
                              hipStream_t stream) {
  const u16* x = (const u16*)d_in[0];
  const u16* Cr = (const u16*)d_in[1];
  const u16* Ci = (const u16*)d_in[2];
  const u16* Wre = (const u16*)d_in[3];
  const u16* Wim = (const u16*)d_in[4];
  const u16* Wg = (const u16*)d_in[6];
  const u16* Wu = (const u16*)d_in[7];
  const u16* Wd = (const u16*)d_in[8];
  char* ws = (char*)d_ws;
  u16* abT = (u16*)(ws);
  u16* h = (u16*)(ws + (6 << 20));
  u16* WguT = (u16*)(ws + (10 << 20));
  u16* WdT = (u16*)(ws + (14 << 20));
  u16* act = (u16*)(ws + (16 << 20));
  f32x2* stats = (f32x2*)(ws + (24 << 20));
  u16* out = (u16*)d_out;
  hipFuncSetAttribute((const void*)k1_hid, hipFuncAttributeMaxDynamicSharedMemorySize, C1::LDS);
  hipFuncSetAttribute((const void*)k2_seq, hipFuncAttributeMaxDynamicSharedMemorySize, C2::LDS);
  hipFuncSetAttribute((const void*)k3_ffn1, hipFuncAttributeMaxDynamicSharedMemorySize, C3::LDS);
  hipFuncSetAttribute((const void*)k4_ffn2, hipFuncAttributeMaxDynamicSharedMemorySize, C4::LDS);
  k1_hid<<<256, 256, C1::LDS, stream>>>(x, Wre, Wim, Wg, Wu, abT, WguT);
  k2_seq<<<256, 256, C2::LDS, stream>>>(x, Cr, Ci, abT, Wd, h, stats, WdT);
  k3_ffn1<<<256, 256, C3::LDS, stream>>>(h, stats, WguT, act);
  k4_ffn2<<<256, 256, C4::LDS, stream>>>(h, act, WdT, out);
}
```

```cpp
#include <hip/hip_runtime.h>
#include <hip/hip_bf16.h>
#include <stdint.h>
#include <type_traits>

typedef unsigned short u16;
typedef __attribute__((ext_vector_type(8))) short bf16x8;
typedef __attribute__((ext_vector_type(4))) float f32x4;
typedef __attribute__((ext_vector_type(2))) float f32x2;
typedef __attribute__((ext_vector_type(2))) __bf16 bf16x2;
typedef __attribute__((ext_vector_type(2))) unsigned u32x2;
typedef __attribute__((ext_vector_type(4))) unsigned u32x4;

#define DEVI __device__ __forceinline__
#define IC(x) std::integral_constant<int, (x)>{}
#define SB() __builtin_amdgcn_sched_barrier(0)

constexpr int SEQ = 2048, DM = 640, FF = 1536;

template <int B, int E, class F> DEVI void static_for(F&& f) {
  if constexpr (B < E) { f(IC(B)); static_for<B + 1, E>(f); }
}
DEVI void glds16(unsigned voff, const char* sbase, unsigned ldsaddr) {
  asm volatile("s_mov_b32 m0, %2\n\ts_nop 0\n\tglobal_load_lds_dwordx4 %0, %1" ::"v"(voff), "s"(sbase), "s"(ldsaddr));
}
template <int N> DEVI void waitvm_bar() { asm volatile("s_waitcnt vmcnt(%0) lgkmcnt(0)\n\ts_barrier" ::"i"(N) : "memory"); }
DEVI void lg_bar() { asm volatile("s_waitcnt lgkmcnt(0)\n\ts_barrier" ::: "memory"); }
DEVI unsigned pack2(float a, float b) { f32x2 p = {a, b}; return __builtin_bit_cast(unsigned, __builtin_convertvector(p, bf16x2)); }
DEVI float bflo(unsigned v) { return __builtin_bit_cast(float, v << 16); }
DEVI float bfhi(unsigned v) { return __builtin_bit_cast(float, v & 0xffff0000u); }
DEVI float dot2(unsigned a, unsigned b, float c) {
  return __builtin_amdgcn_fdot2_f32_bf16(__builtin_bit_cast(bf16x2, a), __builtin_bit_cast(bf16x2, b), c, false);
}
DEVI float red8(float v) { v += __shfl_xor(v, 1); v += __shfl_xor(v, 2); v += __shfl_xor(v, 4); return v; }

template <int TA_, int TB_, int NWA_, int NWB_, int KS_, int BK_, int NST_, int NK_> struct Cfg {
  static constexpr int TA = TA_, TB = TB_, NWA = NWA_, NWB = NWB_, KS = KS_, BK = BK_, NST = NST_, NK = NK_;
  static constexpr int NC = NWA * NWB * KS;
  static constexpr int NL = 4;
  static constexpr int NSUB = BK / 64, PPS = (TA + TB) / 8, NP = NSUB * PPS, STG = NP * 1024, SUBB = PPS * 1024;
  static constexpr int WAF = TA / 16 / NWA, WBF = TB / 16 / NWB, KH = (BK / 32) / KS;
  static constexpr int NF = WAF + WBF, NMF = WAF * WBF;
  static constexpr int GBIG = (NP + NL - 1) / NL, NBIG = (NP % NL == 0) ? NL : NP % NL;
  static constexpr int LDS = NST * STG;
  static constexpr int LASTSLOT = (NK - 1) % NST;
  static constexpr int issued(int k) {
    if (k < -1) return -1;
    int a = 2 * k + 3, b = k - 1 + NST, c = NK - 1;
    if (k == -1) b = NST - 1;
    int m = a < b ? a : b; return m < c ? m : c;
  }
};

template <class C, int G, int STATS, class RowA, class RowB, class Ptrs>
DEVI void loader(char* shm, int p0, int lane, RowA rowA, RowB rowB, Ptrs ptrs, float (&s1)[G], float (&s2)[G]) {
  unsigned voff[G]; bool isA[G];
#pragma unroll
  for (int i = 0; i < G; ++i) {
    int p = p0 + i, sub = p / C::PPS, pr = p % C::PPS, row = pr * 8 + (lane >> 3);
    int ch = (lane & 7) ^ ((row >> 1) & 7);
    isA[i] = pr < C::TA / 8;
    voff[i] = (isA[i] ? rowA(row) : rowB(row - C::TA)) + (unsigned)(ch * 16 + sub * 128);
    s1[i] = 0.f; s2[i] = 0.f;
  }
  const unsigned ldsb = (unsigned)(size_t)(__attribute__((address_space(3))) char*)shm + p0 * 1024;
  const char* rd = shm + p0 * 1024 + lane * 16;
  auto stats = [&](int s) {
    if constexpr (STATS) {
#pragma unroll
      for (int i = 0; i < G; ++i) {
        u32x4 u = *(const u32x4*)(rd + (s % C::NST) * C::STG + i * 1024);
        float a = s1[i], b = s2[i];
        a = dot2(u.x, 0x3f803f80u, a); b = dot2(u.x, u.x, b); a = dot2(u.y, 0x3f803f80u, a); b = dot2(u.y, u.y, b);
        a = dot2(u.z, 0x3f803f80u, a); b = dot2(u.z, u.z, b); a = dot2(u.w, 0x3f803f80u, a); b = dot2(u.w, u.w, b);
        asm volatile("" : "+v"(a), "+v"(b));
        s1[i] = a; s2[i] = b;
      }
    }
  };
  static_for<-1, C::NK - 1>([&](auto K) {
    constexpr int k = decltype(K)::value;
    static_for<C::issued(k - 1) + 1, C::issued(k) + 1>([&](auto S) {
      constexpr int s = decltype(S)::value;
      const char *pa, *pb; ptrs(s, pa, pb);
#pragma unroll
      for (int i = 0; i < G; ++i) glds16(voff[i], isA[i] ? pa : pb, ldsb + (s % C::NST) * C::STG + i * 1024);
    });
    if constexpr (k >= 0) stats(k);
    waitvm_bar<(C::issued(k) - (k + 1)) * G>();
  });
  stats(C::NK - 1);
}

template <class C> DEVI void consumer(f32x4 (&acc)[C::WAF][C::WBF], char* shm, int wid, int lane) {
  constexpr int WAF = C::WAF, WBF = C::WBF, KH = C::KH, NF = C::NF, NMF = C::NMF, STG = C::STG, NST = C::NST, NK = C::NK;
  const int kg = wid / (C::NWA * C::NWB), wab = wid % (C::NWA * C::NWB), wa = wab / C::NWB, wb = wab % C::NWB;
#pragma unroll
  for (int j = 0; j < WAF; ++j)
#pragma unroll
    for (int b = 0; b < WBF; ++b) acc[j][b] = f32x4{0, 0, 0, 0};
  const int fr = lane & 15, fq = lane >> 4;
  const int c0 = fr * 128 + ((fq ^ (fr >> 1)) * 16);
  int rda[KH], rdb[KH];
#pragma unroll
  for (int h = 0; h < KH; ++h) {
    int ks = kg * KH + h, o = (c0 ^ ((ks & 1) * 64)) + (ks >> 1) * C::SUBB;
    rda[h] = o + wa * WAF * 2048; rdb[h] = o + C::TA * 128 + wb * WBF * 2048;
  }
  bf16x8 R[2][NF];
  auto rd1 = [&](bf16x8 (&Rn)[NF], unsigned slot, int h, int f) {
    if (f < WAF) Rn[f] = *(const bf16x8*)(shm + slot + rda[h] + f * 2048);
    else Rn[f] = *(const bf16x8*)(shm + slot + rdb[h] + (f - WAF) * 2048);
  };
  auto body = [&](bf16x8 (&Rc)[NF], bf16x8 (&Rn)[NF], unsigned slot, int h, bool dor) {
    int ri = 0;
#pragma unroll
    for (int m = 0; m < NMF; ++m) {
      { int n = m / WAF, j = m % WAF; acc[j][n] = __builtin_amdgcn_mfma_f32_16x16x32_bf16(Rc[j], Rc[WAF + n], acc[j][n], 0, 0, 0); }
      int rt = ((m + 1) * NF * 4 + 3 * NMF - 1) / (3 * NMF); if (rt > NF) rt = NF;
#pragma unroll
      for (; ri < rt; ++ri) if (dor) rd1(Rn, slot, h, ri);
      SB();
    }
  };
  lg_bar();
#pragma unroll
  for (int f = 0; f < NF; ++f) rd1(R[0], 0, 0, f);
  unsigned slot = 0;
  auto stage = [&](auto P, bool last) {
    constexpr int par = decltype(P)::value;
    unsigned nslot = slot + STG; if (nslot == NST * STG) nslot = 0;
#pragma unroll
    for (int h = 0; h < KH; ++h) {
      const int c = (par + h) & 1;
      if (h == KH - 1) { if (!last) { lg_bar(); body(R[c], R[c ^ 1], nslot, 0, true); } else body(R[c], R[c ^ 1], 0, 0, false); }
      else body(R[c], R[c ^ 1], slot, h + 1, true);
    }
    slot = nslot;
  };
  if constexpr (KH % 2 == 0) {
    for (int k = 0; k < NK - 1; ++k) stage(IC(0), false);
    stage(IC(0), true);
  } else {
    static_assert(NK % 2 == 0, "");
    for (int k = 0; k < NK - 2; k += 2) { stage(IC(0), false); stage(IC(1), false); }
    stage(IC(0), false); stage(IC(1), true);
  }
}

struct TrUnit { u32x2 v[8]; };
DEVI void tr_load(TrUnit& t, const u16* W, int N, int k0, int n0, int lane) {
  const u16* p = W + (size_t)(k0 + 8 * (lane >> 4)) * N + n0 + 4 * (lane & 15);
#pragma unroll
  for (int i = 0; i < 8; ++i) t.v[i] = *(const u32x2*)(p + (size_t)i * N);
}
DEVI void tr_store(const TrUnit& t, u16* WT, int K, int k0, int n0, int lane) {
  u16* p = WT + (size_t)(n0 + 4 * (lane & 15)) * K + k0 + 8 * (lane >> 4);
  u32x4 o;
#define TRP(W_, SEL) o.x = __builtin_amdgcn_perm(t.v[1].W_, t.v[0].W_, SEL); o.y = __builtin_amdgcn_perm(t.v[3].W_, t.v[2].W_, SEL); \
                     o.z = __builtin_amdgcn_perm(t.v[5].W_, t.v[4].W_, SEL); o.w = __builtin_amdgcn_perm(t.v[7].W_, t.v[6].W_, SEL);
  TRP(x, 0x05040100u) *(u32x4*)(p) = o;
  TRP(x, 0x07060302u) *(u32x4*)(p + K) = o;
  TRP(y, 0x05040100u) *(u32x4*)(p + 2 * K) = o;
  TRP(y, 0x07060302u) *(u32x4*)(p + 3 * K) = o;
#undef TRP
}

template <class C, int G, bool LN_IS_A>
DEVI void stats_publish(float* st1, float* st2, int p0, int lane, float (&s1)[G], float (&s2)[G], float sgn) {
  static_assert(C::NSUB == 1, "");
#pragma unroll
  for (int i = 0; i < G; ++i) {
    const int row = (p0 + i) * 8 + (lane >> 3);
    const bool ln = ((p0 + i) < C::TA / 8) == LN_IS_A;
    float a = red8(s1[i]), b = red8(s2[i]);
    float mean = a * (1.0f / DM);
    float var = fmaxf(b * (1.0f / DM) - mean * mean, 0.0f);
    float rstd = sgn * rsqrtf(var + 1e-5f);
    if ((lane & 7) == 0) { st1[row] = ln ? rstd : a; st2[row] = mean * rstd; }
  }
}

#define K1_NST 6
using C1 = Cfg<128, 80, 4, 1, 1, 64, K1_NST, 10>;
__global__ __launch_bounds__(512, 2) void k1_hid(const u16* __restrict__ x, const u16* __restrict__ Wre, const u16* __restrict__ Wim,
                                                 const u16* __restrict__ Wg, const u16* __restrict__ Wu, u16* __restrict__ abT,
                                                 u16* __restrict__ WguT) {
  using C = C1;
  extern __shared__ __attribute__((aligned(16))) char shm[];
  const int bid = blockIdx.x, xcd = bid & 7, idx = bid >> 3;
  const int ts = (xcd >> 1) * 4 + (idx >> 3), te = (xcd & 1) * 8 + (idx & 7);
  const int wid = __builtin_amdgcn_readfirstlane(threadIdx.x >> 6), lane = threadIdx.x & 63;
  const int half = te >> 3;
  constexpr int FREE = ((C::LASTSLOT + 1) % C::NST) * C::STG;
  constexpr int FREE2 = ((C::LASTSLOT + 2) % C::NST) * C::STG;
  float* st1 = (float*)(shm + FREE); float* st2 = st1 + 256;
  char* stg = shm + FREE2;
  if (wid >= 4) {
    const int lw = wid - 4;
    TrUnit tu; const int u = bid * 4 + lw; const bool dotr = u < 960;
    const int tgu = u >= 480, tu_i = u - tgu * 480, tk0 = (tu_i / 24) * 32, tn0 = (tu_i % 24) * 64;
    if (dotr) tr_load(tu, tgu ? Wu : Wg, FF, tk0, tn0, lane);
    const char* Wb = (const char*)(half ? Wim : Wre) + (size_t)(te & 7) * 80 * DM * 2;
    const char* xb = (const char*)x + (size_t)ts * 128 * DM * 2;
    auto rowA = [&](int r) { int s = (r & ~31) + 8 * ((r >> 2) & 3) + 4 * ((r >> 4) & 1) + (r & 3); return (unsigned)(s * DM * 2); };
    auto rowB = [&](int r) { return (unsigned)(r * DM * 2); };
    auto ptrs = [&](int k, const char*& pa, const char*& pb) { pa = xb + k * 128; pb = Wb + k * 128; };
    const float sgn = half ? -1.0f : 1.0f;
    if (lw < 2) { float s1[7], s2[7]; loader<C, 7, 1>(shm, lw * 7, lane, rowA, rowB, ptrs, s1, s2); stats_publish<C, 7, true>(st1, st2, lw * 7, lane, s1, s2, sgn); }
    else { float s1[6], s2[6]; loader<C, 6, 1>(shm, 2 + lw * 6, lane, rowA, rowB, ptrs, s1, s2); stats_publish<C, 6, true>(st1, st2, 2 + lw * 6, lane, s1, s2, sgn); }
    lg_bar();
    if (dotr) tr_store(tu, WguT + (size_t)tgu * FF * DM, DM, tk0, tn0, lane);
  } else {
    f32x4 acc[2][5];
    consumer<C>(acc, shm, wid, lane);
    lg_bar();
    const int fr = lane & 15, fq = lane >> 4;
    f32x4 rs0 = *(const f32x4*)(st1 + wid * 32 + 4 * fq), rs1 = *(const f32x4*)(st1 + wid * 32 + 16 + 4 * fq);
    f32x4 mr0 = *(const f32x4*)(st2 + wid * 32 + 4 * fq), mr1 = *(const f32x4*)(st2 + wid * 32 + 16 + 4 * fq);
#pragma unroll
    for (int b = 0; b < 5; ++b) {
      float w = st1[128 + b * 16 + fr];
      f32x4 v0 = acc[0][b] * rs0 - w * mr0, v1 = acc[1][b] * rs1 - w * mr1;
      u32x4 o; o.x = pack2(v0[0], v0[1]); o.y = pack2(v0[2], v0[3]); o.z = pack2(v1[0], v1[1]); o.w = pack2(v1[2], v1[3]);
      *(u32x4*)(stg + (b * 16 + fr) * 272 + wid * 64 + fq * 16) = o;
    }
  }
  lg_bar();
  u16* ob = abT + (size_t)(te & 7) * 80 * 4096 + half * 2048 + ts * 128;
#pragma unroll
  for (int i = 0; i < 3; ++i) {
    int c = threadIdx.x + i * 512;
    if (i < 2 || c < 1280) {
      int row = c >> 4, ch = c & 15;
      *(u32x4*)(ob + (size_t)row * 4096 + ch * 8) = *(const u32x4*)(stg + row * 272 + ch * 16);
    }
  }
}

template <class C>
DEVI void resid_epilogue(f32x4 (&acc)[5][2], bool is_consumer, char* shm, int wid, int lane, u32x4 r0, u32x4 r1, u16* outp) {
  static_assert(C::LASTSLOT == C::NST - 1 && (C::NST - 1) * C::STG >= 2 * 64 * 336, "");
  if (is_consumer) {
    const int fr = lane & 15, fq = lane >> 4, kg = wid >> 1, wb = wid & 1;
#pragma unroll
    for (int n = 0; n < 2; ++n)
#pragma unroll
      for (int j = 0; j < 5; ++j) *(f32x4*)(shm + kg * (64 * 336) + ((wb * 2 + n) * 16 + fr) * 336 + j * 64 + fq * 16) = acc[j][n];
  }
  lg_bar();
#pragma unroll
  for (int i = 0; i < 2; ++i) {
    int c = threadIdx.x + i * 512;
    if (i == 0 || c < 640) {
      int row = c / 10, ch = c % 10;
      const char* p = shm + row * 336 + ch * 32;
      f32x4 a0 = *(const f32x4*)p + *(const f32x4*)(p + 64 * 336), a1 = *(const f32x4*)(p + 16) + *(const f32x4*)(p + 16 + 64 * 336);
      u32x4 r = i == 0 ? r0 : r1, o;
      o.x = pack2(a0[0] + bflo(r.x), a0[1] + bfhi(r.x)); o.y = pack2(a0[2] + bflo(r.y), a0[3] + bfhi(r.y));
      o.z = pack2(a1[0] + bflo(r.z), a1[1] + bfhi(r.z)); o.w = pack2(a1[2] + bflo(r.w), a1[3] + bfhi(r.w));
      *(u32x4*)(outp + (size_t)row * DM + ch * 8) = o;
    }
  }
}
DEVI void resid_load(const u16* rp, u32x4& r0, u32x4& r1) {
  int c = threadIdx.x; r0 = *(const u32x4*)(rp + (size_t)(c / 10) * DM + (c % 10) * 8);
  c += 512; if (c >= 640) c = 639; r1 = *(const u32x4*)(rp + (size_t)(c / 10) * DM + (c % 10) * 8);
}

using C2 = Cfg<80, 64, 1, 2, 2, 128, 4, 32>;
__global__ __launch_bounds__(512, 2) void k2_seq(const u16* __restrict__ x, const u16* __restrict__ Cr, const u16* __restrict__ Ci,
                                                 const u16* __restrict__ abT, const u16* __restrict__ Wd, u16* __restrict__ h,
                                                 u16* __restrict__ WdT) {
  using C = C2;
  extern __shared__ __attribute__((aligned(16))) char shm[];
  const int bid = blockIdx.x, xcd = bid & 7, idx = bid >> 3;
  const int tt = xcd * 4 + (idx >> 3), te = idx & 7;
  const int wid = __builtin_amdgcn_readfirstlane(threadIdx.x >> 6), lane = threadIdx.x & 63;
  u32x4 r0, r1; resid_load(x + (size_t)tt * 64 * DM + te * 80, r0, r1);
  f32x4 acc[5][2];
  if (wid >= 4) {
    const int lw = wid - 4;
    TrUnit tu; const int u = bid * 2 + (lw >> 1); const bool dotr = (lw & 1) && u < 480;
    const int tk0 = (u / 10) * 32, tn0 = (u % 10) * 64;
    if (dotr) tr_load(tu, Wd, DM, tk0, tn0, lane);
    const char* ab = (const char*)abT + (size_t)te * 80 * 8192;
    const char* cr = (const char*)Cr + (size_t)tt * 64 * 4096;
    const char* ci = (const char*)Ci + (size_t)tt * 64 * 4096;
    float s1[9], s2[9];
    loader<C, 9, 0>(shm, lw * 9, lane, [&](int r) { return (unsigned)(r * 8192); }, [&](int r) { return (unsigned)(r * 4096); },
        [&](int k, const char*& pa, const char*& pb) { pa = ab + k * 256; pb = k < 16 ? cr + k * 256 : ci + (k - 16) * 256; }, s1, s2);
    if (dotr) tr_store(tu, WdT, FF, tk0, tn0, lane);
  } else {
    consumer<C>(acc, shm, wid, lane);
  }
  resid_epilogue<C>(acc, wid < 4, shm, wid, lane, r0, r1, h + (size_t)tt * 64 * DM + te * 80);
}

#define K3_NST 4
using C3 = Cfg<192, 128, 2, 2, 1, 64, K3_NST, 10>;
DEVI float geglu(float g, float u) {
  float z = 1.5957691216057308f * (g + 0.044715f * g * g * g);
  return g * u * __builtin_amdgcn_rcpf(1.0f + __expf(-z));
}
__global__ __launch_bounds__(512, 2) void k3_ffn1(const u16* __restrict__ h, const u16* __restrict__ WguT, u16* __restrict__ act) {
  using C = C3;
  extern __shared__ __attribute__((aligned(16))) char shm[];
  const int bid = blockIdx.x, xcd = bid & 7, idx = bid >> 3;
  const int tm = (xcd >> 2) * 8 + (idx >> 2), tn = (xcd & 3) * 4 + (idx & 3);
  const int wid = __builtin_amdgcn_readfirstlane(threadIdx.x >> 6), lane = threadIdx.x & 63;
  constexpr int FREE = ((C::LASTSLOT + 1) % C::NST) * C::STG;
  float* st1 = (float*)(shm + FREE); float* st2 = st1 + 320;
  char* stg = shm + FREE + 4096;
  if (wid >= 4) {
    const int lw = wid - 4;
    const char* wbp = (const char*)WguT + (size_t)tn * 96 * DM * 2;
    const char* hb = (const char*)h + (size_t)tm * 128 * DM * 2;
    float s1[10], s2[10];
    loader<C, 10, 1>(shm, lw * 10, lane,
        [&](int r) { int p = r >> 5, gu = (r >> 4) & 1, c = r & 15; return (unsigned)((gu * FF + 16 * p + c) * DM * 2); },
        [&](int r) { return (unsigned)(r * DM * 2); },
        [&](int k, const char*& pa, const char*& pb) { pa = wbp + k * 128; pb = hb + k * 128; }, s1, s2);
    stats_publish<C, 10, false>(st1, st2, lw * 10, lane, s1, s2, 1.0f);
    lg_bar();
  } else {
    f32x4 acc[6][4];
    consumer<C>(acc, shm, wid, lane);
    lg_bar();
    const int fr = lane & 15, fq = lane >> 4, wa = wid >> 1, wb = wid & 1;
    f32x4 cs[6];
#pragma unroll
    for (int j = 0; j < 6; ++j) cs[j] = *(const f32x4*)(st1 + wa * 96 + j * 16 + 4 * fq);
#pragma unroll
    for (int n = 0; n < 4; ++n) {
      const int ml = (wb * 4 + n) * 16 + fr;
      const float rstd = st1[192 + ml], mr = st2[192 + ml];
#pragma unroll
      for (int p = 0; p < 3; ++p) {
        f32x4 g = acc[2 * p][n] * rstd - mr * cs[2 * p], u = acc[2 * p + 1][n] * rstd - mr * cs[2 * p + 1];
        u32x2 o; o.x = pack2(geglu(g[0], u[0]), geglu(g[1], u[1])); o.y = pack2(geglu(g[2], u[2]), geglu(g[3], u[3]));
        *(u32x2*)(stg + ml * 208 + (wa * 48 + p * 16 + fq * 4) * 2) = o;
      }
    }
  }
  lg_bar();
  u16* ob = act + (size_t)tm * 128 * FF + tn * 96;
#pragma unroll
  for (int i = 0; i < 3; ++i) {
    int c = threadIdx.x + i * 512, row = c / 12, ch = c % 12;
    *(u32x4*)(ob + (size_t)row * FF + ch * 8) = *(const u32x4*)(stg + row * 208 + ch * 16);
  }
}

using C4 = Cfg<80, 64, 1, 2, 2, 128, 4, 12>;
__global__ __launch_bounds__(512, 2) void k4_ffn2(const u16* __restrict__ h, const u16* __restrict__ act, const u16* __restrict__ WdT,
                                                  u16* __restrict__ out) {
  using C = C4;
  extern __shared__ __attribute__((aligned(16))) char shm[];
  const int bid = blockIdx.x, xcd = bid & 7, idx = bid >> 3;
  const int tm = (xcd >> 1) * 8 + (idx >> 2), td = (xcd & 1) * 4 + (idx & 3);
  const int wid = __builtin_amdgcn_readfirstlane(threadIdx.x >> 6), lane = threadIdx.x & 63;
  u32x4 r0, r1; resid_load(h + (size_t)tm * 64 * DM + td * 80, r0, r1);
  f32x4 acc[5][2];
  if (wid >= 4) {
    const int lw = wid - 4;
    const char* wp = (const char*)WdT + (size_t)td * 80 * FF * 2;
    const char* ap = (const char*)act + (size_t)tm * 64 * FF * 2;
    float s1[9], s2[9];
    loader<C, 9, 0>(shm, lw * 9, lane, [&](int r) { return (unsigned)(r * FF * 2); }, [&](int r) { return (unsigned)(r * FF * 2); },
        [&](int k, const char*& pa, const char*& pb) { pa = wp + k * 256; pb = ap + k * 256; }, s1, s2);
  } else {
    consumer<C>(acc, shm, wid, lane);
  }
  resid_epilogue<C>(acc, wid < 4, shm, wid, lane, r0, r1, out + (size_t)tm * 64 * DM + td * 80);
}

static void launch_stage(int which, void* const* d_in, void* d_out, void* d_ws, hipStream_t stream) {
  const u16* x = (const u16*)d_in[0];
  const u16* Cr = (const u16*)d_in[1];
  const u16* Ci = (const u16*)d_in[2];
  const u16* Wre = (const u16*)d_in[3];
  const u16* Wim = (const u16*)d_in[4];
  const u16* Wg = (const u16*)d_in[6];
  const u16* Wu = (const u16*)d_in[7];
  const u16* Wd = (const u16*)d_in[8];
  char* ws = (char*)d_ws;
  u16* abT = (u16*)(ws);
  u16* h = (u16*)(ws + (6 << 20));
  u16* WguT = (u16*)(ws + (10 << 20));
  u16* WdT = (u16*)(ws + (14 << 20));
  u16* act = (u16*)(ws + (16 << 20));
  u16* out = (u16*)d_out;
  if (which == 0) {
    (void)hipFuncSetAttribute((const void*)k1_hid, hipFuncAttributeMaxDynamicSharedMemorySize, C1::LDS);
    k1_hid<<<256, 512, C1::LDS, stream>>>(x, Wre, Wim, Wg, Wu, abT, WguT);
  } else if (which == 1) {
    (void)hipFuncSetAttribute((const void*)k2_seq, hipFuncAttributeMaxDynamicSharedMemorySize, C2::LDS);
    k2_seq<<<256, 512, C2::LDS, stream>>>(x, Cr, Ci, abT, Wd, h, WdT);
  } else if (which == 2) {
    (void)hipFuncSetAttribute((const void*)k3_ffn1, hipFuncAttributeMaxDynamicSharedMemorySize, C3::LDS);
    k3_ffn1<<<256, 512, C3::LDS, stream>>>(h, WguT, act);
  } else {
    (void)hipFuncSetAttribute((const void*)k4_ffn2, hipFuncAttributeMaxDynamicSharedMemorySize, C4::LDS);
    k4_ffn2<<<256, 512, C4::LDS, stream>>>(h, act, WdT, out);
  }
}

extern "C" void kernel_launch(void* const* d_in, const int* in_sizes, int n_in, void* d_out, int out_size, void* d_ws, size_t ws_size,
                              hipStream_t stream) {
  for (int i = 0; i < 4; ++i) launch_stage(i, d_in, d_out, d_ws, stream);
}
```

```cpp
#include <hip/hip_runtime.h>
#include <hip/hip_bf16.h>
#include <stdint.h>
#include <type_traits>

typedef unsigned short u16;
typedef __attribute__((ext_vector_type(8))) short bf16x8;
typedef __attribute__((ext_vector_type(4))) float f32x4;
typedef __attribute__((ext_vector_type(2))) float f32x2;
typedef __attribute__((ext_vector_type(2))) __bf16 bf16x2;
typedef __attribute__((ext_vector_type(2))) unsigned u32x2;
typedef __attribute__((ext_vector_type(4))) unsigned u32x4;

#define DEVI __device__ __forceinline__
#define IC(x) std::integral_constant<int, (x)>{}
#define SB() __builtin_amdgcn_sched_barrier(0)

constexpr int SEQ = 2048, DM = 640, FF = 1536;

template <int B, int E, class F> DEVI void static_for(F&& f) {
  if constexpr (B < E) { f(IC(B)); static_for<B + 1, E>(f); }
}
DEVI void glds16(unsigned voff, const char* sbase, unsigned ldsaddr) {
  asm volatile("s_mov_b32 m0, %2\n\ts_nop 0\n\tglobal_load_lds_dwordx4 %0, %1" ::"v"(voff), "s"(sbase), "s"(ldsaddr));
}
template <int N> DEVI void waitvm_bar() { asm volatile("s_waitcnt vmcnt(%0) lgkmcnt(0)\n\ts_barrier" ::"i"(N) : "memory"); }
DEVI void lg_bar() { asm volatile("s_waitcnt lgkmcnt(0)\n\ts_barrier" ::: "memory"); }
DEVI unsigned pack2(float a, float b) { f32x2 p = {a, b}; return __builtin_bit_cast(unsigned, __builtin_convertvector(p, bf16x2)); }
DEVI float bflo(unsigned v) { return __builtin_bit_cast(float, v << 16); }
DEVI float bfhi(unsigned v) { return __builtin_bit_cast(float, v & 0xffff0000u); }
DEVI float dot2(unsigned a, unsigned b, float c) {
  return __builtin_amdgcn_fdot2_f32_bf16(__builtin_bit_cast(bf16x2, a), __builtin_bit_cast(bf16x2, b), c, false);
}
DEVI float red8(float v) { v += __shfl_xor(v, 1); v += __shfl_xor(v, 2); v += __shfl_xor(v, 4); return v; }

template <int TA_, int TB_, int NWA_, int NWB_, int KS_, int BK_, int NST_, int NK_, int PRE_> struct Cfg {
  static constexpr int TA = TA_, TB = TB_, NWA = NWA_, NWB = NWB_, KS = KS_, BK = BK_, NST = NST_, NK = NK_;
  static constexpr int NC = NWA * NWB * KS;
  static constexpr int NL = 4;
  static constexpr int NSUB = BK / 64, PPS = (TA + TB) / 8, NP = NSUB * PPS, STG = NP * 1024, SUBB = PPS * 1024;
  static constexpr int WAF = TA / 16 / NWA, WBF = TB / 16 / NWB, KH = (BK / 32) / KS;
  static constexpr int NF = WAF + WBF, NMF = WAF * WBF;
  static constexpr int GBIG = (NP + NL - 1) / NL, NBIG = (NP % NL == 0) ? NL : NP % NL;
  static constexpr int LDS = NST * STG;
  static constexpr int LASTSLOT = (NK - 1) % NST;
  static constexpr int issued(int k) {
    if (k < -1) return -1;
    int a = 2 * k + 1 + PRE_, b = k - 1 + NST, c = NK - 1;
    if (k == -1) b = NST - 1;
    int m = a < b ? a : b; return m < c ? m : c;
  }
};

template <class C, int G, class RowA, class RowB, class Ptrs>
DEVI void loader(char* shm, int p0, int lane, RowA rowA, RowB rowB, Ptrs ptrs) {
  unsigned voff[G]; bool isA[G];
#pragma unroll
  for (int i = 0; i < G; ++i) {
    int p = p0 + i, sub = p / C::PPS, pr = p % C::PPS, row = pr * 8 + (lane >> 3);
    int ch = (lane & 7) ^ ((row >> 1) & 7);
    isA[i] = pr < C::TA / 8;
    voff[i] = (isA[i] ? rowA(row) : rowB(row - C::TA)) + (unsigned)(ch * 16 + sub * 128);
  }
  const unsigned ldsb = (unsigned)(size_t)(__attribute__((address_space(3))) char*)shm + p0 * 1024;
  static_for<-1, C::NK - 1>([&](auto K) {
    constexpr int k = decltype(K)::value;
    static_for<C::issued(k - 1) + 1, C::issued(k) + 1>([&](auto S) {
      constexpr int s = decltype(S)::value;
      const char *pa, *pb; ptrs(s, pa, pb);
#pragma unroll
      for (int i = 0; i < G; ++i) glds16(voff[i], isA[i] ? pa : pb, ldsb + (s % C::NST) * C::STG + i * 1024);
    });
    waitvm_bar<(C::issued(k) - (k + 1)) * G>();
  });
}

template <class C, class Extra> DEVI void consumer(f32x4 (&acc)[C::WAF][C::WBF], char* shm, int wid, int lane, Extra extra ) {
  constexpr int WAF = C::WAF, WBF = C::WBF, KH = C::KH, NF = C::NF, NMF = C::NMF, STG = C::STG, NST = C::NST, NK = C::NK;
  const int kg = wid / (C::NWA * C::NWB), wab = wid % (C::NWA * C::NWB), wa = wab / C::NWB, wb = wab % C::NWB;
#pragma unroll
  for (int j = 0; j < WAF; ++j)
#pragma unroll
    for (int b = 0; b < WBF; ++b) acc[j][b] = f32x4{0, 0, 0, 0};
  const int fr = lane & 15, fq = lane >> 4;
  const int c0 = fr * 128 + ((fq ^ (fr >> 1)) * 16);
  int rda[KH], rdb[KH];
#pragma unroll
  for (int h = 0; h < KH; ++h) {
    int ks = kg * KH + h, o = (c0 ^ ((ks & 1) * 64)) + (ks >> 1) * C::SUBB;
    rda[h] = o + wa * WAF * 2048; rdb[h] = o + C::TA * 128 + wb * WBF * 2048;
  }
  bf16x8 R[2][NF];
  auto rd1 = [&](bf16x8 (&Rn)[NF], unsigned slot, int h, int f) {
    if (f < WAF) Rn[f] = *(const bf16x8*)(shm + slot + rda[h] + f * 2048);
    else Rn[f] = *(const bf16x8*)(shm + slot + rdb[h] + (f - WAF) * 2048);
  };
  auto body = [&](bf16x8 (&Rc)[NF], bf16x8 (&Rn)[NF], unsigned slot, int h, bool dor) {
    int ri = 0;
#pragma unroll
    for (int m = 0; m < NMF; ++m) {
      { int n = m / WAF, j = m % WAF; acc[j][n] = __builtin_amdgcn_mfma_f32_16x16x32_bf16(Rc[j], Rc[WAF + n], acc[j][n], 0, 0, 0); }
      int rt = ((m + 1) * NF * 4 + 3 * NMF - 1) / (3 * NMF); if (rt > NF) rt = NF;
#pragma unroll
      for (; ri < rt; ++ri) if (dor) rd1(Rn, slot, h, ri);
      SB();
    }
    extra(Rc);
    SB();
  };
  lg_bar();
#pragma unroll
  for (int f = 0; f < NF; ++f) rd1(R[0], 0, 0, f);
  unsigned slot = 0;
  auto stage = [&](auto P, bool last) {
    constexpr int par = decltype(P)::value;
    unsigned nslot = slot + STG; if (nslot == NST * STG) nslot = 0;
#pragma unroll
    for (int h = 0; h < KH; ++h) {
      const int c = (par + h) & 1;
      if (h == KH - 1) { if (!last) { lg_bar(); body(R[c], R[c ^ 1], nslot, 0, true); } else body(R[c], R[c ^ 1], 0, 0, false); }
      else body(R[c], R[c ^ 1], slot, h + 1, true);
    }
    slot = nslot;
  };
  if constexpr (KH % 2 == 0) {
    for (int k = 0; k < NK - 1; ++k) stage(IC(0), false);
    stage(IC(0), true);
  } else {
    static_assert(NK % 2 == 0, "");
    for (int k = 0; k < NK - 2; k += 2) { stage(IC(0), false); stage(IC(1), false); }
    stage(IC(0), false); stage(IC(1), true);
  }
}

struct TrUnit { u32x2 v[8]; };
DEVI void tr_load(TrUnit& t, const u16* W, int N, int k0, int n0, int lane) {
  const u16* p = W + (size_t)(k0 + 8 * (lane >> 4)) * N + n0 + 4 * (lane & 15);
#pragma unroll
  for (int i = 0; i < 8; ++i) t.v[i] = *(const u32x2*)(p + (size_t)i * N);
}
DEVI void tr_store(const TrUnit& t, u16* WT, int K, int k0, int n0, int lane) {
  u16* p = WT + (size_t)(n0 + 4 * (lane & 15)) * K + k0 + 8 * (lane >> 4);
  u32x4 o;
#define TRP(W_, SEL) o.x = __builtin_amdgcn_perm(t.v[1].W_, t.v[0].W_, SEL); o.y = __builtin_amdgcn_perm(t.v[3].W_, t.v[2].W_, SEL); \
                     o.z = __builtin_amdgcn_perm(t.v[5].W_, t.v[4].W_, SEL); o.w = __builtin_amdgcn_perm(t.v[7].W_, t.v[6].W_, SEL);
  TRP(x, 0x05040100u) *(u32x4*)(p) = o;
  TRP(x, 0x07060302u) *(u32x4*)(p + K) = o;
  TRP(y, 0x05040100u) *(u32x4*)(p + 2 * K) = o;
  TRP(y, 0x07060302u) *(u32x4*)(p + 3 * K) = o;
#undef TRP
}

template <int CTRL> DEVI float dppf(float v) {
  return __builtin_bit_cast(float, __builtin_amdgcn_update_dpp(0, __builtin_bit_cast(int, v), CTRL, 0xf, 0xf, true));
}
DEVI float red16(float v) { v += dppf<0xB1>(v); v += dppf<0x4E>(v); v += dppf<0x141>(v); v += dppf<0x140>(v); return v; }
DEVI f32x4 tr_colsum(const TrUnit& t) {
  f32x4 c = {0, 0, 0, 0};
#pragma unroll
  for (int i = 0; i < 8; ++i) { c[0] += bflo(t.v[i].x); c[1] += bfhi(t.v[i].x); c[2] += bflo(t.v[i].y); c[3] += bfhi(t.v[i].y); }
#pragma unroll
  for (int i = 0; i < 4; ++i) { c[i] += __shfl_xor(c[i], 16); c[i] += __shfl_xor(c[i], 32); }
  return c;
}

#define K1_NST 6
#define K1_PRE 6
using C1 = Cfg<128, 80, 4, 1, 1, 64, K1_NST, 10, K1_PRE>;
__global__ __launch_bounds__(512, 2) void k1_hid(const u16* __restrict__ x, const u16* __restrict__ Wre, const u16* __restrict__ Wim,
                                                 const u16* __restrict__ Wg, const u16* __restrict__ Wu, u16* __restrict__ abT,
                                                 u16* __restrict__ WguT, float* __restrict__ csp) {
  using C = C1;
  extern __shared__ __attribute__((aligned(16))) char shm[];
  const int bid = blockIdx.x, xcd = bid & 7, idx = bid >> 3;
  const int ts = (xcd >> 1) * 4 + (idx >> 3), te = (xcd & 1) * 8 + (idx & 7);
  const int wid = __builtin_amdgcn_readfirstlane(threadIdx.x >> 6), lane = threadIdx.x & 63;
  const int half = te >> 3;
  constexpr int FREE = ((C::LASTSLOT + 1) % C::NST) * C::STG;
  float* wsum = (float*)(shm + FREE);
  char* stg = shm + FREE + 1024;
  if (wid >= 4) {
    const int lw = wid - 4;
    const char* Wb = (const char*)(half ? Wim : Wre) + (size_t)(te & 7) * 80 * DM * 2;
    const char* xb = (const char*)x + (size_t)ts * 128 * DM * 2;
    TrUnit tu; const int u = bid * 4 + lw; const bool dotr = u < 960;
    const int tgu = u >= 480, tu_i = u - tgu * 480, tkb = tu_i / 24, tk0 = tkb * 32, tn0 = (tu_i % 24) * 64;
    if (dotr) tr_load(tu, tgu ? Wu : Wg, FF, tk0, tn0, lane);
    auto rowA = [&](int r) { int s = (r & ~31) + 8 * ((r >> 2) & 3) + 4 * ((r >> 4) & 1) + (r & 3); return (unsigned)(s * DM * 2); };
    auto rowB = [&](int r) { return (unsigned)(r * DM * 2); };
    auto ptrs = [&](int k, const char*& pa, const char*& pb) { pa = xb + k * 128; pb = Wb + k * 128; };
    if (lw < 2) loader<C, 7>(shm, lw * 7, lane, rowA, rowB, ptrs);
    else loader<C, 6>(shm, 2 + lw * 6, lane, rowA, rowB, ptrs);
    if (dotr) {
      tr_store(tu, WguT + (size_t)tgu * FF * DM, DM, tk0, tn0, lane);
      f32x4 c = tr_colsum(tu);
      if (lane < 16) *(f32x4*)(csp + (size_t)tkb * (2 * FF) + tgu * FF + tn0 + 4 * lane) = c;
    }
    lg_bar();
  } else {
    f32x4 acc[2][5];
    f32x4 sx[2] = {{0, 0, 0, 0}, {0, 0, 0, 0}}, sq[2] = {{0, 0, 0, 0}, {0, 0, 0, 0}}, sw = {0, 0, 0, 0}, sw4 = {0, 0, 0, 0};
    const bf16x8 ones = {0x3f80, 0x3f80, 0x3f80, 0x3f80, 0x3f80, 0x3f80, 0x3f80, 0x3f80};
    consumer<C>(acc, shm, wid, lane, [&](bf16x8 (&R)[7]) {
      sx[0] = __builtin_amdgcn_mfma_f32_16x16x32_bf16(R[0], ones, sx[0], 0, 0, 0);
      sx[1] = __builtin_amdgcn_mfma_f32_16x16x32_bf16(R[1], ones, sx[1], 0, 0, 0);
      sq[0] = __builtin_amdgcn_mfma_f32_16x16x32_bf16(R[0], R[0], sq[0], 0, 0, 0);
      sq[1] = __builtin_amdgcn_mfma_f32_16x16x32_bf16(R[1], R[1], sq[1], 0, 0, 0);
      if (wid == 0) { sw = __builtin_amdgcn_mfma_f32_16x16x32_bf16(ones, R[2], sw, 0, 0, 0);
                      sw4 = __builtin_amdgcn_mfma_f32_16x16x32_bf16(ones, R[6], sw4, 0, 0, 0); }
      else if (wid == 1) sw = __builtin_amdgcn_mfma_f32_16x16x32_bf16(ones, R[3], sw, 0, 0, 0);
      else if (wid == 2) sw = __builtin_amdgcn_mfma_f32_16x16x32_bf16(ones, R[4], sw, 0, 0, 0);
      else sw = __builtin_amdgcn_mfma_f32_16x16x32_bf16(ones, R[5], sw, 0, 0, 0);
    } );
    const int fr = lane & 15, fq = lane >> 4;
    if (lane < 16) { wsum[wid * 16 + lane] = sw[0]; if (wid == 0) wsum[64 + lane] = sw4[0]; }
    f32x4 rs[2], mr[2];
    const float sgn = half ? -1.0f : 1.0f;
#pragma unroll
    for (int a = 0; a < 2; ++a)
#pragma unroll
      for (int r = 0; r < 4; ++r) {
        float s2 = __shfl(sq[a][r], 20 * fq + r);
        float mean = sx[a][r] * (1.0f / DM);
        float var = fmaxf(s2 * (1.0f / DM) - mean * mean, 0.0f);
        float rstd = sgn * rsqrtf(var + 1e-5f);
        rs[a][r] = rstd; mr[a][r] = mean * rstd;
      }
    lg_bar();
#pragma unroll
    for (int b = 0; b < 5; ++b) {
      float w = wsum[b * 16 + fr];
      f32x4 v0 = acc[0][b] * rs[0] - w * mr[0], v1 = acc[1][b] * rs[1] - w * mr[1];
      u32x4 o; o.x = pack2(v0[0], v0[1]); o.y = pack2(v0[2], v0[3]); o.z = pack2(v1[0], v1[1]); o.w = pack2(v1[2], v1[3]);
      *(u32x4*)(stg + (b * 16 + fr) * 272 + wid * 64 + fq * 16) = o;
    }
  }
  lg_bar();
  u16* ob = abT + (size_t)(te & 7) * 80 * 4096 + half * 2048 + ts * 128;
#pragma unroll
  for (int i = 0; i < 3; ++i) {
    int c = threadIdx.x + i * 512;
    if (i < 2 || c < 1280) {
      int row = c >> 4, ch = c & 15;
      *(u32x4*)(ob + (size_t)row * 4096 + ch * 8) = *(const u32x4*)(stg + row * 272 + ch * 16);
    }
  }
}

template <class C, bool STATS>
DEVI void resid_epilogue(f32x4 (&acc)[5][2], bool is_consumer, char* shm, int wid, int lane, u32x4 r0, u32x4 r1, u16* outp, f32x2* stats) {
  static_assert(C::LASTSLOT == C::NST - 1 && (C::NST - 1) * C::STG >= 2 * 64 * 336, "");
  if (is_consumer) {
    const int fr = lane & 15, fq = lane >> 4, kg = wid >> 1, wb = wid & 1;
#pragma unroll
    for (int n = 0; n < 2; ++n)
#pragma unroll
      for (int j = 0; j < 5; ++j) *(f32x4*)(shm + kg * (64 * 336) + ((wb * 2 + n) * 16 + fr) * 336 + j * 64 + fq * 16) = acc[j][n];
  }
  lg_bar();
  const int ch = lane & 15; const bool act = ch < 10; const int chc = act ? ch : 9;
#pragma unroll
  for (int i = 0; i < 2; ++i) {
    const int row = (threadIdx.x >> 4) + i * 32;
    const char* p = shm + row * 336 + chc * 32;
    f32x4 a0 = *(const f32x4*)p + *(const f32x4*)(p + 64 * 336), a1 = *(const f32x4*)(p + 16) + *(const f32x4*)(p + 16 + 64 * 336);
    u32x4 r = i == 0 ? r0 : r1, o;
    o.x = pack2(a0[0] + bflo(r.x), a0[1] + bfhi(r.x)); o.y = pack2(a0[2] + bflo(r.y), a0[3] + bfhi(r.y));
    o.z = pack2(a1[0] + bflo(r.z), a1[1] + bfhi(r.z)); o.w = pack2(a1[2] + bflo(r.w), a1[3] + bfhi(r.w));
    if (act) *(u32x4*)(outp + (size_t)row * DM + ch * 8) = o;
    if constexpr (STATS) {
      float v[8] = {bflo(o.x), bfhi(o.x), bflo(o.y), bfhi(o.y), bflo(o.z), bfhi(o.z), bflo(o.w), bfhi(o.w)};
      float s = ((v[0] + v[1]) + (v[2] + v[3])) + ((v[4] + v[5]) + (v[6] + v[7]));
      s = red16(act ? s : 0.0f);
      const float mu = s * (1.0f / 80);
      float m2 = 0;
#pragma unroll
      for (int e = 0; e < 8; ++e) { float d = v[e] - mu; m2 += d * d; }
      m2 = red16(act ? m2 : 0.0f);
      if (ch == 0) stats[row * 8] = f32x2{s, m2};
    }
  }
}
DEVI void resid_load(const u16* rp, u32x4& r0, u32x4& r1) {
  int ch = threadIdx.x & 15; if (ch > 9) ch = 9;
  const int row = threadIdx.x >> 4;
  r0 = *(const u32x4*)(rp + (size_t)row * DM + ch * 8);
  r1 = *(const u32x4*)(rp + (size_t)(row + 32) * DM + ch * 8);
}

#define K2_PRE 4
using C2 = Cfg<80, 64, 1, 2, 2, 128, 4, 32, K2_PRE>;
__global__ __launch_bounds__(512, 2) void k2_seq(const u16* __restrict__ x, const u16* __restrict__ Cr, const u16* __restrict__ Ci,
                                                 const u16* __restrict__ abT, const u16* __restrict__ Wd, u16* __restrict__ h,
                                                 u16* __restrict__ WdT, f32x2* __restrict__ stats) {
  using C = C2;
  extern __shared__ __attribute__((aligned(16))) char shm[];
  const int bid = blockIdx.x, xcd = bid & 7, idx = bid >> 3;
  const int tt = xcd * 4 + (idx >> 3), te = idx & 7;
  const int wid = __builtin_amdgcn_readfirstlane(threadIdx.x >> 6), lane = threadIdx.x & 63;
  u32x4 r0, r1; resid_load(x + (size_t)tt * 64 * DM + te * 80, r0, r1);
  f32x4 acc[5][2];
  if (wid >= 4) {
    const int lw = wid - 4;
    TrUnit tu; const int u = bid * 2 + (lw >> 1); const bool dotr = (lw & 1) && u < 480;
    const int tk0 = (u / 10) * 32, tn0 = (u % 10) * 64;
    if (dotr) tr_load(tu, Wd, DM, tk0, tn0, lane);
    const char* ab = (const char*)abT + (size_t)te * 80 * 8192;
    const char* cr = (const char*)Cr + (size_t)tt * 64 * 4096;
    const char* ci = (const char*)Ci + (size_t)tt * 64 * 4096;
    loader<C, 9>(shm, lw * 9, lane, [&](int r) { return (unsigned)(r * 8192); }, [&](int r) { return (unsigned)(r * 4096); },
        [&](int k, const char*& pa, const char*& pb) { pa = ab + k * 256; pb = k < 16 ? cr + k * 256 : ci + (k - 16) * 256; });
    if (dotr) tr_store(tu, WdT, FF, tk0, tn0, lane);
  } else {
    consumer<C>(acc, shm, wid, lane, [](auto&) {} );
  }
  resid_epilogue<C, true>(acc, wid < 4, shm, wid, lane, r0, r1, h + (size_t)tt * 64 * DM + te * 80, stats + tt * 64 * 8 + te);
}

#define K3_NST 4
#define K3_PRE 3
using C3 = Cfg<192, 128, 2, 2, 1, 64, K3_NST, 10, K3_PRE>;
__global__ __launch_bounds__(512, 2) void k3_ffn1(const u16* __restrict__ h, const u16* __restrict__ WguT, const float* __restrict__ csp,
                                                  const f32x2* __restrict__ stats, u16* __restrict__ act) {
  using C = C3;
  extern __shared__ __attribute__((aligned(16))) char shm[];
  const int bid = blockIdx.x, xcd = bid & 7, idx = bid >> 3;
  const int tm = (xcd >> 2) * 8 + (idx >> 2), tn = (xcd & 3) * 4 + (idx & 3);
  const int wid = __builtin_amdgcn_readfirstlane(threadIdx.x >> 6), lane = threadIdx.x & 63;
  constexpr int FREE = ((C::LASTSLOT + 1) % C::NST) * C::STG;
  float* csum = (float*)(shm + FREE);
  f32x2* rowst = (f32x2*)(shm + FREE + 1024);
  char* stg = shm + FREE + 4096;
  if (wid >= 4) {
    const int lw = wid - 4, lt = threadIdx.x - 256;
    const char* wbp = (const char*)WguT + (size_t)tn * 96 * DM * 2;
    const char* hb = (const char*)h + (size_t)tm * 128 * DM * 2;
    float cp[20]; f32x4 sp[4];
    { int r = lt < 192 ? lt : 191; int col = ((r >> 4) & 1) * FF + tn * 96 + 16 * (r >> 5) + (r & 15);
#pragma unroll
      for (int i = 0; i < 20; ++i) cp[i] = csp[(size_t)i * (2 * FF) + col];
      const f32x4* q = (const f32x4*)(stats + (size_t)(tm * 128 + (lt & 127)) * 8);
#pragma unroll
      for (int i = 0; i < 4; ++i) sp[i] = q[i]; }
    loader<C, 10>(shm, lw * 10, lane,
        [&](int r) { int p = r >> 5, gu = (r >> 4) & 1, c = r & 15; return (unsigned)((gu * FF + 16 * p + c) * DM * 2); },
        [&](int r) { return (unsigned)(r * DM * 2); },
        [&](int k, const char*& pa, const char*& pb) { pa = wbp + k * 128; pb = hb + k * 128; });
    float c = 0;
#pragma unroll
    for (int i = 0; i < 20; ++i) c += cp[i];
    if (lt < 192) csum[lt] = c;
    float mean = 0, m2 = 0;
#pragma unroll
    for (int i = 0; i < 4; ++i) { mean += sp[i][0] + sp[i][2]; m2 += sp[i][1] + sp[i][3]; }
    mean *= (1.0f / DM);
#pragma unroll
    for (int i = 0; i < 4; ++i) {
      float d = sp[i][0] * (1.0f / 80) - mean; m2 += 80.0f * d * d;
      d = sp[i][2] * (1.0f / 80) - mean; m2 += 80.0f * d * d;
    }
    if (lt < 128) rowst[lt] = f32x2{mean, m2 * (1.0f / DM) + 1e-5f};
    lg_bar();
  } else {
    f32x4 acc[6][4];
    consumer<C>(acc, shm, wid, lane, [](auto&) {} );
    lg_bar();
    const int fr = lane & 15, fq = lane >> 4, wa = wid >> 1, wb = wid & 1;
    f32x4 cs[6];
#pragma unroll
    for (int j = 0; j < 6; ++j) cs[j] = *(const f32x4*)(csum + wa * 96 + j * 16 + 4 * fq);
#pragma unroll
    for (int n = 0; n < 4; ++n) {
      const int ml = (wb * 4 + n) * 16 + fr;
      const f32x2 st = rowst[ml];
      const float rstd = rsqrtf(st[1]), nmean = -st[0], ve = st[1];
      const float k1 = -2.3022081985f * rstd;
      const float k2 = k1 * 0.044715f * rstd * rstd;
#pragma unroll
      for (int p = 0; p < 3; ++p) {
        f32x4 G = cs[2 * p] * nmean + acc[2 * p][n], U = cs[2 * p + 1] * nmean + acc[2 * p + 1][n];
        f32x4 z = G * (G * G * k2 + k1);
        f32x4 e; e[0] = __builtin_amdgcn_exp2f(z[0]); e[1] = __builtin_amdgcn_exp2f(z[1]); e[2] = __builtin_amdgcn_exp2f(z[2]); e[3] = __builtin_amdgcn_exp2f(z[3]);
        f32x4 d = e * ve + ve;
        f32x4 r; r[0] = __builtin_amdgcn_rcpf(d[0]); r[1] = __builtin_amdgcn_rcpf(d[1]); r[2] = __builtin_amdgcn_rcpf(d[2]); r[3] = __builtin_amdgcn_rcpf(d[3]);
        f32x4 v = G * U * r;
        u32x2 o; o.x = pack2(v[0], v[1]); o.y = pack2(v[2], v[3]);
        *(u32x2*)(stg + ml * 208 + (wa * 48 + p * 16 + fq * 4) * 2) = o;
      }
    }
  }
  lg_bar();
  u16* ob = act + (size_t)tm * 128 * FF + tn * 96;
#pragma unroll
  for (int i = 0; i < 3; ++i) {
    int c = threadIdx.x + i * 512, row = c / 12, ch = c % 12;
    *(u32x4*)(ob + (size_t)row * FF + ch * 8) = *(const u32x4*)(stg + row * 208 + ch * 16);
  }
}

#define K4_PRE 4
using C4 = Cfg<80, 64, 1, 2, 2, 128, 4, 12, K4_PRE>;
__global__ __launch_bounds__(512, 2) void k4_ffn2(const u16* __restrict__ h, const u16* __restrict__ act, const u16* __restrict__ WdT,
                                                  u16* __restrict__ out) {
  using C = C4;
  extern __shared__ __attribute__((aligned(16))) char shm[];
  const int bid = blockIdx.x, xcd = bid & 7, idx = bid >> 3;
  const int tm = (xcd >> 1) * 8 + (idx >> 2), td = (xcd & 1) * 4 + (idx & 3);
  const int wid = __builtin_amdgcn_readfirstlane(threadIdx.x >> 6), lane = threadIdx.x & 63;
  u32x4 r0, r1; resid_load(h + (size_t)tm * 64 * DM + td * 80, r0, r1);
  f32x4 acc[5][2];
  if (wid >= 4) {
    const int lw = wid - 4;
    const char* wp = (const char*)WdT + (size_t)td * 80 * FF * 2;
    const char* ap = (const char*)act + (size_t)tm * 64 * FF * 2;
    loader<C, 9>(shm, lw * 9, lane, [&](int r) { return (unsigned)(r * FF * 2); }, [&](int r) { return (unsigned)(r * FF * 2); },
        [&](int k, const char*& pa, const char*& pb) { pa = wp + k * 256; pb = ap + k * 256; });
  } else {
    consumer<C>(acc, shm, wid, lane, [](auto&) {} );
  }
  resid_epilogue<C, false>(acc, wid < 4, shm, wid, lane, r0, r1, out + (size_t)tm * 64 * DM + td * 80, nullptr);
}

static void launch_stage(int which, void* const* d_in, void* d_out, void* d_ws, hipStream_t stream) {
  const u16* x = (const u16*)d_in[0];
  const u16* Cr = (const u16*)d_in[1];
  const u16* Ci = (const u16*)d_in[2];
  const u16* Wre = (const u16*)d_in[3];
  const u16* Wim = (const u16*)d_in[4];
  const u16* Wg = (const u16*)d_in[6];
  const u16* Wu = (const u16*)d_in[7];
  const u16* Wd = (const u16*)d_in[8];
  char* ws = (char*)d_ws;
  u16* abT = (u16*)(ws);
  u16* h = (u16*)(ws + (6 << 20));
  u16* WguT = (u16*)(ws + (10 << 20));
  u16* WdT = (u16*)(ws + (14 << 20));
  u16* act = (u16*)(ws + (16 << 20));
  f32x2* stats = (f32x2*)(ws + (24 << 20));
  float* csp = (float*)(ws + (25 << 20));
  u16* out = (u16*)d_out;
  if (which == 0) {
    (void)hipFuncSetAttribute((const void*)k1_hid, hipFuncAttributeMaxDynamicSharedMemorySize, C1::LDS);
    k1_hid<<<256, 512, C1::LDS, stream>>>(x, Wre, Wim, Wg, Wu, abT, WguT, csp);
  } else if (which == 1) {
    (void)hipFuncSetAttribute((const void*)k2_seq, hipFuncAttributeMaxDynamicSharedMemorySize, C2::LDS);
    k2_seq<<<256, 512, C2::LDS, stream>>>(x, Cr, Ci, abT, Wd, h, WdT, stats);
  } else if (which == 2) {
    (void)hipFuncSetAttribute((const void*)k3_ffn1, hipFuncAttributeMaxDynamicSharedMemorySize, C3::LDS);
    k3_ffn1<<<256, 512, C3::LDS, stream>>>(h, WguT, csp, stats, act);
  } else {
    (void)hipFuncSetAttribute((const void*)k4_ffn2, hipFuncAttributeMaxDynamicSharedMemorySize, C4::LDS);
    k4_ffn2<<<256, 512, C4::LDS, stream>>>(h, act, WdT, out);
  }
}

extern "C" void kernel_launch(void* const* d_in, const int* in_sizes, int n_in, void* d_out, int out_size, void* d_ws, size_t ws_size,
                              hipStream_t stream) {
  for (int i = 0; i < 4; ++i) launch_stage(i, d_in, d_out, d_ws, stream);
}
```

```cpp
#include <hip/hip_runtime.h>
#include <hip/hip_bf16.h>
#include <stdint.h>
#include <type_traits>

typedef unsigned short u16;
typedef __attribute__((ext_vector_type(8))) short bf16x8;
typedef __attribute__((ext_vector_type(4))) float f32x4;
typedef __attribute__((ext_vector_type(2))) float f32x2;
typedef __attribute__((ext_vector_type(2))) __bf16 bf16x2;
typedef __attribute__((ext_vector_type(2))) unsigned u32x2;
typedef __attribute__((ext_vector_type(4))) unsigned u32x4;

#define DEVI __device__ __forceinline__
#define IC(x) std::integral_constant<int, (x)>{}
#define SB() __builtin_amdgcn_sched_barrier(0)
#define MFMA(a, b, c) __builtin_amdgcn_mfma_f32_16x16x32_bf16(a, b, c, 0, 0, 0)

constexpr int SEQ = 2048, DM = 640, FF = 1536;

template <int B, int E, class F> DEVI void static_for(F&& f) {
  if constexpr (B < E) { f(IC(B)); static_for<B + 1, E>(f); }
}
DEVI void glds16(unsigned voff, const char* sbase, unsigned ldsaddr) {
  asm volatile("s_mov_b32 m0, %2\n\ts_nop 0\n\tglobal_load_lds_dwordx4 %0, %1" ::"v"(voff), "s"(sbase), "s"(ldsaddr));
}
template <int N> DEVI void waitvm_bar() { asm volatile("s_waitcnt vmcnt(%0)\n\ts_barrier" ::"i"(N) : "memory"); }
DEVI void lg_bar() { asm volatile("s_waitcnt lgkmcnt(0)\n\ts_barrier" ::: "memory"); }
DEVI unsigned pack2(float a, float b) { f32x2 p = {a, b}; return __builtin_bit_cast(unsigned, __builtin_convertvector(p, bf16x2)); }
DEVI float bflo(unsigned v) { return __builtin_bit_cast(float, v << 16); }
DEVI float bfhi(unsigned v) { return __builtin_bit_cast(float, v & 0xffff0000u); }
template <int CTRL> DEVI float dppf(float v) {
  return __builtin_bit_cast(float, __builtin_amdgcn_update_dpp(0, __builtin_bit_cast(int, v), CTRL, 0xf, 0xf, true));
}
DEVI float red16(float v) { v += dppf<0xB1>(v); v += dppf<0x4E>(v); v += dppf<0x141>(v); v += dppf<0x140>(v); return v; }

template <int TA_, int TB_, int NWA_, int NWB_, int KS_, int BK_, int NST_, int NK_, int PRE_> struct Cfg {
  static constexpr int TA = TA_, TB = TB_, NWA = NWA_, NWB = NWB_, KS = KS_, BK = BK_, NST = NST_, NK = NK_;
  static constexpr int NSUB = BK / 64, PA = TA / 8, PB = TB / 8, PPS = PA + PB, STG = NSUB * PPS * 1024, SUBB = PPS * 1024;
  static constexpr int WAF = TA / 16 / NWA, WBF = TB / 16 / NWB, KH = (BK / 32) / KS;
  static constexpr int NF = WAF + WBF, NMF = WAF * WBF;
  static constexpr int LDS = NST * STG;
  static constexpr int LASTSLOT = (NK - 1) % NST;
  static constexpr int issued(int k) {
    if (k < -1) return -1;
    int a = 2 * k + 1 + PRE_, b = k - 1 + NST, c = NK - 1;
    if (k == -1) b = NST - 1;
    int m = a < b ? a : b; return m < c ? m : c;
  }
  static constexpr int steady_begin() { int k = 0; while (!(issued(k) == k - 1 + NST && issued(k - 1) == k - 2 + NST) && k < NK) ++k; return k; }
  static constexpr int K0 = steady_begin() < NK - NST + 1 ? steady_begin() : NK - NST + 1;
  static constexpr int K1 = NK - NST + 1 > K0 ? NK - NST + 1 : K0;
};

template <class C, int GA, int GB, class SA, class SB_, class Ptrs>
DEVI void loader(char* shm, int a0, int b0, int lane, SA sA, unsigned vA, SB_ sB, unsigned vB, Ptrs ptrs) {
  constexpr int G = GA + GB;
  const unsigned sw0 = (unsigned)(((lane & 7) ^ (lane >> 4)) * 16);
  unsigned voff[G], lo[G];
  const unsigned ldsb = (unsigned)(size_t)(__attribute__((address_space(3))) char*)shm;
#pragma unroll
  for (int i = 0; i < GA; ++i) {
    int p = a0 + i, sub = p / C::PA, pr = p - sub * C::PA;
    voff[i] = vA + (sw0 ^ (unsigned)((pr & 1) * 64)) + (sA(pr) + (unsigned)sub * 128u);
    lo[i] = ldsb + sub * C::SUBB + pr * 1024;
  }
#pragma unroll
  for (int i = 0; i < GB; ++i) {
    int p = b0 + i, sub = p / C::PB, pr = p - sub * C::PB;
    voff[GA + i] = vB + (sw0 ^ (unsigned)((pr & 1) * 64)) + (sB(pr) + (unsigned)sub * 128u);
    lo[GA + i] = ldsb + sub * C::SUBB + (C::PA + pr) * 1024;
  }
  auto issue = [&](int s, unsigned slot) {
    const char *pa, *pb; ptrs(s, pa, pb);
#pragma unroll
    for (int i = 0; i < G; ++i) glds16(voff[i], i < GA ? pa : pb, lo[i] + slot);
  };
  auto irregular = [&](auto K) {
    constexpr int k = decltype(K)::value;
    static_for<C::issued(k - 1) + 1, C::issued(k) + 1>([&](auto S) { constexpr int s = decltype(S)::value; issue(s, (s % C::NST) * C::STG); });
    waitvm_bar<(C::issued(k) - (k + 1)) * G>();
  };
  static_for<-1, C::K0>(irregular);
  unsigned slot = ((C::K0 - 1 + C::NST) % C::NST) * C::STG;
  for (int k = C::K0; k < C::K1; ++k) {
    issue(k - 1 + C::NST, slot);
    slot += C::STG; if (slot == C::NST * C::STG) slot = 0;
    waitvm_bar<(C::NST - 2) * G>();
  }
  static_for<C::K1, C::NK - 1>(irregular);
}

template <class C, class Extra> DEVI void consumer(f32x4 (&acc)[C::WAF][C::WBF], char* shm, int wid, int lane, Extra extra ) {
  constexpr int WAF = C::WAF, WBF = C::WBF, KH = C::KH, NF = C::NF, NMF = C::NMF, STG = C::STG, NST = C::NST, NK = C::NK;
  const int kg = wid / (C::NWA * C::NWB), wab = wid % (C::NWA * C::NWB), wa = wab / C::NWB, wb = wab % C::NWB;
#pragma unroll
  for (int j = 0; j < WAF; ++j)
#pragma unroll
    for (int b = 0; b < WBF; ++b) acc[j][b] = f32x4{0, 0, 0, 0};
  const int fr = lane & 15, fq = lane >> 4;
  const int c0 = fr * 128 + ((fq ^ (fr >> 1)) * 16);
  int rda[KH], rdb[KH];
#pragma unroll
  for (int h = 0; h < KH; ++h) {
    int ks = kg * KH + h, o = (c0 ^ ((ks & 1) * 64)) + (ks >> 1) * C::SUBB;
    rda[h] = o + wa * WAF * 2048; rdb[h] = o + C::TA * 128 + wb * WBF * 2048;
  }
  bf16x8 R[2][NF];
  auto rd1 = [&](bf16x8 (&Rn)[NF], unsigned slot, int h, int f) {
    if (f < WAF) Rn[f] = *(const bf16x8*)(shm + slot + rda[h] + f * 2048);
    else Rn[f] = *(const bf16x8*)(shm + slot + rdb[h] + (f - WAF) * 2048);
  };
  auto body = [&](bf16x8 (&Rc)[NF], bf16x8 (&Rn)[NF], unsigned slot, int h, bool dor) {
    int ri = 0;
#pragma unroll
    for (int m = 0; m < NMF; ++m) {
      { int n = m / WAF, j = m % WAF; acc[j][n] = MFMA(Rc[j], Rc[WAF + n], acc[j][n]); }
      int rt = ((m + 1) * NF * 4 + 3 * NMF - 1) / (3 * NMF); if (rt > NF) rt = NF;
#pragma unroll
      for (; ri < rt; ++ri) if (dor) rd1(Rn, slot, h, ri);
      SB();
    }
    extra(Rc);
    SB();
  };
  lg_bar();
#pragma unroll
  for (int f = 0; f < NF; ++f) rd1(R[0], 0, 0, f);
  unsigned slot = 0;
  auto stage = [&](auto P, bool last) {
    constexpr int par = decltype(P)::value;
    unsigned nslot = slot + STG; if (nslot == NST * STG) nslot = 0;
#pragma unroll
    for (int h = 0; h < KH; ++h) {
      const int c = (par + h) & 1;
      if (h == KH - 1) { if (!last) { lg_bar(); body(R[c], R[c ^ 1], nslot, 0, true); } else body(R[c], R[c ^ 1], 0, 0, false); }
      else body(R[c], R[c ^ 1], slot, h + 1, true);
    }
    slot = nslot;
  };
  if constexpr (KH % 2 == 0) {
    for (int k = 0; k < NK - 1; ++k) stage(IC(0), false);
    stage(IC(0), true);
  } else {
    static_assert(NK % 2 == 0, "");
    for (int k = 0; k < NK - 2; k += 2) { stage(IC(0), false); stage(IC(1), false); }
    stage(IC(0), false); stage(IC(1), true);
  }
}

struct TrUnit { u32x2 v[8]; };
DEVI void tr_load(TrUnit& t, const u16* W, int N, int k0, int n0, int lane) {
  const u16* p = W + (size_t)(k0 + 8 * (lane >> 4)) * N + n0 + 4 * (lane & 15);
#pragma unroll
  for (int i = 0; i < 8; ++i) t.v[i] = *(const u32x2*)(p + (size_t)i * N);
}
DEVI void tr_store(const TrUnit& t, u16* WT, int K, int k0, int n0, int lane) {
  u16* p = WT + (size_t)(n0 + 4 * (lane & 15)) * K + k0 + 8 * (lane >> 4);
  u32x4 o;
#define TRP(W_, SEL) o.x = __builtin_amdgcn_perm(t.v[1].W_, t.v[0].W_, SEL); o.y = __builtin_amdgcn_perm(t.v[3].W_, t.v[2].W_, SEL); \
                     o.z = __builtin_amdgcn_perm(t.v[5].W_, t.v[4].W_, SEL); o.w = __builtin_amdgcn_perm(t.v[7].W_, t.v[6].W_, SEL);
  TRP(x, 0x05040100u) *(u32x4*)(p) = o;
  TRP(x, 0x07060302u) *(u32x4*)(p + K) = o;
  TRP(y, 0x05040100u) *(u32x4*)(p + 2 * K) = o;
  TRP(y, 0x07060302u) *(u32x4*)(p + 3 * K) = o;
#undef TRP
}
DEVI f32x4 tr_colsum(const TrUnit& t) {
  f32x4 c = {0, 0, 0, 0};
#pragma unroll
  for (int i = 0; i < 8; ++i) { c[0] += bflo(t.v[i].x); c[1] += bfhi(t.v[i].x); c[2] += bflo(t.v[i].y); c[3] += bfhi(t.v[i].y); }
#pragma unroll
  for (int i = 0; i < 4; ++i) { c[i] += __shfl_xor(c[i], 16); c[i] += __shfl_xor(c[i], 32); }
  return c;
}

#define K1_NST 6
#define K1_PRE 3
using C1 = Cfg<128, 80, 4, 1, 1, 64, K1_NST, 10, K1_PRE>;
template <int W> DEVI void k1_consumer(char* shm, float* wsum, char* stg, int lane, float sgn ) {
  using C = C1;
  f32x4 acc[2][5];
  f32x4 sx[2] = {{0, 0, 0, 0}, {0, 0, 0, 0}}, sq[2] = {{0, 0, 0, 0}, {0, 0, 0, 0}}, sw = {0, 0, 0, 0}, sw4 = {0, 0, 0, 0};
  bf16x8 ones = {0x3f80, 0x3f80, 0x3f80, 0x3f80, 0x3f80, 0x3f80, 0x3f80, 0x3f80};
  asm volatile("" : "+v"(ones));
  consumer<C>(acc, shm, W, lane, [&](bf16x8 (&R)[7]) {
    sx[0] = MFMA(R[0], ones, sx[0]); sx[1] = MFMA(R[1], ones, sx[1]);
    sq[0] = MFMA(R[0], R[0], sq[0]); sq[1] = MFMA(R[1], R[1], sq[1]);
    sw = MFMA(ones, R[2 + W], sw);
    if constexpr (W == 0) sw4 = MFMA(ones, R[6], sw4);
  } );
  const int fr = lane & 15, fq = lane >> 4;
  if (lane < 16) { wsum[W * 16 + lane] = sw[0]; if constexpr (W == 0) wsum[64 + lane] = sw4[0]; }
  f32x4 rs[2], mr[2];
#pragma unroll
  for (int a = 0; a < 2; ++a)
#pragma unroll
    for (int r = 0; r < 4; ++r) {
      float s2 = __shfl(sq[a][r], 20 * fq + r);
      float mean = sx[a][r] * (1.0f / DM);
      float var = fmaxf(s2 * (1.0f / DM) - mean * mean, 0.0f);
      float rstd = sgn * rsqrtf(var + 1e-5f);
      rs[a][r] = rstd; mr[a][r] = mean * rstd;
    }
  lg_bar();
#pragma unroll
  for (int b = 0; b < 5; ++b) {
    float w = wsum[b * 16 + fr];
    f32x4 v0 = acc[0][b] * rs[0] - w * mr[0], v1 = acc[1][b] * rs[1] - w * mr[1];
    u32x4 o; o.x = pack2(v0[0], v0[1]); o.y = pack2(v0[2], v0[3]); o.z = pack2(v1[0], v1[1]); o.w = pack2(v1[2], v1[3]);
    *(u32x4*)(stg + (b * 16 + fr) * 272 + W * 64 + fq * 16) = o;
  }
}
__global__ __launch_bounds__(512, 2) void k1_hid(const u16* __restrict__ x, const u16* __restrict__ Wre, const u16* __restrict__ Wim,
                                                 const u16* __restrict__ Wg, const u16* __restrict__ Wu, u16* __restrict__ abT,
                                                 u16* __restrict__ WguT, float* __restrict__ csp) {
  using C = C1;
  extern __shared__ __attribute__((aligned(16))) char shm[];
  const int bid = blockIdx.x, xcd = bid & 7, idx = bid >> 3;
  const int ts = (xcd >> 1) * 4 + (idx >> 3), te = (xcd & 1) * 8 + (idx & 7);
  const int wid = __builtin_amdgcn_readfirstlane(threadIdx.x >> 6), lane = threadIdx.x & 63;
  const int half = te >> 3;
  constexpr int FREE = ((C::LASTSLOT + 1) % C::NST) * C::STG;
  float* wsum = (float*)(shm + FREE);
  char* stg = shm + FREE + 1024;
  if (wid >= 4) {
    const int lw = wid - 4, j = lane >> 3;
    const char* Wb = (const char*)(half ? Wim : Wre) + (size_t)(te & 7) * 80 * DM * 2;
    const char* xb = (const char*)x + (size_t)ts * 128 * DM * 2;
    auto sA = [&](int p) { return (unsigned)((32 * (p >> 2) + 16 * (p & 1) + 4 * ((p >> 1) & 1)) * DM * 2); };
    auto sB = [&](int p) { return (unsigned)(p * 8 * DM * 2); };
    auto ptrs = [&](int k, const char*& pa, const char*& pb) { pa = xb + k * 128; pb = Wb + k * 128; };
    const unsigned vA = (unsigned)((8 * (j >> 2) + (j & 3)) * DM * 2), vB = (unsigned)(j * DM * 2);
    if (lw < 2) loader<C, 4, 3>(shm, lw * 4, lw * 3, lane, sA, vA, sB, vB, ptrs);
    else loader<C, 4, 2>(shm, lw * 4, 2 + lw * 2, lane, sA, vA, sB, vB, ptrs);
    lg_bar();
  } else {
    { TrUnit tu; const int u = bid * 4 + wid;
      if (u < 960) {
        const int tgu = u >= 480, tu_i = u - tgu * 480, tkb = tu_i / 24, tk0 = tkb * 32, tn0 = (tu_i % 24) * 64;
        tr_load(tu, tgu ? Wu : Wg, FF, tk0, tn0, lane);
        tr_store(tu, WguT + (size_t)tgu * FF * DM, DM, tk0, tn0, lane);
        f32x4 c = tr_colsum(tu);
        if (lane < 16) *(f32x4*)(csp + (size_t)tkb * (2 * FF) + tgu * FF + tn0 + 4 * lane) = c;
      } }
    const float sgn = half ? -1.0f : 1.0f;
    if (wid == 0) k1_consumer<0>(shm, wsum, stg, lane, sgn );
    else if (wid == 1) k1_consumer<1>(shm, wsum, stg, lane, sgn );
    else if (wid == 2) k1_consumer<2>(shm, wsum, stg, lane, sgn );
    else k1_consumer<3>(shm, wsum, stg, lane, sgn );
  }
  lg_bar();
  u16* ob = abT + (size_t)(te & 7) * 80 * 4096 + half * 2048 + ts * 128;
#pragma unroll
  for (int i = 0; i < 3; ++i) {
    int c = threadIdx.x + i * 512;
    if (i < 2 || c < 1280) {
      int row = c >> 4, ch = c & 15;
      *(u32x4*)(ob + (size_t)row * 4096 + ch * 8) = *(const u32x4*)(stg + row * 272 + ch * 16);
    }
  }
}

template <class C, bool STATS>
DEVI void resid_epilogue(f32x4 (&acc)[5][2], bool is_consumer, char* shm, int wid, int lane, u32x4 r0, u32x4 r1, u16* outp, f32x2* stats) {
  static_assert(C::LASTSLOT == C::NST - 1 && (C::NST - 1) * C::STG >= 2 * 64 * 336, "");
  if (is_consumer) {
    const int fr = lane & 15, fq = lane >> 4, kg = wid >> 1, wb = wid & 1;
#pragma unroll
    for (int n = 0; n < 2; ++n)
#pragma unroll
      for (int j = 0; j < 5; ++j) *(f32x4*)(shm + kg * (64 * 336) + ((wb * 2 + n) * 16 + fr) * 336 + j * 64 + fq * 16) = acc[j][n];
  }
  lg_bar();
  const int ch = lane & 15; const bool act = ch < 10; const int chc = act ? ch : 9;
#pragma unroll
  for (int i = 0; i < 2; ++i) {
    const int row = (threadIdx.x >> 4) + i * 32;
    const char* p = shm + row * 336 + chc * 32;
    f32x4 a0 = *(const f32x4*)p + *(const f32x4*)(p + 64 * 336), a1 = *(const f32x4*)(p + 16) + *(const f32x4*)(p + 16 + 64 * 336);
    u32x4 r = i == 0 ? r0 : r1, o;
    o.x = pack2(a0[0] + bflo(r.x), a0[1] + bfhi(r.x)); o.y = pack2(a0[2] + bflo(r.y), a0[3] + bfhi(r.y));
    o.z = pack2(a1[0] + bflo(r.z), a1[1] + bfhi(r.z)); o.w = pack2(a1[2] + bflo(r.w), a1[3] + bfhi(r.w));
    if (act) *(u32x4*)(outp + (size_t)row * DM + ch * 8) = o;
    if constexpr (STATS) {
      float v[8] = {bflo(o.x), bfhi(o.x), bflo(o.y), bfhi(o.y), bflo(o.z), bfhi(o.z), bflo(o.w), bfhi(o.w)};
      float s = ((v[0] + v[1]) + (v[2] + v[3])) + ((v[4] + v[5]) + (v[6] + v[7]));
      s = red16(act ? s : 0.0f);
      const float mu = s * (1.0f / 80);
      float m2 = 0;
#pragma unroll
      for (int e = 0; e < 8; ++e) { float d = v[e] - mu; m2 += d * d; }
      m2 = red16(act ? m2 : 0.0f);
      if (ch == 0) stats[row * 8] = f32x2{s, m2};
    }
  }
}
DEVI void resid_load(const u16* rp, u32x4& r0, u32x4& r1) {
  int ch = threadIdx.x & 15; if (ch > 9) ch = 9;
  const int row = threadIdx.x >> 4;
  r0 = *(const u32x4*)(rp + (size_t)row * DM + ch * 8);
  r1 = *(const u32x4*)(rp + (size_t)(row + 32) * DM + ch * 8);
}

#define K2_PRE 4
using C2 = Cfg<80, 64, 1, 2, 2, 128, 4, 32, K2_PRE>;
__global__ __launch_bounds__(512, 2) void k2_seq(const u16* __restrict__ x, const u16* __restrict__ Cr, const u16* __restrict__ Ci,
                                                 const u16* __restrict__ abT, const u16* __restrict__ Wd, const float* __restrict__ csp,
                                                 u16* __restrict__ h, u16* __restrict__ WdT, f32x2* __restrict__ stats, float* __restrict__ csum) {
  using C = C2;
  extern __shared__ __attribute__((aligned(16))) char shm[];
  const int bid = blockIdx.x, xcd = bid & 7, idx = bid >> 3;
  const int tt = xcd * 4 + (idx >> 3), te = idx & 7;
  const int wid = __builtin_amdgcn_readfirstlane(threadIdx.x >> 6), lane = threadIdx.x & 63;
  u32x4 r0, r1;
  f32x4 acc[5][2];
  if (wid >= 4) {
    const int lw = wid - 4, j = lane >> 3;
    const char* ab = (const char*)abT + (size_t)te * 80 * 8192;
    const char* cr = (const char*)Cr + (size_t)tt * 64 * 4096;
    const char* ci = (const char*)Ci + (size_t)tt * 64 * 4096;
    loader<C, 5, 4>(shm, lw * 5, lw * 4, lane, [&](int p) { return (unsigned)(p * 8 * 8192); }, (unsigned)(j * 8192),
        [&](int p) { return (unsigned)(p * 8 * 4096); }, (unsigned)(j * 4096),
        [&](int k, const char*& pa, const char*& pb) { pa = ab + k * 256; pb = k < 16 ? cr + k * 256 : ci + (k - 16) * 256; });
    resid_load(x + (size_t)tt * 64 * DM + te * 80, r0, r1);
  } else {
    resid_load(x + (size_t)tt * 64 * DM + te * 80, r0, r1);
    if (wid < 2) {
      const int u = bid * 2 + wid;
      if (u < 480) { TrUnit tu; const int tk0 = (u / 10) * 32, tn0 = (u % 10) * 64;
        tr_load(tu, Wd, DM, tk0, tn0, lane); tr_store(tu, WdT, FF, tk0, tn0, lane); }
    } else if (wid == 2 && bid < 48) {
      const int col = bid * 64 + lane; float c = 0;
#pragma unroll
      for (int i = 0; i < 20; ++i) c += csp[(size_t)i * (2 * FF) + col];
      csum[col] = c;
    }
    consumer<C>(acc, shm, wid, lane, [](auto&) {} );
  }
  resid_epilogue<C, true>(acc, wid < 4, shm, wid, lane, r0, r1, h + (size_t)tt * 64 * DM + te * 80, stats + tt * 64 * 8 + te);
}

#define K3_NST 4
#define K3_PRE 2
using C3 = Cfg<192, 128, 2, 2, 1, 64, K3_NST, 10, K3_PRE>;
__global__ __launch_bounds__(512, 2) void k3_ffn1(const u16* __restrict__ h, const u16* __restrict__ WguT, const float* __restrict__ csum,
                                                  const f32x2* __restrict__ stats, u16* __restrict__ act) {
  using C = C3;
  extern __shared__ __attribute__((aligned(16))) char shm[];
  const int bid = blockIdx.x, xcd = bid & 7, idx = bid >> 3;
  const int tm = (xcd >> 2) * 8 + (idx >> 2), tn = (xcd & 3) * 4 + (idx & 3);
  const int wid = __builtin_amdgcn_readfirstlane(threadIdx.x >> 6), lane = threadIdx.x & 63;
  constexpr int FREE = ((C::LASTSLOT + 1) % C::NST) * C::STG;
  char* stg = shm + FREE;
  if (wid >= 4) {
    const int lw = wid - 4, j = lane >> 3;
    const char* wbp = (const char*)WguT + (size_t)tn * 96 * DM * 2;
    const char* hb = (const char*)h + (size_t)tm * 128 * DM * 2;
    loader<C, 6, 4>(shm, lw * 6, lw * 4, lane,
        [&](int p) { return (unsigned)((((p >> 1) & 1) * FF + 16 * (p >> 2) + 8 * (p & 1)) * DM * 2); }, (unsigned)(j * DM * 2),
        [&](int p) { return (unsigned)(p * 8 * DM * 2); }, (unsigned)(j * DM * 2),
        [&](int k, const char*& pa, const char*& pb) { pa = wbp + k * 128; pb = hb + k * 128; });
  } else {
    const int fr = lane & 15, fq = lane >> 4, wa = wid >> 1, wb = wid & 1;
    f32x4 cs[6]; float nmean[4], ve[4];
#pragma unroll
    for (int j = 0; j < 6; ++j) cs[j] = *(const f32x4*)(csum + (j & 1) * FF + tn * 96 + wa * 48 + (j >> 1) * 16 + 4 * fq);
#pragma unroll
    for (int n = 0; n < 4; ++n) {
      const f32x4* q = (const f32x4*)(stats + (size_t)(tm * 128 + (wb * 4 + n) * 16 + fr) * 8);
      f32x4 sp[4] = {q[0], q[1], q[2], q[3]};
      float mean = 0, m2 = 0;
#pragma unroll
      for (int i = 0; i < 4; ++i) { mean += sp[i][0] + sp[i][2]; m2 += sp[i][1] + sp[i][3]; }
      mean *= (1.0f / DM);
#pragma unroll
      for (int i = 0; i < 4; ++i) {
        float d = sp[i][0] * (1.0f / 80) - mean; m2 += 80.0f * d * d;
        d = sp[i][2] * (1.0f / 80) - mean; m2 += 80.0f * d * d;
      }
      nmean[n] = -mean; ve[n] = m2 * (1.0f / DM) + 1e-5f;
      asm volatile("" : "+v"(nmean[n]), "+v"(ve[n]));
    }
    f32x4 acc[6][4];
    consumer<C>(acc, shm, wid, lane, [](auto&) {} );
#pragma unroll
    for (int n = 0; n < 4; ++n) {
      const int ml = (wb * 4 + n) * 16 + fr;
      const float rstd = rsqrtf(ve[n]);
      const float k1 = -2.3022081985f * rstd;
      const float k2 = k1 * 0.044715f * rstd * rstd;
#pragma unroll
      for (int p = 0; p < 3; ++p) {
        f32x4 G = cs[2 * p] * nmean[n] + acc[2 * p][n], U = cs[2 * p + 1] * nmean[n] + acc[2 * p + 1][n];
        f32x4 z = G * (G * G * k2 + k1);
        f32x4 e; e[0] = __builtin_amdgcn_exp2f(z[0]); e[1] = __builtin_amdgcn_exp2f(z[1]); e[2] = __builtin_amdgcn_exp2f(z[2]); e[3] = __builtin_amdgcn_exp2f(z[3]);
        f32x4 d = e * ve[n] + ve[n];
        f32x4 r; r[0] = __builtin_amdgcn_rcpf(d[0]); r[1] = __builtin_amdgcn_rcpf(d[1]); r[2] = __builtin_amdgcn_rcpf(d[2]); r[3] = __builtin_amdgcn_rcpf(d[3]);
        f32x4 v = G * U * r;
        u32x2 o; o.x = pack2(v[0], v[1]); o.y = pack2(v[2], v[3]);
        *(u32x2*)(stg + ml * 208 + (wa * 48 + p * 16 + fq * 4) * 2) = o;
      }
    }
  }
  lg_bar();
  u16* ob = act + (size_t)tm * 128 * FF + tn * 96;
#pragma unroll
  for (int i = 0; i < 3; ++i) {
    int c = threadIdx.x + i * 512, row = c / 12, ch = c % 12;
    *(u32x4*)(ob + (size_t)row * FF + ch * 8) = *(const u32x4*)(stg + row * 208 + ch * 16);
  }
}

#define K4_PRE 4
using C4 = Cfg<80, 64, 1, 2, 2, 128, 4, 12, K4_PRE>;
__global__ __launch_bounds__(512, 2) void k4_ffn2(const u16* __restrict__ h, const u16* __restrict__ act, const u16* __restrict__ WdT,
                                                  u16* __restrict__ out) {
  using C = C4;
  extern __shared__ __attribute__((aligned(16))) char shm[];
  const int bid = blockIdx.x, xcd = bid & 7, idx = bid >> 3;
  const int tm = (xcd >> 1) * 8 + (idx >> 2), td = (xcd & 1) * 4 + (idx & 3);
  const int wid = __builtin_amdgcn_readfirstlane(threadIdx.x >> 6), lane = threadIdx.x & 63;
  u32x4 r0, r1;
  f32x4 acc[5][2];
  if (wid >= 4) {
    const int lw = wid - 4, j = lane >> 3;
    const char* wp = (const char*)WdT + (size_t)td * 80 * FF * 2;
    const char* ap = (const char*)act + (size_t)tm * 64 * FF * 2;
    loader<C, 5, 4>(shm, lw * 5, lw * 4, lane, [&](int p) { return (unsigned)(p * 8 * FF * 2); }, (unsigned)(j * FF * 2),
        [&](int p) { return (unsigned)(p * 8 * FF * 2); }, (unsigned)(j * FF * 2),
        [&](int k, const char*& pa, const char*& pb) { pa = wp + k * 256; pb = ap + k * 256; });
    resid_load(h + (size_t)tm * 64 * DM + td * 80, r0, r1);
  } else {
    resid_load(h + (size_t)tm * 64 * DM + td * 80, r0, r1);
    consumer<C>(acc, shm, wid, lane, [](auto&) {} );
  }
  resid_epilogue<C, false>(acc, wid < 4, shm, wid, lane, r0, r1, out + (size_t)tm * 64 * DM + td * 80, nullptr);
}

static void launch_stage(int which, void* const* d_in, void* d_out, void* d_ws, hipStream_t stream) {
  const u16* x = (const u16*)d_in[0];
  const u16* Cr = (const u16*)d_in[1];
  const u16* Ci = (const u16*)d_in[2];
  const u16* Wre = (const u16*)d_in[3];
  const u16* Wim = (const u16*)d_in[4];
  const u16* Wg = (const u16*)d_in[6];
  const u16* Wu = (const u16*)d_in[7];
  const u16* Wd = (const u16*)d_in[8];
  char* ws = (char*)d_ws;
  u16* abT = (u16*)(ws);
  u16* h = (u16*)(ws + (6 << 20));
  u16* WguT = (u16*)(ws + (10 << 20));
  u16* WdT = (u16*)(ws + (14 << 20));
  u16* act = (u16*)(ws + (16 << 20));
  f32x2* stats = (f32x2*)(ws + (24 << 20));
  float* csp = (float*)(ws + (25 << 20));
  float* csum = (float*)(ws + (26 << 20));
  u16* out = (u16*)d_out;
  if (which == 0) {
    (void)hipFuncSetAttribute((const void*)k1_hid, hipFuncAttributeMaxDynamicSharedMemorySize, C1::LDS);
    k1_hid<<<256, 512, C1::LDS, stream>>>(x, Wre, Wim, Wg, Wu, abT, WguT, csp);
  } else if (which == 1) {
    (void)hipFuncSetAttribute((const void*)k2_seq, hipFuncAttributeMaxDynamicSharedMemorySize, C2::LDS);
    k2_seq<<<256, 512, C2::LDS, stream>>>(x, Cr, Ci, abT, Wd, csp, h, WdT, stats, csum);
  } else if (which == 2) {
    (void)hipFuncSetAttribute((const void*)k3_ffn1, hipFuncAttributeMaxDynamicSharedMemorySize, C3::LDS);
    k3_ffn1<<<256, 512, C3::LDS, stream>>>(h, WguT, csum, stats, act);
  } else {
    (void)hipFuncSetAttribute((const void*)k4_ffn2, hipFuncAttributeMaxDynamicSharedMemorySize, C4::LDS);
    k4_ffn2<<<256, 512, C4::LDS, stream>>>(h, act, WdT, out);
  }
}

extern "C" void kernel_launch(void* const* d_in, const int* in_sizes, int n_in, void* d_out, int out_size, void* d_ws, size_t ws_size,
                              hipStream_t stream) {
  for (int i = 0; i < 4; ++i) launch_stage(i, d_in, d_out, d_ws, stream);
}
```

```cpp
#include <hip/hip_runtime.h>
#include <hip/hip_bf16.h>
#include <stdint.h>
#include <type_traits>

typedef unsigned short u16;
typedef __attribute__((ext_vector_type(8))) short bf16x8;
typedef __attribute__((ext_vector_type(4))) float f32x4;
typedef __attribute__((ext_vector_type(2))) float f32x2;
typedef __attribute__((ext_vector_type(2))) __bf16 bf16x2;
typedef __attribute__((ext_vector_type(2))) unsigned u32x2;
typedef __attribute__((ext_vector_type(4))) unsigned u32x4;

#define DEVI __device__ __forceinline__
#define IC(x) std::integral_constant<int, (x)>{}
#define SB() __builtin_amdgcn_sched_barrier(0)
#define MFMA(a, b, c) __builtin_amdgcn_mfma_f32_16x16x32_bf16(a, b, c, 0, 0, 0)

constexpr int SEQ = 2048, DM = 640, FF = 1536;

template <int B, int E, class F> DEVI void static_for(F&& f) {
  if constexpr (B < E) { f(IC(B)); static_for<B + 1, E>(f); }
}
DEVI void glds16(unsigned voff, const char* sbase, unsigned ldsaddr) {
  asm volatile("s_mov_b32 m0, %2\n\ts_nop 0\n\tglobal_load_lds_dwordx4 %0, %1" ::"v"(voff), "s"(sbase), "s"(ldsaddr));
}
template <int N> DEVI void waitvm_bar() { asm volatile("s_waitcnt vmcnt(%0)\n\ts_barrier" ::"i"(N) : "memory"); }
DEVI void bar() { asm volatile("s_barrier" ::: "memory"); }
DEVI void touch(unsigned& sink, unsigned voff, const void* sbase) { asm volatile("global_load_dword %0, %1, %2" : "+v"(sink) : "v"(voff), "s"(sbase)); }
DEVI void touch_done(unsigned& sink) { asm volatile("s_waitcnt vmcnt(0)" : "+v"(sink)::"memory"); }
DEVI void lg_bar() { asm volatile("s_waitcnt lgkmcnt(0)\n\ts_barrier" ::: "memory"); }
DEVI unsigned pack2(float a, float b) { f32x2 p = {a, b}; return __builtin_bit_cast(unsigned, __builtin_convertvector(p, bf16x2)); }
DEVI float bflo(unsigned v) { return __builtin_bit_cast(float, v << 16); }
DEVI float bfhi(unsigned v) { return __builtin_bit_cast(float, v & 0xffff0000u); }
template <int CTRL> DEVI float dppf(float v) {
  return __builtin_bit_cast(float, __builtin_amdgcn_update_dpp(0, __builtin_bit_cast(int, v), CTRL, 0xf, 0xf, true));
}
DEVI float red16(float v) { v += dppf<0xB1>(v); v += dppf<0x4E>(v); v += dppf<0x141>(v); v += dppf<0x140>(v); return v; }

template <int TA_, int TB_, int NWA_, int NWB_, int KS_, int BK_, int NST_, int NK_, int PRE_> struct Cfg {
  static constexpr int TA = TA_, TB = TB_, NWA = NWA_, NWB = NWB_, KS = KS_, BK = BK_, NST = NST_, NK = NK_;
  static constexpr int NSUB = BK / 64, PA = TA / 8, PB = TB / 8, PPS = PA + PB, STG = NSUB * PPS * 1024, SUBB = PPS * 1024;
  static constexpr int WAF = TA / 16 / NWA, WBF = TB / 16 / NWB, KH = (BK / 32) / KS;
  static constexpr int NF = WAF + WBF, NMF = WAF * WBF;
  static constexpr int LDS = NST * STG;
  static constexpr int LASTSLOT = (NK - 1) % NST;
  static constexpr int issued(int k) {
    if (k < -1) return -1;
    int a = 2 * k + 1 + PRE_, b = k - 1 + NST, c = NK - 1;
    if (k == -1) b = NST - 1;
    int m = a < b ? a : b; return m < c ? m : c;
  }
  static constexpr int steady_begin() { int k = 0; while (!(issued(k) == k - 1 + NST && issued(k - 1) == k - 2 + NST) && k < NK) ++k; return k; }
  static constexpr int K0 = steady_begin() < NK - NST + 1 ? steady_begin() : NK - NST + 1;
  static constexpr int K1 = NK - NST + 1 > K0 ? NK - NST + 1 : K0;
};

template <class C, int GA, int GB, class SA, class SB_, class Ptrs>
DEVI void loader(char* shm, int a0, int b0, int lane, SA sA, unsigned vA, SB_ sB, unsigned vB, Ptrs ptrs) {
  constexpr int G = GA + GB;
  const unsigned sw0 = (unsigned)(((lane & 7) ^ (lane >> 4)) * 16);
  unsigned voff[G], lo[G];
  const unsigned ldsb = (unsigned)(size_t)(__attribute__((address_space(3))) char*)shm;
#pragma unroll
  for (int i = 0; i < GA; ++i) {
    int p = a0 + i, sub = p / C::PA, pr = p - sub * C::PA;
    voff[i] = vA + (sw0 ^ (unsigned)((pr & 1) * 64)) + (sA(pr) + (unsigned)sub * 128u);
    lo[i] = ldsb + sub * C::SUBB + pr * 1024;
  }
#pragma unroll
  for (int i = 0; i < GB; ++i) {
    int p = b0 + i, sub = p / C::PB, pr = p - sub * C::PB;
    voff[GA + i] = vB + (sw0 ^ (unsigned)((pr & 1) * 64)) + (sB(pr) + (unsigned)sub * 128u);
    lo[GA + i] = ldsb + sub * C::SUBB + (C::PA + pr) * 1024;
  }
  auto issue = [&](int s, unsigned slot) {
    const char *pa, *pb; ptrs(s, pa, pb);
#pragma unroll
    for (int i = 0; i < G; ++i) glds16(voff[i], i < GA ? pa : pb, lo[i] + slot);
  };
  auto irregular = [&](auto K) {
    constexpr int k = decltype(K)::value;
    static_for<C::issued(k - 1) + 1, C::issued(k) + 1>([&](auto S) { constexpr int s = decltype(S)::value; issue(s, (s % C::NST) * C::STG); });
    waitvm_bar<(C::issued(k) - (k + 1)) * G>();
  };
  static_for<-1, C::K0>(irregular);
  unsigned slot = ((C::K0 - 1 + C::NST) % C::NST) * C::STG;
  for (int k = C::K0; k < C::K1; ++k) {
    issue(k - 1 + C::NST, slot);
    slot += C::STG; if (slot == C::NST * C::STG) slot = 0;
    waitvm_bar<(C::NST - 2) * G>();
  }
  static_for<C::K1, C::NK - 1>(irregular);
}

template <class C, class Extra> DEVI void consumer(f32x4 (&acc)[C::WAF][C::WBF], char* shm, int wid, int lane, Extra extra ) {
  constexpr int WAF = C::WAF, WBF = C::WBF, KH = C::KH, NF = C::NF, NMF = C::NMF, STG = C::STG, NST = C::NST, NK = C::NK;
  const int kg = wid / (C::NWA * C::NWB), wab = wid % (C::NWA * C::NWB), wa = wab / C::NWB, wb = wab % C::NWB;
#pragma unroll
  for (int j = 0; j < WAF; ++j)
#pragma unroll
    for (int b = 0; b < WBF; ++b) acc[j][b] = f32x4{0, 0, 0, 0};
  const int fr = lane & 15, fq = lane >> 4;
  const int c0 = fr * 128 + ((fq ^ (fr >> 1)) * 16);
  int rda[KH], rdb[KH];
#pragma unroll
  for (int h = 0; h < KH; ++h) {
    int ks = kg * KH + h, o = (c0 ^ ((ks & 1) * 64)) + (ks >> 1) * C::SUBB;
    rda[h] = o + wa * WAF * 2048; rdb[h] = o + C::TA * 128 + wb * WBF * 2048;
  }
  bf16x8 R[2][NF];
  auto rd1 = [&](bf16x8 (&Rn)[NF], unsigned slot, int h, int f) {
    if (f < WAF) Rn[f] = *(const bf16x8*)(shm + slot + rda[h] + f * 2048);
    else Rn[f] = *(const bf16x8*)(shm + slot + rdb[h] + (f - WAF) * 2048);
  };
  auto body = [&](bf16x8 (&Rc)[NF], bf16x8 (&Rn)[NF], unsigned slot, int h, bool dor) {
    int ri = 0;
#pragma unroll
    for (int m = 0; m < NMF; ++m) {
      { int n = m / WAF, j = m % WAF; acc[j][n] = MFMA(Rc[j], Rc[WAF + n], acc[j][n]); }
      int rt = ((m + 1) * NF * 4 + 3 * NMF - 1) / (3 * NMF); if (rt > NF) rt = NF;
#pragma unroll
      for (; ri < rt; ++ri) if (dor) rd1(Rn, slot, h, ri);
      SB();
    }
    extra(Rc);
    SB();
  };
  lg_bar();
#pragma unroll
  for (int f = 0; f < NF; ++f) rd1(R[0], 0, 0, f);
  unsigned slot = 0;
  auto stage = [&](auto P, bool last) {
    constexpr int par = decltype(P)::value;
    unsigned nslot = slot + STG; if (nslot == NST * STG) nslot = 0;
#pragma unroll
    for (int h = 0; h < KH; ++h) {
      const int c = (par + h) & 1;
      if (h == KH - 1) { if (!last) { lg_bar(); body(R[c], R[c ^ 1], nslot, 0, true); } else body(R[c], R[c ^ 1], 0, 0, false); }
      else body(R[c], R[c ^ 1], slot, h + 1, true);
    }
    slot = nslot;
  };
  if constexpr (KH % 2 == 0) {
    for (int k = 0; k < NK - 1; ++k) stage(IC(0), false);
    stage(IC(0), true);
  } else {
    static_assert(NK % 2 == 0, "");
    for (int k = 0; k < NK - 2; k += 2) { stage(IC(0), false); stage(IC(1), false); }
    stage(IC(0), false); stage(IC(1), true);
  }
}

struct TrUnit { u32x2 v[8]; };
DEVI void tr_load(TrUnit& t, const u16* W, int N, int k0, int n0, int lane) {
  const u16* p = W + (size_t)(k0 + 8 * (lane >> 4)) * N + n0 + 4 * (lane & 15);
#pragma unroll
  for (int i = 0; i < 8; ++i) t.v[i] = *(const u32x2*)(p + (size_t)i * N);
}
DEVI void tr_store(const TrUnit& t, u16* WT, int K, int k0, int n0, int lane) {
  const int src = (((lane & 3) << 4) | (lane >> 2)) << 2;
  u16* p = WT + (size_t)(n0 + 4 * (lane >> 2)) * K + k0 + 8 * (lane & 3);
  u32x4 o;
#define BP(v_) (unsigned)__builtin_amdgcn_ds_bpermute(src, (int)(v_))
#define TRP(W_, SEL) o.x = BP(__builtin_amdgcn_perm(t.v[1].W_, t.v[0].W_, SEL)); o.y = BP(__builtin_amdgcn_perm(t.v[3].W_, t.v[2].W_, SEL)); \
                     o.z = BP(__builtin_amdgcn_perm(t.v[5].W_, t.v[4].W_, SEL)); o.w = BP(__builtin_amdgcn_perm(t.v[7].W_, t.v[6].W_, SEL));
  TRP(x, 0x05040100u) *(u32x4*)(p) = o;
  TRP(x, 0x07060302u) *(u32x4*)(p + K) = o;
  TRP(y, 0x05040100u) *(u32x4*)(p + 2 * K) = o;
  TRP(y, 0x07060302u) *(u32x4*)(p + 3 * K) = o;
#undef TRP
#undef BP
}
DEVI f32x4 tr_colsum(const TrUnit& t) {
  f32x4 c = {0, 0, 0, 0};
#pragma unroll
  for (int i = 0; i < 8; ++i) { c[0] += bflo(t.v[i].x); c[1] += bfhi(t.v[i].x); c[2] += bflo(t.v[i].y); c[3] += bfhi(t.v[i].y); }
#pragma unroll
  for (int i = 0; i < 4; ++i) { c[i] += __shfl_xor(c[i], 16); c[i] += __shfl_xor(c[i], 32); }
  return c;
}

#define K1_NST 6
#define K1_PRE 3
using C1 = Cfg<128, 80, 4, 1, 1, 64, K1_NST, 10, K1_PRE>;
template <int W> DEVI void k1_consumer(char* shm, float* wsum, char* stg, int lane, float sgn ) {
  using C = C1;
  f32x4 acc[2][5];
  f32x4 sx[2] = {{0, 0, 0, 0}, {0, 0, 0, 0}}, sq[2] = {{0, 0, 0, 0}, {0, 0, 0, 0}}, sw = {0, 0, 0, 0}, sw4 = {0, 0, 0, 0};
  bf16x8 ones = {0x3f80, 0x3f80, 0x3f80, 0x3f80, 0x3f80, 0x3f80, 0x3f80, 0x3f80};
  asm volatile("" : "+v"(ones));
  consumer<C>(acc, shm, W, lane, [&](bf16x8 (&R)[7]) {
    sx[0] = MFMA(R[0], ones, sx[0]); sx[1] = MFMA(R[1], ones, sx[1]);
    sq[0] = MFMA(R[0], R[0], sq[0]); sq[1] = MFMA(R[1], R[1], sq[1]);
    sw = MFMA(ones, R[2 + W], sw);
    if constexpr (W == 0) sw4 = MFMA(ones, R[6], sw4);
  } );
  const int fr = lane & 15, fq = lane >> 4;
  if (lane < 16) { wsum[W * 16 + lane] = sw[0]; if constexpr (W == 0) wsum[64 + lane] = sw4[0]; }
  f32x4 rs[2], mr[2];
#pragma unroll
  for (int a = 0; a < 2; ++a)
#pragma unroll
    for (int r = 0; r < 4; ++r) {
      float s2 = __shfl(sq[a][r], 20 * fq + r);
      float mean = sx[a][r] * (1.0f / DM);
      float var = fmaxf(s2 * (1.0f / DM) - mean * mean, 0.0f);
      float rstd = sgn * rsqrtf(var + 1e-5f);
      rs[a][r] = rstd; mr[a][r] = mean * rstd;
    }
  lg_bar();
#pragma unroll
  for (int b = 0; b < 5; ++b) {
    float w = wsum[b * 16 + fr];
    f32x4 v0 = acc[0][b] * rs[0] - w * mr[0], v1 = acc[1][b] * rs[1] - w * mr[1];
    u32x4 o; o.x = pack2(v0[0], v0[1]); o.y = pack2(v0[2], v0[3]); o.z = pack2(v1[0], v1[1]); o.w = pack2(v1[2], v1[3]);
    *(u32x4*)(stg + (b * 16 + fr) * 272 + W * 64 + fq * 16) = o;
  }
}
__global__ __launch_bounds__(512, 2) void k1_hid(const u16* __restrict__ x, const u16* __restrict__ Wre, const u16* __restrict__ Wim,
                                                 const u16* __restrict__ Wg, const u16* __restrict__ Wu, u16* __restrict__ abT,
                                                 u16* __restrict__ WguT, float* __restrict__ csp) {
  using C = C1;
  extern __shared__ __attribute__((aligned(16))) char shm[];
  const int bid = blockIdx.x, xcd = bid & 7, idx = bid >> 3;
  const int ts = (xcd >> 1) * 4 + (idx >> 3), te = (xcd & 1) * 8 + (idx & 7);
  const int wid = __builtin_amdgcn_readfirstlane(threadIdx.x >> 6), lane = threadIdx.x & 63;
  const int half = te >> 3;
  constexpr int FREE = ((C::LASTSLOT + 1) % C::NST) * C::STG;
  float* wsum = (float*)(shm + FREE);
  char* stg = shm + FREE + 1024;
  if (wid == 7) {
    TrUnit tu[4];
#pragma unroll
    for (int q = 0; q < 4; ++q) {
      int u = bid * 4 + q; if (u > 959) u = 959;
      const int tgu = u >= 480, tu_i = u - tgu * 480, tkb = tu_i / 24;
      tr_load(tu[q], tgu ? Wu : Wg, FF, tkb * 32, (tu_i % 24) * 64, lane);
    }
    static_for<-1, C::NK - 1>([&](auto K) {
      constexpr int k = decltype(K)::value;
      if constexpr (k >= 2 && k < 6) {
        constexpr int q = k - 2;
        const int u = bid * 4 + q;
        if (u < 960) {
          const int tgu = u >= 480, tu_i = u - tgu * 480, tkb = tu_i / 24, tn0 = (tu_i % 24) * 64;
          tr_store(tu[q], WguT + (size_t)tgu * FF * DM, DM, tkb * 32, tn0, lane);
          f32x4 c = tr_colsum(tu[q]);
          if (lane < 16) *(f32x4*)(csp + (size_t)tkb * (2 * FF) + tgu * FF + tn0 + 4 * lane) = c;
        }
      }
      bar();
    });
    lg_bar();
  } else if (wid >= 4) {
    const int lw = wid - 4, j = lane >> 3;
    const char* Wb = (const char*)(half ? Wim : Wre) + (size_t)(te & 7) * 80 * DM * 2;
    const char* xb = (const char*)x + (size_t)ts * 128 * DM * 2;
    auto sA = [&](int p) { return (unsigned)((32 * (p >> 2) + 16 * (p & 1) + 4 * ((p >> 1) & 1)) * DM * 2); };
    auto sB = [&](int p) { return (unsigned)(p * 8 * DM * 2); };
    auto ptrs = [&](int k, const char*& pa, const char*& pb) { pa = xb + k * 128; pb = Wb + k * 128; };
    const unsigned vA = (unsigned)((8 * (j >> 2) + (j & 3)) * DM * 2), vB = (unsigned)(j * DM * 2);
    if (lw == 0) loader<C, 6, 3>(shm, 0, 0, lane, sA, vA, sB, vB, ptrs);
    else if (lw == 1) loader<C, 5, 4>(shm, 6, 3, lane, sA, vA, sB, vB, ptrs);
    else loader<C, 5, 3>(shm, 11, 7, lane, sA, vA, sB, vB, ptrs);
    lg_bar();
  } else {
    const float sgn = half ? -1.0f : 1.0f;
    if (wid == 0) k1_consumer<0>(shm, wsum, stg, lane, sgn );
    else if (wid == 1) k1_consumer<1>(shm, wsum, stg, lane, sgn );
    else if (wid == 2) k1_consumer<2>(shm, wsum, stg, lane, sgn );
    else k1_consumer<3>(shm, wsum, stg, lane, sgn );
  }
  lg_bar();
  u16* ob = abT + (size_t)(te & 7) * 80 * 4096 + half * 2048 + ts * 128;
#pragma unroll
  for (int i = 0; i < 3; ++i) {
    int c = threadIdx.x + i * 512;
    if (i < 2 || c < 1280) {
      int row = c >> 4, ch = c & 15;
      *(u32x4*)(ob + (size_t)row * 4096 + ch * 8) = *(const u32x4*)(stg + row * 272 + ch * 16);
    }
  }
}

DEVI u32x4 add8(f32x4 a0, f32x4 a1, u32x4 r) {
  u32x4 o;
  o.x = pack2(a0[0] + bflo(r.x), a0[1] + bfhi(r.x)); o.y = pack2(a0[2] + bflo(r.y), a0[3] + bfhi(r.y));
  o.z = pack2(a1[0] + bflo(r.z), a1[1] + bfhi(r.z)); o.w = pack2(a1[2] + bflo(r.w), a1[3] + bfhi(r.w));
  return o;
}
DEVI u32x4 addbf8(u32x4 a, u32x4 b) {
  u32x4 o;
  o.x = pack2(bflo(a.x) + bflo(b.x), bfhi(a.x) + bfhi(b.x)); o.y = pack2(bflo(a.y) + bflo(b.y), bfhi(a.y) + bfhi(b.y));
  o.z = pack2(bflo(a.z) + bflo(b.z), bfhi(a.z) + bfhi(b.z)); o.w = pack2(bflo(a.w) + bflo(b.w), bfhi(a.w) + bfhi(b.w));
  return o;
}

#define K2_PRE 6
#define K2_NST 6
using C2 = Cfg<80, 128, 1, 2, 2, 64, K2_NST, 32, K2_PRE>;
__global__ __launch_bounds__(512, 2) void k2_seq(const u16* __restrict__ x, const u16* __restrict__ Cr, const u16* __restrict__ Ci,
                                                 const u16* __restrict__ abT, const u16* __restrict__ Wd, const float* __restrict__ csp,
                                                 u16* __restrict__ P, u16* __restrict__ WdT, float* __restrict__ csum) {
  using C = C2;
  extern __shared__ __attribute__((aligned(16))) char shm[];
  const int bid = blockIdx.x, xcd = bid & 7, idx = bid >> 3;
  const int half = xcd & 1, tt = (xcd >> 1) * 4 + (idx >> 3), te = idx & 7;
  const int wid = __builtin_amdgcn_readfirstlane(threadIdx.x >> 6), lane = threadIdx.x & 63;
  static_assert((C::NST - 1 - C::LASTSLOT) * C::STG >= 2 * 128 * 336, "");
  char* red = shm + (C::LASTSLOT + 1) * C::STG;
  u32x4 rx[3] = {{0, 0, 0, 0}, {0, 0, 0, 0}, {0, 0, 0, 0}};
  auto xload = [&]() {
    if (half == 0) {
#pragma unroll
      for (int i = 0; i < 3; ++i) { int c = threadIdx.x + i * 512; if (c > 1279) c = 1279;
        rx[i] = *(const u32x4*)(x + (size_t)(tt * 128 + c / 10) * DM + te * 80 + (c % 10) * 8); }
    }
  };
  if (wid == 7) {
    TrUnit tu[2]; float cp[20];
#pragma unroll
    for (int q = 0; q < 2; ++q) { int u = bid * 2 + q; if (u > 479) u = 479; tr_load(tu[q], Wd, DM, (u / 10) * 32, (u % 10) * 64, lane); }
    { const int col = (bid < 48 ? bid : 47) * 64 + lane;
#pragma unroll
      for (int i = 0; i < 20; ++i) cp[i] = csp[(size_t)i * (2 * FF) + col]; }
    for (int k = -1; k < C::NK - 1; ++k) {
      if (k == 4) { const int u = bid * 2; if (u < 480) tr_store(tu[0], WdT, FF, (u / 10) * 32, (u % 10) * 64, lane); }
      if (k == 6) { const int u = bid * 2 + 1; if (u < 480) tr_store(tu[1], WdT, FF, (u / 10) * 32, (u % 10) * 64, lane); }
      if (k == 8) {
        float c = 0;
#pragma unroll
        for (int i = 0; i < 20; ++i) c += cp[i];
        if (bid < 48) csum[bid * 64 + lane] = c;
      }
      if (k == 20) xload();
      bar();
    }
  } else if (wid >= 4) {
    const int lw = wid - 4, j = lane >> 3;
    const char* ab = (const char*)abT + (size_t)te * 80 * 8192 + half * 4096;
    const char* cc = (const char*)(half ? Ci : Cr) + (size_t)tt * 128 * 4096;
    auto sA = [&](int p) { return (unsigned)(p * 8 * 8192); };
    auto sB = [&](int p) { return (unsigned)(p * 8 * 4096); };
    auto ptrs = [&](int k, const char*& pa, const char*& pb) { pa = ab + k * 128; pb = cc + k * 128; };
    if (lw == 0) loader<C, 4, 5>(shm, 0, 0, lane, sA, (unsigned)(j * 8192), sB, (unsigned)(j * 4096), ptrs);
    else if (lw == 1) loader<C, 3, 6>(shm, 4, 5, lane, sA, (unsigned)(j * 8192), sB, (unsigned)(j * 4096), ptrs);
    else loader<C, 3, 5>(shm, 7, 11, lane, sA, (unsigned)(j * 8192), sB, (unsigned)(j * 4096), ptrs);
    xload();
  } else {
    xload();
    f32x4 acc[5][4];
    consumer<C>(acc, shm, wid, lane, [](auto&) {} );
    const int fr = lane & 15, fq = lane >> 4, kg = wid >> 1, wb = wid & 1;
#pragma unroll
    for (int n = 0; n < 4; ++n)
#pragma unroll
      for (int j = 0; j < 5; ++j) *(f32x4*)(red + kg * (128 * 336) + ((wb * 4 + n) * 16 + fr) * 336 + j * 64 + fq * 16) = acc[j][n];
  }
  lg_bar();
  u16* op = P + (size_t)half * SEQ * DM + (size_t)tt * 128 * DM + te * 80;
#pragma unroll
  for (int i = 0; i < 3; ++i) {
    const int c = threadIdx.x + i * 512;
    if (i < 2 || c < 1280) {
      const int row = c / 10, ch = c % 10;
      const char* p = red + row * 336 + ch * 32;
      f32x4 a0 = *(const f32x4*)p + *(const f32x4*)(p + 128 * 336), a1 = *(const f32x4*)(p + 16) + *(const f32x4*)(p + 16 + 128 * 336);
      *(u32x4*)(op + (size_t)row * DM + ch * 8) = add8(a0, a1, rx[i]);
    }
  }
}

#define K3_PRE 2
using C3 = Cfg<192, 128, 2, 2, 1, 64, 4, 10, K3_PRE>;
DEVI void gload16(u32x4& d, unsigned voff, const char* sbase) { asm volatile("global_load_dwordx4 %0, %1, %2" : "=v"(d) : "v"(voff), "s"(sbase)); }
__global__ __launch_bounds__(512, 2) void k3_ffn1(const u16* __restrict__ P, const u16* __restrict__ WguT, const float* __restrict__ csum,
                                                  u16* __restrict__ act) {
  using C = C3;
  extern __shared__ __attribute__((aligned(16))) char shm[];
  const int bid = blockIdx.x, xcd = bid & 7, idx = bid >> 3;
  const int tm = (xcd >> 2) * 8 + (idx >> 2), tn = (xcd & 3) * 4 + (idx & 3);
  const int wid = __builtin_amdgcn_readfirstlane(threadIdx.x >> 6), lane = threadIdx.x & 63;
  constexpr int FREE = ((C::LASTSLOT + 1) % C::NST) * C::STG;
  f32x2* rowst = (f32x2*)(shm + FREE);
  char* stg = shm + FREE + 4096;
  if (wid >= 4) {
    const int lw = wid - 4, j = lane >> 3;
    const char* wbp = (const char*)WguT + (size_t)tn * 96 * DM * 2;
    const char* p0 = (const char*)P + (size_t)(tm * 128 + lw * 32) * DM * 2;
    const char* p1 = p0 + (size_t)SEQ * DM * 2;
    const unsigned sw0 = (unsigned)(((lane & 7) ^ (lane >> 4)) * 16);
    const unsigned ldsb = (unsigned)(size_t)(__attribute__((address_space(3))) char*)shm;
    unsigned vw[6];
#pragma unroll
    for (int i = 0; i < 6; ++i) { const int p = lw * 6 + i;
      vw[i] = (unsigned)(j * DM * 2) + (sw0 ^ (unsigned)((p & 1) * 64)) + (unsigned)((((p >> 1) & 1) * FF + 16 * (p >> 2) + 8 * (p & 1)) * DM * 2); }
    const unsigned vh = (unsigned)(j * DM * 2 + (lane & 7) * 16);
    char* hw = shm + (24 + lw * 4) * 1024 + j * 128;
    u32x4 buf[3][8];
    f32x2 s1[4], s2[4];
#pragma unroll
    for (int i = 0; i < 4; ++i) { s1[i] = f32x2{0, 0}; s2[i] = f32x2{0, 0}; }
    static_for<-1, C::NK - 1>([&](auto K) {
      constexpr int k = decltype(K)::value;
      static_for<C::issued(k - 1) + 1, C::issued(k) + 1>([&](auto S) {
        constexpr int s = decltype(S)::value;
#pragma unroll
        for (int i = 0; i < 4; ++i) { gload16(buf[s % 3][2 * i], vh + i * 8 * DM * 2, p0 + s * 128); gload16(buf[s % 3][2 * i + 1], vh + i * 8 * DM * 2, p1 + s * 128); }
#pragma unroll
        for (int i = 0; i < 6; ++i) glds16(vw[i], wbp + s * 128, ldsb + (s % C::NST) * C::STG + (lw * 6 + i) * 1024);
      });
      constexpr int c = k + 1;
      u32x4 (&b)[8] = buf[c % 3];
      asm volatile("s_waitcnt vmcnt(%8)" : "+v"(b[0]), "+v"(b[1]), "+v"(b[2]), "+v"(b[3]), "+v"(b[4]), "+v"(b[5]), "+v"(b[6]), "+v"(b[7])
                   : "i"((C::issued(k) - c) * 14 + 6) : "memory");
#pragma unroll
      for (int i = 0; i < 4; ++i) {
        const u32x4 u = b[2 * i], v = b[2 * i + 1];
        f32x2 h0 = f32x2{bflo(u.x), bfhi(u.x)} + f32x2{bflo(v.x), bfhi(v.x)}, h1 = f32x2{bflo(u.y), bfhi(u.y)} + f32x2{bflo(v.y), bfhi(v.y)};
        f32x2 h2 = f32x2{bflo(u.z), bfhi(u.z)} + f32x2{bflo(v.z), bfhi(v.z)}, h3 = f32x2{bflo(u.w), bfhi(u.w)} + f32x2{bflo(v.w), bfhi(v.w)};
        u32x4 o; o.x = pack2(h0[0], h0[1]); o.y = pack2(h1[0], h1[1]); o.z = pack2(h2[0], h2[1]); o.w = pack2(h3[0], h3[1]);
        *(u32x4*)(hw + (c % C::NST) * C::STG + i * 1024 + (sw0 ^ (unsigned)((i & 1) * 64))) = o;
        s1[i] += (h0 + h1) + (h2 + h3);
        s2[i] += (h0 * h0 + h1 * h1) + (h2 * h2 + h3 * h3);
        asm volatile("" : "+v"(s1[i]), "+v"(s2[i]));
      }
      asm volatile("s_waitcnt vmcnt(%0) lgkmcnt(0)\n\ts_barrier" ::"i"((C::issued(k) - c) * 14) : "memory");
    });
#pragma unroll
    for (int i = 0; i < 4; ++i) {
      float a = s1[i][0] + s1[i][1], b = s2[i][0] + s2[i][1];
      a += dppf<0xB1>(a); a += dppf<0x4E>(a); a += dppf<0x141>(a);
      b += dppf<0xB1>(b); b += dppf<0x4E>(b); b += dppf<0x141>(b);
      const float mean = a * (1.0f / DM);
      const float var = fmaxf(b * (1.0f / DM) - mean * mean, 0.0f);
      if ((lane & 7) == 0) rowst[lw * 32 + i * 8 + j] = f32x2{-mean, var + 1e-5f};
    }
    lg_bar();
  } else {
    const int fr = lane & 15, fq = lane >> 4, wa = wid >> 1, wb = wid & 1;
    f32x4 cs[6];
#pragma unroll
    for (int j = 0; j < 6; ++j) cs[j] = *(const f32x4*)(csum + (j & 1) * FF + tn * 96 + wa * 48 + (j >> 1) * 16 + 4 * fq);
    f32x4 acc[6][4];
    consumer<C>(acc, shm, wid, lane, [](auto&) {} );
    lg_bar();
#pragma unroll
    for (int n = 0; n < 4; ++n) {
      const int ml = (wb * 4 + n) * 16 + fr;
      const f32x2 st = rowst[ml];
      const float nmean = st[0], ve = st[1];
      const float rstd = rsqrtf(ve);
      const float k1 = -2.3022081985f * rstd;
      const float k2 = k1 * 0.044715f * rstd * rstd;
#pragma unroll
      for (int p = 0; p < 3; ++p) {
        f32x4 G = cs[2 * p] * nmean + acc[2 * p][n], U = cs[2 * p + 1] * nmean + acc[2 * p + 1][n];
        f32x4 z = G * (G * G * k2 + k1);
        f32x4 e; e[0] = __builtin_amdgcn_exp2f(z[0]); e[1] = __builtin_amdgcn_exp2f(z[1]); e[2] = __builtin_amdgcn_exp2f(z[2]); e[3] = __builtin_amdgcn_exp2f(z[3]);
        f32x4 d = e * ve + ve;
        f32x4 r; r[0] = __builtin_amdgcn_rcpf(d[0]); r[1] = __builtin_amdgcn_rcpf(d[1]); r[2] = __builtin_amdgcn_rcpf(d[2]); r[3] = __builtin_amdgcn_rcpf(d[3]);
        f32x4 v = G * U * r;
        u32x2 o; o.x = pack2(v[0], v[1]); o.y = pack2(v[2], v[3]);
        *(u32x2*)(stg + ml * 208 + (wa * 48 + p * 16 + fq * 4) * 2) = o;
      }
    }
  }
  lg_bar();
  u16* ob = act + (size_t)tm * 128 * FF + tn * 96;
#pragma unroll
  for (int i = 0; i < 3; ++i) {
    int c = threadIdx.x + i * 512, row = c / 12, ch = c % 12;
    *(u32x4*)(ob + (size_t)row * FF + ch * 8) = *(const u32x4*)(stg + row * 208 + ch * 16);
  }
}

#define K4_PRE 4
using C4 = Cfg<80, 64, 1, 2, 2, 128, 4, 12, K4_PRE>;
__global__ __launch_bounds__(512, 2) void k4_ffn2(const u16* __restrict__ P, const u16* __restrict__ act, const u16* __restrict__ WdT,
                                                  u16* __restrict__ out) {
  using C = C4;
  extern __shared__ __attribute__((aligned(16))) char shm[];
  const int bid = blockIdx.x, xcd = bid & 7, idx = bid >> 3;
  const int tm = (xcd >> 1) * 8 + (idx >> 2), td = (xcd & 1) * 4 + (idx & 3);
  const int wid = __builtin_amdgcn_readfirstlane(threadIdx.x >> 6), lane = threadIdx.x & 63;
  static_assert(C::LASTSLOT == C::NST - 1 && (C::NST - 1) * C::STG >= 2 * 64 * 336, "");
  u32x4 ra[2], rb[2];
  auto rload = [&]() {
#pragma unroll
    for (int i = 0; i < 2; ++i) { int c = threadIdx.x + i * 512; if (c > 639) c = 639;
      const u16* p = P + (size_t)(tm * 64 + c / 10) * DM + td * 80 + (c % 10) * 8;
      ra[i] = *(const u32x4*)p; rb[i] = *(const u32x4*)(p + (size_t)SEQ * DM); }
  };
  if (wid >= 4) {
    const int lw = wid - 4, j = lane >> 3;
    const char* wp = (const char*)WdT + (size_t)td * 80 * FF * 2;
    const char* ap = (const char*)act + (size_t)tm * 64 * FF * 2;
    auto sA = [&](int p) { return (unsigned)(p * 8 * FF * 2); };
    auto ptrs = [&](int k, const char*& pa, const char*& pb) { pa = wp + k * 256; pb = ap + k * 256; };
    loader<C, 5, 4>(shm, lw * 5, lw * 4, lane, sA, (unsigned)(j * FF * 2), sA, (unsigned)(j * FF * 2), ptrs);
    rload();
  } else {
    rload();
    f32x4 acc[5][2];
    consumer<C>(acc, shm, wid, lane, [](auto&) {} );
    const int fr = lane & 15, fq = lane >> 4, kg = wid >> 1, wb = wid & 1;
#pragma unroll
    for (int n = 0; n < 2; ++n)
#pragma unroll
      for (int j = 0; j < 5; ++j) *(f32x4*)(shm + kg * (64 * 336) + ((wb * 2 + n) * 16 + fr) * 336 + j * 64 + fq * 16) = acc[j][n];
  }
  lg_bar();
  u16* op = out + (size_t)tm * 64 * DM + td * 80;
#pragma unroll
  for (int i = 0; i < 2; ++i) {
    const int c = threadIdx.x + i * 512;
    if (i == 0 || c < 640) {
      const int row = c / 10, ch = c % 10;
      const char* p = shm + row * 336 + ch * 32;
      f32x4 a0 = *(const f32x4*)p + *(const f32x4*)(p + 64 * 336), a1 = *(const f32x4*)(p + 16) + *(const f32x4*)(p + 16 + 64 * 336);
      *(u32x4*)(op + (size_t)row * DM + ch * 8) = add8(a0, a1, addbf8(ra[i], rb[i]));
    }
  }
}

static void launch_stage(int which, void* const* d_in, void* d_out, void* d_ws, hipStream_t stream) {
  const u16* x = (const u16*)d_in[0];
  const u16* Cr = (const u16*)d_in[1];
  const u16* Ci = (const u16*)d_in[2];
  const u16* Wre = (const u16*)d_in[3];
  const u16* Wim = (const u16*)d_in[4];
  const u16* Wg = (const u16*)d_in[6];
  const u16* Wu = (const u16*)d_in[7];
  const u16* Wd = (const u16*)d_in[8];
  char* ws = (char*)d_ws;
  u16* abT = (u16*)(ws);
  u16* WguT = (u16*)(ws + (10 << 20));
  u16* WdT = (u16*)(ws + (14 << 20));
  u16* act = (u16*)(ws + (16 << 20));
  u16* P = (u16*)(ws + (28 << 20));
  float* csp = (float*)(ws + (25 << 20));
  float* csum = (float*)(ws + (26 << 20));
  u16* out = (u16*)d_out;
  if (which == 0) {
    (void)hipFuncSetAttribute((const void*)k1_hid, hipFuncAttributeMaxDynamicSharedMemorySize, C1::LDS);
    k1_hid<<<256, 512, C1::LDS, stream>>>(x, Wre, Wim, Wg, Wu, abT, WguT, csp);
  } else if (which == 1) {
    (void)hipFuncSetAttribute((const void*)k2_seq, hipFuncAttributeMaxDynamicSharedMemorySize, C2::LDS);
    k2_seq<<<256, 512, C2::LDS, stream>>>(x, Cr, Ci, abT, Wd, csp, P, WdT, csum);
  } else if (which == 2) {
    (void)hipFuncSetAttribute((const void*)k3_ffn1, hipFuncAttributeMaxDynamicSharedMemorySize, C3::LDS);
    k3_ffn1<<<256, 512, C3::LDS, stream>>>(P, WguT, csum, act);
  } else {
    (void)hipFuncSetAttribute((const void*)k4_ffn2, hipFuncAttributeMaxDynamicSharedMemorySize, C4::LDS);
    k4_ffn2<<<256, 512, C4::LDS, stream>>>(P, act, WdT, out);
  }
}

extern "C" void kernel_launch(void* const* d_in, const int* in_sizes, int n_in, void* d_out, int out_size, void* d_ws, size_t ws_size,
                              hipStream_t stream) {
  for (int i = 0; i < 4; ++i) launch_stage(i, d_in, d_out, d_ws, stream);
}
```

```cpp
#include <hip/hip_runtime.h>
#include <hip/hip_bf16.h>
#include <stdint.h>
#include <type_traits>

typedef unsigned short u16;
typedef __attribute__((ext_vector_type(8))) short bf16x8;
typedef __attribute__((ext_vector_type(4))) float f32x4;
typedef __attribute__((ext_vector_type(2))) float f32x2;
typedef __attribute__((ext_vector_type(2))) __bf16 bf16x2;
typedef __attribute__((ext_vector_type(2))) unsigned u32x2;
typedef __attribute__((ext_vector_type(4))) unsigned u32x4;

#define DEVI __device__ __forceinline__
#define IC(x) std::integral_constant<int, (x)>{}
#define SB() __builtin_amdgcn_sched_barrier(0)
#define MFMA(a, b, c) __builtin_amdgcn_mfma_f32_16x16x32_bf16(a, b, c, 0, 0, 0)

constexpr int SEQ = 2048, DM = 640, FF = 1536;

template <int B, int E, class F> DEVI void static_for(F&& f) {
  if constexpr (B < E) { f(IC(B)); static_for<B + 1, E>(f); }
}
DEVI void glds16(unsigned voff, const char* sbase, unsigned ldsaddr) {
  asm volatile("s_mov_b32 m0, %2\n\ts_nop 0\n\tglobal_load_lds_dwordx4 %0, %1" ::"v"(voff), "s"(sbase), "s"(ldsaddr));
}
template <int N> DEVI void waitvm_bar() { asm volatile("s_waitcnt vmcnt(%0)\n\ts_barrier" ::"i"(N) : "memory"); }
DEVI void bar() { asm volatile("s_barrier" ::: "memory"); }
DEVI void touch(unsigned& sink, unsigned voff, const void* sbase) { asm volatile("global_load_dword %0, %1, %2" : "+v"(sink) : "v"(voff), "s"(sbase)); }
DEVI void touch_done(unsigned& sink) { asm volatile("s_waitcnt vmcnt(0)" : "+v"(sink)::"memory"); }
DEVI void lg_bar() { asm volatile("s_waitcnt lgkmcnt(0)\n\ts_barrier" ::: "memory"); }
DEVI unsigned pack2(float a, float b) { f32x2 p = {a, b}; return __builtin_bit_cast(unsigned, __builtin_convertvector(p, bf16x2)); }
DEVI float bflo(unsigned v) { return __builtin_bit_cast(float, v << 16); }
DEVI float bfhi(unsigned v) { return __builtin_bit_cast(float, v & 0xffff0000u); }
template <int CTRL> DEVI float dppf(float v) {
  return __builtin_bit_cast(float, __builtin_amdgcn_update_dpp(0, __builtin_bit_cast(int, v), CTRL, 0xf, 0xf, true));
}
DEVI float red16(float v) { v += dppf<0xB1>(v); v += dppf<0x4E>(v); v += dppf<0x141>(v); v += dppf<0x140>(v); return v; }

template <int TA_, int TB_, int NWA_, int NWB_, int KS_, int BK_, int NST_, int NK_, int PRE_> struct Cfg {
  static constexpr int TA = TA_, TB = TB_, NWA = NWA_, NWB = NWB_, KS = KS_, BK = BK_, NST = NST_, NK = NK_;
  static constexpr int NSUB = BK / 64, PA = TA / 8, PB = TB / 8, PPS = PA + PB, STG = NSUB * PPS * 1024, SUBB = PPS * 1024;
  static constexpr int WAF = TA / 16 / NWA, WBF = TB / 16 / NWB, KH = (BK / 32) / KS;
  static constexpr int NF = WAF + WBF, NMF = WAF * WBF;
  static constexpr int LDS = NST * STG;
  static constexpr int LASTSLOT = (NK - 1) % NST;
  static constexpr int issued(int k) {
    if (k < -1) return -1;
    int a = 2 * k + 1 + PRE_, b = k - 1 + NST, c = NK - 1;
    if (k == -1) b = NST - 1;
    int m = a < b ? a : b; return m < c ? m : c;
  }
  static constexpr int steady_begin() { int k = 0; while (!(issued(k) == k - 1 + NST && issued(k - 1) == k - 2 + NST) && k < NK) ++k; return k; }
  static constexpr int K0 = steady_begin() < NK - NST + 1 ? steady_begin() : NK - NST + 1;
  static constexpr int K1 = NK - NST + 1 > K0 ? NK - NST + 1 : K0;
};

template <class C, int GA, int GB, class SA, class SB_, class Ptrs>
DEVI void loader(char* shm, int a0, int b0, int lane, SA sA, unsigned vA, SB_ sB, unsigned vB, Ptrs ptrs) {
  constexpr int G = GA + GB;
  const unsigned sw0 = (unsigned)(((lane & 7) ^ (lane >> 4)) * 16);
  unsigned voff[G], lo[G];
  const unsigned ldsb = (unsigned)(size_t)(__attribute__((address_space(3))) char*)shm;
#pragma unroll
  for (int i = 0; i < GA; ++i) {
    int p = a0 + i, sub = p / C::PA, pr = p - sub * C::PA;
    voff[i] = vA + (sw0 ^ (unsigned)((pr & 1) * 64)) + (sA(pr) + (unsigned)sub * 128u);
    lo[i] = ldsb + sub * C::SUBB + pr * 1024;
  }
#pragma unroll
  for (int i = 0; i < GB; ++i) {
    int p = b0 + i, sub = p / C::PB, pr = p - sub * C::PB;
    voff[GA + i] = vB + (sw0 ^ (unsigned)((pr & 1) * 64)) + (sB(pr) + (unsigned)sub * 128u);
    lo[GA + i] = ldsb + sub * C::SUBB + (C::PA + pr) * 1024;
  }
  auto issue = [&](int s, unsigned slot) {
    const char *pa, *pb; ptrs(s, pa, pb);
#pragma unroll
    for (int i = 0; i < G; ++i) glds16(voff[i], i < GA ? pa : pb, lo[i] + slot);
  };
  auto irregular = [&](auto K) {
    constexpr int k = decltype(K)::value;
    static_for<C::issued(k - 1) + 1, C::issued(k) + 1>([&](auto S) { constexpr int s = decltype(S)::value; issue(s, (s % C::NST) * C::STG); });
    waitvm_bar<(C::issued(k) - (k + 1)) * G>();
  };
  static_for<-1, C::K0>(irregular);
  unsigned slot = ((C::K0 - 1 + C::NST) % C::NST) * C::STG;
  for (int k = C::K0; k < C::K1; ++k) {
    issue(k - 1 + C::NST, slot);
    slot += C::STG; if (slot == C::NST * C::STG) slot = 0;
    waitvm_bar<(C::NST - 2) * G>();
  }
  static_for<C::K1, C::NK - 1>(irregular);
}

template <class C, class Extra> DEVI void consumer(f32x4 (&acc)[C::WAF][C::WBF], char* shm, int wid, int lane, Extra extra ) {
  constexpr int WAF = C::WAF, WBF = C::WBF, KH = C::KH, NF = C::NF, NMF = C::NMF, STG = C::STG, NST = C::NST, NK = C::NK;
  const int kg = wid / (C::NWA * C::NWB), wab = wid % (C::NWA * C::NWB), wa = wab / C::NWB, wb = wab % C::NWB;
#pragma unroll
  for (int j = 0; j < WAF; ++j)
#pragma unroll
    for (int b = 0; b < WBF; ++b) acc[j][b] = f32x4{0, 0, 0, 0};
  const int fr = lane & 15, fq = lane >> 4;
  const int c0 = fr * 128 + ((fq ^ (fr >> 1)) * 16);
  int rda[KH], rdb[KH];
#pragma unroll
  for (int h = 0; h < KH; ++h) {
    int ks = kg * KH + h, o = (c0 ^ ((ks & 1) * 64)) + (ks >> 1) * C::SUBB;
    rda[h] = o + wa * WAF * 2048; rdb[h] = o + C::TA * 128 + wb * WBF * 2048;
  }
  bf16x8 R[2][NF];
  auto rd1 = [&](bf16x8 (&Rn)[NF], unsigned slot, int h, int f) {
    if (f < WAF) Rn[f] = *(const bf16x8*)(shm + slot + rda[h] + f * 2048);
    else Rn[f] = *(const bf16x8*)(shm + slot + rdb[h] + (f - WAF) * 2048);
  };
  auto body = [&](bf16x8 (&Rc)[NF], bf16x8 (&Rn)[NF], unsigned slot, int h, bool dor) {
    int ri = 0;
#pragma unroll
    for (int m = 0; m < NMF; ++m) {
      { int n = m / WAF, j = m % WAF; acc[j][n] = MFMA(Rc[j], Rc[WAF + n], acc[j][n]); }
      int rt = ((m + 1) * NF * 4 + 3 * NMF - 1) / (3 * NMF); if (rt > NF) rt = NF;
#pragma unroll
      for (; ri < rt; ++ri) if (dor) rd1(Rn, slot, h, ri);
      SB();
    }
    extra(Rc);
    SB();
  };
  lg_bar();
#pragma unroll
  for (int f = 0; f < NF; ++f) rd1(R[0], 0, 0, f);
  unsigned slot = 0;
  auto stage = [&](auto P, bool last) {
    constexpr int par = decltype(P)::value;
    unsigned nslot = slot + STG; if (nslot == NST * STG) nslot = 0;
#pragma unroll
    for (int h = 0; h < KH; ++h) {
      const int c = (par + h) & 1;
      if (h == KH - 1) { if (!last) { lg_bar(); body(R[c], R[c ^ 1], nslot, 0, true); } else body(R[c], R[c ^ 1], 0, 0, false); }
      else body(R[c], R[c ^ 1], slot, h + 1, true);
    }
    slot = nslot;
  };
  if constexpr (KH % 2 == 0) {
    for (int k = 0; k < NK - 1; ++k) stage(IC(0), false);
    stage(IC(0), true);
  } else {
    static_assert(NK % 2 == 0, "");
    for (int k = 0; k < NK - 2; k += 2) { stage(IC(0), false); stage(IC(1), false); }
    stage(IC(0), false); stage(IC(1), true);
  }
}

struct TrUnit { u32x2 v[8]; };
DEVI void tr_load(TrUnit& t, const u16* W, int N, int k0, int n0, int lane) {
  const u16* p = W + (size_t)(k0 + 8 * (lane >> 4)) * N + n0 + 4 * (lane & 15);
#pragma unroll
  for (int i = 0; i < 8; ++i) t.v[i] = *(const u32x2*)(p + (size_t)i * N);
}
DEVI void tr_store(const TrUnit& t, u16* WT, int K, int k0, int n0, int lane) {
  const int src = (((lane & 3) << 4) | (lane >> 2)) << 2;
  u16* p = WT + (size_t)(n0 + 4 * (lane >> 2)) * K + k0 + 8 * (lane & 3);
  u32x4 o;
#define BP(v_) (unsigned)__builtin_amdgcn_ds_bpermute(src, (int)(v_))
#define TRP(W_, SEL) o.x = BP(__builtin_amdgcn_perm(t.v[1].W_, t.v[0].W_, SEL)); o.y = BP(__builtin_amdgcn_perm(t.v[3].W_, t.v[2].W_, SEL)); \
                     o.z = BP(__builtin_amdgcn_perm(t.v[5].W_, t.v[4].W_, SEL)); o.w = BP(__builtin_amdgcn_perm(t.v[7].W_, t.v[6].W_, SEL));
  TRP(x, 0x05040100u) *(u32x4*)(p) = o;
  TRP(x, 0x07060302u) *(u32x4*)(p + K) = o;
  TRP(y, 0x05040100u) *(u32x4*)(p + 2 * K) = o;
  TRP(y, 0x07060302u) *(u32x4*)(p + 3 * K) = o;
#undef TRP
#undef BP
}
DEVI float fadd_(float a, float b) { float r; asm("v_add_f32 %0, %1, %2" : "=v"(r) : "v"(a), "v"(b)); return r; }
template <int H> DEVI void tr_colsum(const TrUnit& t, float (&c)[4]) {
#pragma unroll
  for (int i = 4 * H; i < 4 * H + 4; ++i) {
    c[0] = fadd_(c[0], bflo(t.v[i].x)); c[1] = fadd_(c[1], bfhi(t.v[i].x)); c[2] = fadd_(c[2], bflo(t.v[i].y)); c[3] = fadd_(c[3], bfhi(t.v[i].y)); }
  if constexpr (H == 1) {
#pragma unroll
    for (int i = 0; i < 4; ++i) { c[i] = fadd_(c[i], __shfl_xor(c[i], 16)); c[i] = fadd_(c[i], __shfl_xor(c[i], 32)); }
  }
}
struct TrJob { const u16* src; u16* dst; int lds, ldd, k0, n0, cso; bool ok, cs; };
DEVI TrJob tr_job(int u, const u16* Wg, const u16* Wu, const u16* Wd, u16* WguT, u16* WdT) {
  TrJob j; j.ok = u < 1440; if (!j.ok) u = 1439;
  const int w = u / 480, r = u - w * 480;
  j.cs = w < 2;
  if (w < 2) { const int kb = r / 24; j.src = w ? Wu : Wg; j.dst = WguT + (size_t)w * FF * DM; j.lds = FF; j.ldd = DM; j.k0 = kb * 32; j.n0 = (r - kb * 24) * 64; j.cso = kb * 2 * FF + w * FF + j.n0; }
  else { const int kb = r / 10; j.src = Wd; j.dst = WdT; j.lds = DM; j.ldd = FF; j.k0 = kb * 32; j.n0 = (r - kb * 10) * 64; j.cso = 0; }
  return j;
}
template <int NB> DEVI void side_jobs(int bid, int lane, const u16* Wg, const u16* Wu, const u16* Wd, u16* WguT, u16* WdT, float* csp) {
  TrUnit tu[2]; float c[4];
  static_for<0, NB>([&](auto K) {
    constexpr int k = decltype(K)::value;
    if constexpr (k % 4 == 0 && k / 4 < 6) { TrJob j = tr_job(bid + 256 * (k / 4), Wg, Wu, Wd, WguT, WdT); tr_load(tu[(k / 4) & 1], j.src, j.lds, j.k0, j.n0, lane); }
    if constexpr (k >= 3 && (k - 3) / 4 < 6) {
      constexpr int i = (k - 3) / 4, ph = (k - 3) % 4;
      TrJob j = tr_job(bid + 256 * i, Wg, Wu, Wd, WguT, WdT);
      if constexpr (ph == 0) { if (j.ok) tr_store(tu[i & 1], j.dst, j.ldd, j.k0, j.n0, lane); }
      if constexpr (ph == 1) { c[0] = c[1] = c[2] = c[3] = 0.f; tr_colsum<0>(tu[i & 1], c); asm volatile("" : "+v"(c[0]), "+v"(c[1]), "+v"(c[2]), "+v"(c[3])); }
      if constexpr (ph == 2) { tr_colsum<1>(tu[i & 1], c); if (j.ok && j.cs && lane < 16) *(f32x4*)(csp + j.cso + 4 * lane) = f32x4{c[0], c[1], c[2], c[3]}; }
    }
    bar();
  });
}

#define K1_NST 6
#define K1_PFC 1
#define K1_PRE 3
using C1 = Cfg<128, 80, 4, 1, 1, 64, K1_NST, 10, K1_PRE>;
template <int W> DEVI void k1_consumer(char* shm, float* wsum, char* stg, int lane, float sgn ) {
  using C = C1;
  f32x4 acc[2][5];
  f32x4 sx[2] = {{0, 0, 0, 0}, {0, 0, 0, 0}}, sq[2] = {{0, 0, 0, 0}, {0, 0, 0, 0}}, sw = {0, 0, 0, 0}, sw4 = {0, 0, 0, 0};
  bf16x8 ones = {0x3f80, 0x3f80, 0x3f80, 0x3f80, 0x3f80, 0x3f80, 0x3f80, 0x3f80};
  asm volatile("" : "+v"(ones));
  consumer<C>(acc, shm, W, lane, [&](bf16x8 (&R)[7]) {
    sx[0] = MFMA(R[0], ones, sx[0]); sx[1] = MFMA(R[1], ones, sx[1]);
    sq[0] = MFMA(R[0], R[0], sq[0]); sq[1] = MFMA(R[1], R[1], sq[1]);
    sw = MFMA(ones, R[2 + W], sw);
    if constexpr (W == 0) sw4 = MFMA(ones, R[6], sw4);
  } );
  const int fr = lane & 15, fq = lane >> 4;
  if (lane < 16) { wsum[W * 16 + lane] = sw[0]; if constexpr (W == 0) wsum[64 + lane] = sw4[0]; }
  f32x4 rs[2], mr[2];
#pragma unroll
  for (int a = 0; a < 2; ++a)
#pragma unroll
    for (int r = 0; r < 4; ++r) {
      float s2 = __shfl(sq[a][r], 20 * fq + r);
      float mean = sx[a][r] * (1.0f / DM);
      float var = fmaxf(s2 * (1.0f / DM) - mean * mean, 0.0f);
      float rstd = sgn * rsqrtf(var + 1e-5f);
      rs[a][r] = rstd; mr[a][r] = mean * rstd;
    }
  lg_bar();
#pragma unroll
  for (int b = 0; b < 5; ++b) {
    float w = wsum[b * 16 + fr];
    f32x4 v0 = acc[0][b] * rs[0] - w * mr[0], v1 = acc[1][b] * rs[1] - w * mr[1];
    u32x4 o; o.x = pack2(v0[0], v0[1]); o.y = pack2(v0[2], v0[3]); o.z = pack2(v1[0], v1[1]); o.w = pack2(v1[2], v1[3]);
    *(u32x4*)(stg + (b * 16 + fr) * 272 + W * 64 + fq * 16) = o;
  }
}
__global__ __launch_bounds__(512, 2) void k1_hid(const u16* __restrict__ x, const u16* __restrict__ Wre, const u16* __restrict__ Wim,
                                                 const u16* __restrict__ Cr, const u16* __restrict__ Ci, u16* __restrict__ abT) {
  using C = C1;
  extern __shared__ __attribute__((aligned(16))) char shm[];
  const int bid = blockIdx.x, xcd = bid & 7, idx = bid >> 3;
  const int ts = (xcd >> 1) * 4 + (idx >> 3), te = (xcd & 1) * 8 + (idx & 7);
  const int wid = __builtin_amdgcn_readfirstlane(threadIdx.x >> 6), lane = threadIdx.x & 63;
  const int half = te >> 3;
  constexpr int FREE = ((C::LASTSLOT + 1) % C::NST) * C::STG;
  float* wsum = (float*)(shm + FREE);
  char* stg = shm + FREE + 1024;
  if (wid == 7) {
    const unsigned dummy = (unsigned)(size_t)(__attribute__((address_space(3))) char*)shm + C::LDS;
    const char* cp = (const char*)((bid & 1) ? Ci : Cr) + (size_t)(bid >> 1) * 65536;
    static_for<-1, C::NK - 1>([&](auto K) {
      constexpr int k = decltype(K)::value;
      if constexpr (k >= 0 && k < 8) {
#pragma unroll
        for (int i = 0; i < 8; ++i) glds16(lane * 16 + (k * 8 + i) * 1024, cp, dummy);
      }
      bar();
    });
    asm volatile("s_waitcnt vmcnt(0)" ::: "memory");
    lg_bar();
  } else if (wid >= 4) {
    const int lw = wid - 4, j = lane >> 3;
    const char* Wb = (const char*)(half ? Wim : Wre) + (size_t)(te & 7) * 80 * DM * 2;
    const char* xb = (const char*)x + (size_t)ts * 128 * DM * 2;
    auto sA = [&](int p) { return (unsigned)((32 * (p >> 2) + 16 * (p & 1) + 4 * ((p >> 1) & 1)) * DM * 2); };
    auto sB = [&](int p) { return (unsigned)(p * 8 * DM * 2); };
    auto ptrs = [&](int k, const char*& pa, const char*& pb) { pa = xb + k * 128; pb = Wb + k * 128; };
    const unsigned vA = (unsigned)((8 * (j >> 2) + (j & 3)) * DM * 2), vB = (unsigned)(j * DM * 2);
    if (lw == 0) loader<C, 6, 3>(shm, 0, 0, lane, sA, vA, sB, vB, ptrs);
    else if (lw == 1) loader<C, 5, 4>(shm, 6, 3, lane, sA, vA, sB, vB, ptrs);
    else loader<C, 5, 3>(shm, 11, 7, lane, sA, vA, sB, vB, ptrs);
    lg_bar();
  } else {
    const float sgn = half ? -1.0f : 1.0f;
    if (wid == 0) k1_consumer<0>(shm, wsum, stg, lane, sgn );
    else if (wid == 1) k1_consumer<1>(shm, wsum, stg, lane, sgn );
    else if (wid == 2) k1_consumer<2>(shm, wsum, stg, lane, sgn );
    else k1_consumer<3>(shm, wsum, stg, lane, sgn );
  }
  lg_bar();
  u16* ob = abT + (size_t)(te & 7) * 80 * 4096 + half * 2048 + ts * 128;
#pragma unroll
  for (int i = 0; i < 3; ++i) {
    int c = threadIdx.x + i * 512;
    if (i < 2 || c < 1280) {
      int row = c >> 4, ch = c & 15;
      *(u32x4*)(ob + (size_t)row * 4096 + ch * 8) = *(const u32x4*)(stg + row * 272 + ch * 16);
    }
  }
}

template <class C, bool STATS>
DEVI void resid_epilogue(f32x4 (&acc)[5][2], bool is_consumer, char* shm, int wid, int lane, u32x4 r0, u32x4 r1, u16* outp, f32x2* stats) {
  static_assert(C::LASTSLOT == C::NST - 1 && (C::NST - 1) * C::STG >= 2 * 64 * 336, "");
  if (is_consumer) {
    const int fr = lane & 15, fq = lane >> 4, kg = wid >> 1, wb = wid & 1;
#pragma unroll
    for (int n = 0; n < 2; ++n)
#pragma unroll
      for (int j = 0; j < 5; ++j) *(f32x4*)(shm + kg * (64 * 336) + ((wb * 2 + n) * 16 + fr) * 336 + j * 64 + fq * 16) = acc[j][n];
  }
  lg_bar();
  const int ch = lane & 15; const bool act = ch < 10; const int chc = act ? ch : 9;
#pragma unroll
  for (int i = 0; i < 2; ++i) {
    const int row = (threadIdx.x >> 4) + i * 32;
    const char* p = shm + row * 336 + chc * 32;
    f32x4 a0 = *(const f32x4*)p + *(const f32x4*)(p + 64 * 336), a1 = *(const f32x4*)(p + 16) + *(const f32x4*)(p + 16 + 64 * 336);
    u32x4 r = i == 0 ? r0 : r1, o;
    o.x = pack2(a0[0] + bflo(r.x), a0[1] + bfhi(r.x)); o.y = pack2(a0[2] + bflo(r.y), a0[3] + bfhi(r.y));
    o.z = pack2(a1[0] + bflo(r.z), a1[1] + bfhi(r.z)); o.w = pack2(a1[2] + bflo(r.w), a1[3] + bfhi(r.w));
    if (act) *(u32x4*)(outp + (size_t)row * DM + ch * 8) = o;
    if constexpr (STATS) {
      float v[8] = {bflo(o.x), bfhi(o.x), bflo(o.y), bfhi(o.y), bflo(o.z), bfhi(o.z), bflo(o.w), bfhi(o.w)};
      float s = ((v[0] + v[1]) + (v[2] + v[3])) + ((v[4] + v[5]) + (v[6] + v[7]));
      s = red16(act ? s : 0.0f);
      const float mu = s * (1.0f / 80);
      float m2 = 0;
#pragma unroll
      for (int e = 0; e < 8; ++e) { float d = v[e] - mu; m2 += d * d; }
      m2 = red16(act ? m2 : 0.0f);
      if (ch == 0) stats[row * 8] = f32x2{s, m2};
    }
  }
}
DEVI void resid_load(const u16* rp, u32x4& r0, u32x4& r1) {
  int ch = threadIdx.x & 15; if (ch > 9) ch = 9;
  const int row = threadIdx.x >> 4;
  r0 = *(const u32x4*)(rp + (size_t)row * DM + ch * 8);
  r1 = *(const u32x4*)(rp + (size_t)(row + 32) * DM + ch * 8);
}

#define K2_PF 0
#define K2_PRE 4
using C2 = Cfg<80, 64, 1, 2, 2, 128, 4, 32, K2_PRE>;
__global__ __launch_bounds__(512, 2) void k2_seq(const u16* __restrict__ x, const u16* __restrict__ Cr, const u16* __restrict__ Ci,
                                                 const u16* __restrict__ abT, const u16* __restrict__ Wg, const u16* __restrict__ Wu,
                                                 const u16* __restrict__ Wd, u16* __restrict__ h, u16* __restrict__ WguT, u16* __restrict__ WdT,
                                                 f32x2* __restrict__ stats, float* __restrict__ csp) {
  using C = C2;
  extern __shared__ __attribute__((aligned(16))) char shm[];
  const int bid = blockIdx.x, xcd = bid & 7, idx = bid >> 3;
  const int tt = xcd * 4 + (idx >> 3), te = idx & 7;
  const int wid = __builtin_amdgcn_readfirstlane(threadIdx.x >> 6), lane = threadIdx.x & 63;
  u32x4 r0, r1;
  f32x4 acc[5][2];
  if (wid == 7) {
    side_jobs<C::NK>(bid, lane, Wg, Wu, Wd, WguT, WdT, csp);
    resid_load(x + (size_t)tt * 64 * DM + te * 80, r0, r1);
  } else if (wid >= 4) {
    const int lw = wid - 4, j = lane >> 3;
    const char* ab = (const char*)abT + (size_t)te * 80 * 8192;
    const char* cr = (const char*)Cr + (size_t)tt * 64 * 4096;
    const char* ci = (const char*)Ci + (size_t)tt * 64 * 4096;
    auto sA = [&](int p) { return (unsigned)(p * 8 * 8192); };
    auto sB = [&](int p) { return (unsigned)(p * 8 * 4096); };
    auto ptrs = [&](int k, const char*& pa, const char*& pb) { pa = ab + k * 256; pb = k < 16 ? cr + k * 256 : ci + (k - 16) * 256; };
    if (lw < 2) loader<C, 7, 5>(shm, lw * 7, lw * 5, lane, sA, (unsigned)(j * 8192), sB, (unsigned)(j * 4096), ptrs);
    else loader<C, 6, 6>(shm, 14, 10, lane, sA, (unsigned)(j * 8192), sB, (unsigned)(j * 4096), ptrs);
    resid_load(x + (size_t)tt * 64 * DM + te * 80, r0, r1);
  } else {
    resid_load(x + (size_t)tt * 64 * DM + te * 80, r0, r1);
    consumer<C>(acc, shm, wid, lane, [](auto&) {} );
  }
  resid_epilogue<C, true>(acc, wid < 4, shm, wid, lane, r0, r1, h + (size_t)tt * 64 * DM + te * 80, stats + tt * 64 * 8 + te);
}

#define K3_NST 4
#define K3_PF 0
#define K3_PRE 2
using C3 = Cfg<192, 128, 2, 2, 1, 64, K3_NST, 10, K3_PRE>;
__global__ __launch_bounds__(512, 2) void k3_ffn1(const u16* __restrict__ h, const u16* __restrict__ WguT, const float* __restrict__ csum,
                                                  const f32x2* __restrict__ stats, u16* __restrict__ act) {
  using C = C3;
  extern __shared__ __attribute__((aligned(16))) char shm[];
  const int bid = blockIdx.x, xcd = bid & 7, idx = bid >> 3;
  const int tm = (xcd >> 2) * 8 + (idx >> 2), tn = (xcd & 3) * 4 + (idx & 3);
  const int wid = __builtin_amdgcn_readfirstlane(threadIdx.x >> 6), lane = threadIdx.x & 63;
  constexpr int FREE = ((C::LASTSLOT + 1) % C::NST) * C::STG;
  float* lcs = (float*)(shm + FREE);
  f32x2* rowst = (f32x2*)(shm + FREE + 1024);
  char* stg = shm + FREE + 4096;
  if (wid == 7) {
    f32x4 sp[8]; float cv[3][20];
    const f32x4* q = (const f32x4*)(stats + (size_t)tm * 128 * 8) + lane;
#pragma unroll
    for (int i = 0; i < 8; ++i) sp[i] = q[i * 64];
#pragma unroll
    for (int i = 0; i < 3; ++i) { const int r = i * 64 + lane; const float* cq = csum + ((r >> 4) & 1) * FF + tn * 96 + 16 * (r >> 5) + (r & 15);
#pragma unroll
      for (int t = 0; t < 20; ++t) cv[i][t] = cq[t * 2 * FF]; }
    unsigned sink = 0;
    for (int k = -1; k < C::NK - 1; ++k) bar();
    touch_done(sink);
#pragma unroll
    for (int i = 0; i < 8; ++i) {
      float sm = sp[i][0] + sp[i][2]; sm += dppf<0xB1>(sm); sm += dppf<0x4E>(sm);
      const float mean = sm * (1.0f / DM);
      float d0 = sp[i][0] * (1.0f / 80) - mean, d1 = sp[i][2] * (1.0f / 80) - mean;
      float m2 = sp[i][1] + sp[i][3] + 80.0f * (d0 * d0 + d1 * d1);
      m2 += dppf<0xB1>(m2); m2 += dppf<0x4E>(m2);
      if ((lane & 3) == 0) rowst[i * 16 + (lane >> 2)] = f32x2{-mean, m2 * (1.0f / DM) + 1e-5f};
    }
#pragma unroll
    for (int i = 0; i < 3; ++i) { float c = 0;
#pragma unroll
      for (int t = 0; t < 20; ++t) c += cv[i][t];
      lcs[i * 64 + lane] = c; }
    lg_bar();
  } else if (wid >= 4) {
    const int lw = wid - 4, j = lane >> 3;
    const char* wbp = (const char*)WguT + (size_t)tn * 96 * DM * 2;
    const char* hb = (const char*)h + (size_t)tm * 128 * DM * 2;
    auto sA = [&](int p) { return (unsigned)((((p >> 1) & 1) * FF + 16 * (p >> 2) + 8 * (p & 1)) * DM * 2); };
    auto sB = [&](int p) { return (unsigned)(p * 8 * DM * 2); };
    auto ptrs = [&](int k, const char*& pa, const char*& pb) { pa = wbp + k * 128; pb = hb + k * 128; };
    if (lw == 0) loader<C, 8, 6>(shm, 0, 0, lane, sA, (unsigned)(j * DM * 2), sB, (unsigned)(j * DM * 2), ptrs);
    else loader<C, 8, 5>(shm, lw * 8, 1 + lw * 5, lane, sA, (unsigned)(j * DM * 2), sB, (unsigned)(j * DM * 2), ptrs);
    lg_bar();
  } else {
    const int fr = lane & 15, fq = lane >> 4, wa = wid >> 1, wb = wid & 1;
    f32x4 acc[6][4];
    consumer<C>(acc, shm, wid, lane, [](auto&) {} );
    lg_bar();
    f32x4 cs[6]; float nmean[4], ve[4];
#pragma unroll
    for (int j = 0; j < 6; ++j) cs[j] = *(const f32x4*)(lcs + wa * 96 + j * 16 + 4 * fq);
#pragma unroll
    for (int n = 0; n < 4; ++n) { const f32x2 st = rowst[(wb * 4 + n) * 16 + fr]; nmean[n] = st[0]; ve[n] = st[1]; }
#pragma unroll
    for (int n = 0; n < 4; ++n) {
      const int ml = (wb * 4 + n) * 16 + fr;
      const float rstd = rsqrtf(ve[n]);
      const float k1 = -2.3022081985f * rstd;
      const float k2 = k1 * 0.044715f * rstd * rstd;
      f32x4 G[3], U[3], z[3];
#pragma unroll
      for (int p = 0; p < 3; ++p) G[p] = cs[2 * p] * nmean[n] + acc[2 * p][n];
#pragma unroll
      for (int p = 0; p < 3; ++p) z[p] = G[p] * G[p];
#pragma unroll
      for (int p = 0; p < 3; ++p) U[p] = cs[2 * p + 1] * nmean[n] + acc[2 * p + 1][n];
#pragma unroll
      for (int p = 0; p < 3; ++p) z[p] = z[p] * k2 + k1;
#pragma unroll
      for (int p = 0; p < 3; ++p) z[p] = z[p] * G[p];
      SB();
#pragma unroll
      for (int p = 0; p < 3; ++p) { z[p][0] = __builtin_amdgcn_exp2f(z[p][0]); z[p][1] = __builtin_amdgcn_exp2f(z[p][1]); z[p][2] = __builtin_amdgcn_exp2f(z[p][2]); z[p][3] = __builtin_amdgcn_exp2f(z[p][3]); U[p] = U[p] * G[p]; }
#pragma unroll
      for (int p = 0; p < 3; ++p) z[p] = z[p] * ve[n] + ve[n];
      SB();
#pragma unroll
      for (int p = 0; p < 3; ++p) { z[p][0] = __builtin_amdgcn_rcpf(z[p][0]); z[p][1] = __builtin_amdgcn_rcpf(z[p][1]); z[p][2] = __builtin_amdgcn_rcpf(z[p][2]); z[p][3] = __builtin_amdgcn_rcpf(z[p][3]); }
#pragma unroll
      for (int p = 0; p < 3; ++p) {
        f32x4 v = U[p] * z[p];
        u32x2 o; o.x = pack2(v[0], v[1]); o.y = pack2(v[2], v[3]);
        *(u32x2*)(stg + ml * 208 + (wa * 48 + p * 16 + fq * 4) * 2) = o;
      }
    }
  }
  lg_bar();
  u16* ob = act + (size_t)tm * 128 * FF + tn * 96;
#pragma unroll
  for (int i = 0; i < 3; ++i) {
    int c = threadIdx.x + i * 512, row = c / 12, ch = c % 12;
    *(u32x4*)(ob + (size_t)row * FF + ch * 8) = *(const u32x4*)(stg + row * 208 + ch * 16);
  }
}

#define K4_PF 0
#define K4_PRE 4
using C4 = Cfg<80, 64, 1, 2, 2, 128, 4, 12, K4_PRE>;
__global__ __launch_bounds__(512, 2) void k4_ffn2(const u16* __restrict__ h, const u16* __restrict__ act, const u16* __restrict__ WdT,
                                                  u16* __restrict__ out) {
  using C = C4;
  extern __shared__ __attribute__((aligned(16))) char shm[];
  const int bid = blockIdx.x, xcd = bid & 7, idx = bid >> 3;
  const int tm = (xcd >> 1) * 8 + (idx >> 2), td = (xcd & 1) * 4 + (idx & 3);
  const int wid = __builtin_amdgcn_readfirstlane(threadIdx.x >> 6), lane = threadIdx.x & 63;
  u32x4 r0, r1;
  f32x4 acc[5][2];
  if (wid == 7) {
    unsigned sink = 0;
    for (int k = -1; k < C::NK - 1; ++k) bar();
    touch_done(sink);
    resid_load(h + (size_t)tm * 64 * DM + td * 80, r0, r1);
  } else if (wid >= 4) {
    const int lw = wid - 4, j = lane >> 3;
    const char* wp = (const char*)WdT + (size_t)td * 80 * FF * 2;
    const char* ap = (const char*)act + (size_t)tm * 64 * FF * 2;
    auto sA = [&](int p) { return (unsigned)(p * 8 * FF * 2); };
    auto ptrs = [&](int k, const char*& pa, const char*& pb) { pa = wp + k * 256; pb = ap + k * 256; };
    if (lw < 2) loader<C, 7, 5>(shm, lw * 7, lw * 5, lane, sA, (unsigned)(j * FF * 2), sA, (unsigned)(j * FF * 2), ptrs);
    else loader<C, 6, 6>(shm, 14, 10, lane, sA, (unsigned)(j * FF * 2), sA, (unsigned)(j * FF * 2), ptrs);
    resid_load(h + (size_t)tm * 64 * DM + td * 80, r0, r1);
  } else {
    resid_load(h + (size_t)tm * 64 * DM + td * 80, r0, r1);
    consumer<C>(acc, shm, wid, lane, [](auto&) {} );
  }
  resid_epilogue<C, false>(acc, wid < 4, shm, wid, lane, r0, r1, out + (size_t)tm * 64 * DM + td * 80, nullptr);
}

static void launch_stage(int which, void* const* d_in, void* d_out, void* d_ws, hipStream_t stream) {
  const u16* x = (const u16*)d_in[0];
  const u16* Cr = (const u16*)d_in[1];
  const u16* Ci = (const u16*)d_in[2];
  const u16* Wre = (const u16*)d_in[3];
  const u16* Wim = (const u16*)d_in[4];
  const u16* Wg = (const u16*)d_in[6];
  const u16* Wu = (const u16*)d_in[7];
  const u16* Wd = (const u16*)d_in[8];
  char* ws = (char*)d_ws;
  u16* abT = (u16*)(ws);
  u16* h = (u16*)(ws + (6 << 20));
  u16* WguT = (u16*)(ws + (10 << 20));
  u16* WdT = (u16*)(ws + (14 << 20));
  u16* act = (u16*)(ws + (16 << 20));
  f32x2* stats = (f32x2*)(ws + (24 << 20));
  float* csp = (float*)(ws + (25 << 20));
  float* csum = (float*)(ws + (26 << 20));
  u16* out = (u16*)d_out;
  if (which == 0) {
    (void)hipFuncSetAttribute((const void*)k1_hid, hipFuncAttributeMaxDynamicSharedMemorySize, C1::LDS + 1024);
    k1_hid<<<256, 512, C1::LDS + 1024, stream>>>(x, Wre, Wim, Cr, Ci, abT);
  } else if (which == 1) {
    (void)hipFuncSetAttribute((const void*)k2_seq, hipFuncAttributeMaxDynamicSharedMemorySize, C2::LDS);
    k2_seq<<<256, 512, C2::LDS, stream>>>(x, Cr, Ci, abT, Wg, Wu, Wd, h, WguT, WdT, stats, csp);
  } else if (which == 2) {
    (void)hipFuncSetAttribute((const void*)k3_ffn1, hipFuncAttributeMaxDynamicSharedMemorySize, C3::LDS);
    k3_ffn1<<<256, 512, C3::LDS, stream>>>(h, WguT, csp, stats, act);
  } else {
    (void)hipFuncSetAttribute((const void*)k4_ffn2, hipFuncAttributeMaxDynamicSharedMemorySize, C4::LDS);
    k4_ffn2<<<256, 512, C4::LDS, stream>>>(h, act, WdT, out);
  }
}

extern "C" void kernel_launch(void* const* d_in, const int* in_sizes, int n_in, void* d_out, int out_size, void* d_ws, size_t ws_size,
                              hipStream_t stream) {
  for (int i = 0; i < 4; ++i) launch_stage(i, d_in, d_out, d_ws, stream);
}
```

```cpp
#include <hip/hip_runtime.h>
#include <hip/hip_bf16.h>
#include <stdint.h>
#include <type_traits>

typedef unsigned short u16;
typedef __attribute__((ext_vector_type(8))) short bf16x8;
typedef __attribute__((ext_vector_type(4))) float f32x4;
typedef __attribute__((ext_vector_type(2))) float f32x2;
typedef __attribute__((ext_vector_type(2))) __bf16 bf16x2;
typedef __attribute__((ext_vector_type(2))) unsigned u32x2;
typedef __attribute__((ext_vector_type(4))) unsigned u32x4;

#define DEVI __device__ __forceinline__
#define IC(x) std::integral_constant<int, (x)>{}
#define SB() __builtin_amdgcn_sched_barrier(0)
#define MFMA(a, b, c) __builtin_amdgcn_mfma_f32_16x16x32_bf16(a, b, c, 0, 0, 0)

constexpr int SEQ = 2048, DM = 640, FF = 1536;

template <int B, int E, class F> DEVI void static_for(F&& f) {
  if constexpr (B < E) { f(IC(B)); static_for<B + 1, E>(f); }
}
DEVI void glds16(unsigned voff, const char* sbase, unsigned ldsaddr) {
  asm volatile("s_mov_b32 m0, %2\n\ts_nop 0\n\tglobal_load_lds_dwordx4 %0, %1" ::"v"(voff), "s"(sbase), "s"(ldsaddr));
}
template <int N> DEVI void waitvm_bar() { asm volatile("s_waitcnt vmcnt(%0)\n\ts_barrier" ::"i"(N) : "memory"); }
DEVI void bar() { asm volatile("s_barrier" ::: "memory"); }
DEVI void touch(unsigned& sink, unsigned voff, const void* sbase) { asm volatile("global_load_dword %0, %1, %2" : "+v"(sink) : "v"(voff), "s"(sbase)); }
DEVI void touch_done(unsigned& sink) { asm volatile("s_waitcnt vmcnt(0)" : "+v"(sink)::"memory"); }
DEVI void lg_bar() { asm volatile("s_waitcnt lgkmcnt(0)\n\ts_barrier" ::: "memory"); }
DEVI unsigned pack2(float a, float b) { f32x2 p = {a, b}; return __builtin_bit_cast(unsigned, __builtin_convertvector(p, bf16x2)); }
DEVI float bflo(unsigned v) { return __builtin_bit_cast(float, v << 16); }
DEVI float bfhi(unsigned v) { return __builtin_bit_cast(float, v & 0xffff0000u); }
template <int CTRL> DEVI float dppf(float v) {
  return __builtin_bit_cast(float, __builtin_amdgcn_update_dpp(0, __builtin_bit_cast(int, v), CTRL, 0xf, 0xf, true));
}
DEVI float red16(float v) { v += dppf<0xB1>(v); v += dppf<0x4E>(v); v += dppf<0x141>(v); v += dppf<0x140>(v); return v; }

template <int TA_, int TB_, int NWA_, int NWB_, int KS_, int BK_, int NST_, int NK_, int PRE_> struct Cfg {
  static constexpr int TA = TA_, TB = TB_, NWA = NWA_, NWB = NWB_, KS = KS_, BK = BK_, NST = NST_, NK = NK_;
  static constexpr int NSUB = BK / 64, PA = TA / 8, PB = TB / 8, PPS = PA + PB, STG = NSUB * PPS * 1024, SUBB = PPS * 1024;
  static constexpr int WAF = TA / 16 / NWA, WBF = TB / 16 / NWB, KH = (BK / 32) / KS;
  static constexpr int NF = WAF + WBF, NMF = WAF * WBF;
  static constexpr int LDS = NST * STG;
  static constexpr int LASTSLOT = (NK - 1) % NST;
  static constexpr int issued(int k) {
    if (k < -1) return -1;
    int a = 2 * k + 1 + PRE_, b = k - 1 + NST, c = NK - 1;
    if (k == -1) b = NST - 1;
    int m = a < b ? a : b; return m < c ? m : c;
  }
  static constexpr int steady_begin() { int k = 0; while (!(issued(k) == k - 1 + NST && issued(k - 1) == k - 2 + NST) && k < NK) ++k; return k; }
  static constexpr int K0 = steady_begin() < NK - NST + 1 ? steady_begin() : NK - NST + 1;
  static constexpr int K1 = NK - NST + 1 > K0 ? NK - NST + 1 : K0;
};

template <class C, int GA, int GB, int EXTRA = 0, class SA, class SB_, class Ptrs, class Tail>
DEVI void loader(char* shm, int a0, int b0, int lane, SA sA, unsigned vA, SB_ sB, unsigned vB, Ptrs ptrs, Tail tail) {
  constexpr int G = GA + GB;
  const unsigned sw0 = (unsigned)(((lane & 7) ^ (lane >> 4)) * 16);
  unsigned voff[G], lo[G];
  const unsigned ldsb = (unsigned)(size_t)(__attribute__((address_space(3))) char*)shm;
#pragma unroll
  for (int i = 0; i < GA; ++i) {
    int p = a0 + i, sub = p / C::PA, pr = p - sub * C::PA;
    voff[i] = vA + (sw0 ^ (unsigned)((pr & 1) * 64)) + (sA(pr) + (unsigned)sub * 128u);
    lo[i] = ldsb + sub * C::SUBB + pr * 1024;
  }
#pragma unroll
  for (int i = 0; i < GB; ++i) {
    int p = b0 + i, sub = p / C::PB, pr = p - sub * C::PB;
    voff[GA + i] = vB + (sw0 ^ (unsigned)((pr & 1) * 64)) + (sB(pr) + (unsigned)sub * 128u);
    lo[GA + i] = ldsb + sub * C::SUBB + (C::PA + pr) * 1024;
  }
  auto issue = [&](int s, unsigned slot) {
    const char *pa, *pb; ptrs(s, pa, pb);
#pragma unroll
    for (int i = 0; i < G; ++i) glds16(voff[i], i < GA ? pa : pb, lo[i] + slot);
  };
  static_for<-1, C::K0>([&](auto K) {
    constexpr int k = decltype(K)::value;
    static_for<C::issued(k - 1) + 1, C::issued(k) + 1>([&](auto S) { constexpr int s = decltype(S)::value; issue(s, (s % C::NST) * C::STG); });
    waitvm_bar<(C::issued(k) - (k + 1)) * G>();
  });
  unsigned slot = ((C::K0 - 1 + C::NST) % C::NST) * C::STG;
  for (int k = C::K0; k < C::K1; ++k) {
    issue(k - 1 + C::NST, slot);
    slot += C::STG; if (slot == C::NST * C::STG) slot = 0;
    waitvm_bar<(C::NST - 2) * G>();
  }
  static_assert(C::issued(C::K1 - 1) == C::NK - 1, "ring fully issued before the tail");
  tail();
  static_for<C::K1, C::NK - 1>([&](auto K) { constexpr int k = decltype(K)::value; waitvm_bar<(C::NK - 2 - k) * G + EXTRA>(); });
}

template <class C, class Extra> DEVI void consumer(f32x4 (&acc)[C::WAF][C::WBF], char* shm, int wid, int lane, Extra extra ) {
  constexpr int WAF = C::WAF, WBF = C::WBF, KH = C::KH, NF = C::NF, NMF = C::NMF, STG = C::STG, NST = C::NST, NK = C::NK;
  const int kg = wid / (C::NWA * C::NWB), wab = wid % (C::NWA * C::NWB), wa = wab / C::NWB, wb = wab % C::NWB;
#pragma unroll
  for (int j = 0; j < WAF; ++j)
#pragma unroll
    for (int b = 0; b < WBF; ++b) acc[j][b] = f32x4{0, 0, 0, 0};
  const int fr = lane & 15, fq = lane >> 4;
  const int c0 = fr * 128 + ((fq ^ (fr >> 1)) * 16);
  int rda[KH], rdb[KH];
#pragma unroll
  for (int h = 0; h < KH; ++h) {
    int ks = kg * KH + h, o = (c0 ^ ((ks & 1) * 64)) + (ks >> 1) * C::SUBB;
    rda[h] = o + wa * WAF * 2048; rdb[h] = o + C::TA * 128 + wb * WBF * 2048;
  }
  bf16x8 R[2][NF];
  auto rd1 = [&](bf16x8 (&Rn)[NF], unsigned slot, int h, int f) {
    if (f < WAF) Rn[f] = *(const bf16x8*)(shm + slot + rda[h] + f * 2048);
    else Rn[f] = *(const bf16x8*)(shm + slot + rdb[h] + (f - WAF) * 2048);
  };
  auto body = [&](bf16x8 (&Rc)[NF], bf16x8 (&Rn)[NF], unsigned slot, int h, bool dor) {
    int ri = 0;
#pragma unroll
    for (int m = 0; m < NMF; ++m) {
      { int n = m / WAF, j = m % WAF; acc[j][n] = MFMA(Rc[j], Rc[WAF + n], acc[j][n]); }
      int rt = ((m + 1) * NF * 4 + 3 * NMF - 1) / (3 * NMF); if (rt > NF) rt = NF;
#pragma unroll
      for (; ri < rt; ++ri) if (dor) rd1(Rn, slot, h, ri);
      SB();
    }
    extra(Rc);
    SB();
  };
  lg_bar();
#pragma unroll
  for (int f = 0; f < NF; ++f) rd1(R[0], 0, 0, f);
  unsigned slot = 0;
  auto stage = [&](auto P, bool last) {
    constexpr int par = decltype(P)::value;
    unsigned nslot = slot + STG; if (nslot == NST * STG) nslot = 0;
#pragma unroll
    for (int h = 0; h < KH; ++h) {
      const int c = (par + h) & 1;
      if (h == KH - 1) { if (!last) { lg_bar(); body(R[c], R[c ^ 1], nslot, 0, true); } else body(R[c], R[c ^ 1], 0, 0, false); }
      else body(R[c], R[c ^ 1], slot, h + 1, true);
    }
    slot = nslot;
  };
  if constexpr (KH % 2 == 0) {
    for (int k = 0; k < NK - 1; ++k) stage(IC(0), false);
    stage(IC(0), true);
  } else {
    static_assert(NK % 2 == 0, "");
    for (int k = 0; k < NK - 2; k += 2) { stage(IC(0), false); stage(IC(1), false); }
    stage(IC(0), false); stage(IC(1), true);
  }
}

struct TrUnit { u32x2 v[8]; };
DEVI void tr_load(TrUnit& t, const u16* W, int N, int k0, int n0, int lane) {
  const unsigned vo = (unsigned)(((8 * (lane >> 4)) * N + 4 * (lane & 15)) * 2);
  const char* b = (const char*)(W + (size_t)k0 * N + n0);
#pragma unroll
  for (int i = 0; i < 8; ++i) asm volatile("global_load_dwordx2 %0, %1, %2" : "=v"(t.v[i]) : "v"(vo), "s"(b + (size_t)i * N * 2));
}
DEVI void tr_wait(TrUnit& t) {
  asm volatile("s_waitcnt vmcnt(0)" : "+v"(t.v[0]), "+v"(t.v[1]), "+v"(t.v[2]), "+v"(t.v[3]), "+v"(t.v[4]), "+v"(t.v[5]), "+v"(t.v[6]), "+v"(t.v[7])::"memory");
}
DEVI void tr_store(const TrUnit& t, u16* WT, int K, int k0, int n0, int lane) {
  const int src = (((lane & 3) << 4) | (lane >> 2)) << 2;
  u16* p = WT + (size_t)(n0 + 4 * (lane >> 2)) * K + k0 + 8 * (lane & 3);
  u32x4 o;
#define BP(v_) (unsigned)__builtin_amdgcn_ds_bpermute(src, (int)(v_))
#define TRP(W_, SEL) o.x = BP(__builtin_amdgcn_perm(t.v[1].W_, t.v[0].W_, SEL)); o.y = BP(__builtin_amdgcn_perm(t.v[3].W_, t.v[2].W_, SEL)); \
                     o.z = BP(__builtin_amdgcn_perm(t.v[5].W_, t.v[4].W_, SEL)); o.w = BP(__builtin_amdgcn_perm(t.v[7].W_, t.v[6].W_, SEL));
  TRP(x, 0x05040100u) *(u32x4*)(p) = o;
  TRP(x, 0x07060302u) *(u32x4*)(p + K) = o;
  TRP(y, 0x05040100u) *(u32x4*)(p + 2 * K) = o;
  TRP(y, 0x07060302u) *(u32x4*)(p + 3 * K) = o;
#undef TRP
#undef BP
}
DEVI float fadd_(float a, float b) { float r; asm("v_add_f32 %0, %1, %2" : "=v"(r) : "v"(a), "v"(b)); return r; }
template <int H> DEVI void tr_colsum(const TrUnit& t, float (&c)[4]) {
#pragma unroll
  for (int i = 4 * H; i < 4 * H + 4; ++i) {
    c[0] = fadd_(c[0], bflo(t.v[i].x)); c[1] = fadd_(c[1], bfhi(t.v[i].x)); c[2] = fadd_(c[2], bflo(t.v[i].y)); c[3] = fadd_(c[3], bfhi(t.v[i].y)); }
  if constexpr (H == 1) {
#pragma unroll
    for (int i = 0; i < 4; ++i) { c[i] = fadd_(c[i], __shfl_xor(c[i], 16)); c[i] = fadd_(c[i], __shfl_xor(c[i], 32)); }
  }
}
struct TrJob { const u16* src; u16* dst; int lds, ldd, k0, n0, cso; bool ok, cs; };
DEVI TrJob tr_job(int u, const u16* Wg, const u16* Wu, const u16* Wd, u16* WguT, u16* WdT) {
  TrJob j; j.ok = u < 1440; if (!j.ok) u = 1439;
  const int w = u / 480, r = u - w * 480;
  j.cs = w < 2;
  if (w < 2) { const int kb = r / 24; j.src = w ? Wu : Wg; j.dst = WguT + (size_t)w * FF * DM; j.lds = FF; j.ldd = DM; j.k0 = kb * 32; j.n0 = (r - kb * 24) * 64; j.cso = kb * 2 * FF + w * FF + j.n0; }
  else { const int kb = r / 10; j.src = Wd; j.dst = WdT; j.lds = DM; j.ldd = FF; j.k0 = kb * 32; j.n0 = (r - kb * 10) * 64; j.cso = 0; }
  return j;
}

#define K1_NST 6
#define K1_PRE 3
using C1 = Cfg<128, 80, 4, 1, 1, 64, K1_NST, 10, K1_PRE>;
template <int W> DEVI void k1_consumer(char* shm, float* wsum, char* stg, int lane, float sgn ) {
  using C = C1;
  f32x4 acc[2][5];
  f32x4 sx[2] = {{0, 0, 0, 0}, {0, 0, 0, 0}}, sq[2] = {{0, 0, 0, 0}, {0, 0, 0, 0}}, sw = {0, 0, 0, 0}, sw4 = {0, 0, 0, 0};
  bf16x8 ones = {0x3f80, 0x3f80, 0x3f80, 0x3f80, 0x3f80, 0x3f80, 0x3f80, 0x3f80};
  asm volatile("" : "+v"(ones));
  consumer<C>(acc, shm, W, lane, [&](bf16x8 (&R)[7]) {
    sx[0] = MFMA(R[0], ones, sx[0]); sx[1] = MFMA(R[1], ones, sx[1]);
    sq[0] = MFMA(R[0], R[0], sq[0]); sq[1] = MFMA(R[1], R[1], sq[1]);
    sw = MFMA(ones, R[2 + W], sw);
    if constexpr (W == 0) sw4 = MFMA(ones, R[6], sw4);
  } );
  const int fr = lane & 15, fq = lane >> 4;
  if (lane < 16) { wsum[W * 16 + lane] = sw[0]; if constexpr (W == 0) wsum[64 + lane] = sw4[0]; }
  f32x4 rs[2], mr[2];
#pragma unroll
  for (int a = 0; a < 2; ++a)
#pragma unroll
    for (int r = 0; r < 4; ++r) {
      float s2 = __shfl(sq[a][r], 20 * fq + r);
      float mean = sx[a][r] * (1.0f / DM);
      float var = fmaxf(s2 * (1.0f / DM) - mean * mean, 0.0f);
      float rstd = sgn * rsqrtf(var + 1e-5f);
      rs[a][r] = rstd; mr[a][r] = mean * rstd;
    }
  lg_bar();
#pragma unroll
  for (int b = 0; b < 5; ++b) {
    float w = wsum[b * 16 + fr];
    f32x4 v0 = acc[0][b] * rs[0] - w * mr[0], v1 = acc[1][b] * rs[1] - w * mr[1];
    u32x4 o; o.x = pack2(v0[0], v0[1]); o.y = pack2(v0[2], v0[3]); o.z = pack2(v1[0], v1[1]); o.w = pack2(v1[2], v1[3]);
    *(u32x4*)(stg + (b * 16 + fr) * 272 + W * 64 + fq * 16) = o;
  }
}
#define K1_PFC 1
__global__ __launch_bounds__(512, 2) void k1_hid(const u16* __restrict__ x, const u16* __restrict__ Wre, const u16* __restrict__ Wim,
                                                 const u16* __restrict__ Cr, const u16* __restrict__ Ci, const u16* __restrict__ Wg,
                                                 const u16* __restrict__ Wu, const u16* __restrict__ Wd, u16* __restrict__ abT,
                                                 u16* __restrict__ WguT, u16* __restrict__ WdT, float* __restrict__ csp) {
  using C = C1;
  extern __shared__ __attribute__((aligned(16))) char shm[];
  const int bid = blockIdx.x, xcd = bid & 7, idx = bid >> 3;
  const int ts = (xcd >> 1) * 4 + (idx >> 3), te = (xcd & 1) * 8 + (idx & 7);
  const int wid = __builtin_amdgcn_readfirstlane(threadIdx.x >> 6), lane = threadIdx.x & 63;
  const int half = te >> 3;
  constexpr int FREE = ((C::LASTSLOT + 1) % C::NST) * C::STG;
  float* wsum = (float*)(shm + FREE);
  char* stg = shm + FREE + 1024;
  if (wid >= 4) {
    const int lw = wid - 4, j = lane >> 3;
    const char* Wb = (const char*)(half ? Wim : Wre) + (size_t)(te & 7) * 80 * DM * 2;
    const char* xb = (const char*)x + (size_t)ts * 128 * DM * 2;
    auto sA = [&](int p) { return (unsigned)((32 * (p >> 2) + 16 * (p & 1) + 4 * ((p >> 1) & 1)) * DM * 2); };
    auto sB = [&](int p) { return (unsigned)(p * 8 * DM * 2); };
    auto ptrs = [&](int k, const char*& pa, const char*& pb) { pa = xb + k * 128; pb = Wb + k * 128; };
    const unsigned vA = (unsigned)((8 * (j >> 2) + (j & 3)) * DM * 2), vB = (unsigned)(j * DM * 2);
    TrUnit tu[2];
    const TrJob j0 = tr_job(bid * 4 + lw, Wg, Wu, Wd, WguT, WdT), j1 = tr_job(1024 + bid * 4 + lw, Wg, Wu, Wd, WguT, WdT);
    auto tail = [&]() {
      tr_load(tu[0], j0.src, j0.lds, j0.k0, j0.n0, lane);
      tr_load(tu[1], j1.src, j1.lds, j1.k0, j1.n0, lane);
      const unsigned dummy = (unsigned)(size_t)(__attribute__((address_space(3))) char*)shm + C::LDS;
      const char* cp = (const char*)((bid & 1) ? Ci : Cr) + (size_t)(bid >> 1) * 65536 + lw * 16384;
#pragma unroll
      for (int i = 0; i < 16; ++i) glds16(lane * 16 + i * 1024, cp, dummy);
    };
    constexpr int EX = 16 + (K1_PFC ? 16 : 0);
    if (lw < 2) loader<C, 4, 3, EX>(shm, lw * 4, lw * 3, lane, sA, vA, sB, vB, ptrs, tail);
    else loader<C, 4, 2, EX>(shm, lw * 4, 2 + lw * 2, lane, sA, vA, sB, vB, ptrs, tail);
    tr_wait(tu[0]); tr_wait(tu[1]);
#pragma unroll
    for (int q = 0; q < 2; ++q) {
      const TrJob& jb = q ? j1 : j0;
      if (jb.ok) {
        tr_store(tu[q], jb.dst, jb.ldd, jb.k0, jb.n0, lane);
        if (jb.cs) {
          float c[4] = {0.f, 0.f, 0.f, 0.f}; tr_colsum<0>(tu[q], c); tr_colsum<1>(tu[q], c);
          if (lane < 16) *(f32x4*)(csp + jb.cso + 4 * lane) = f32x4{c[0], c[1], c[2], c[3]};
        }
      }
      if (q == 0) lg_bar();
    }
  } else {
    const float sgn = half ? -1.0f : 1.0f;
    if (wid == 0) k1_consumer<0>(shm, wsum, stg, lane, sgn );
    else if (wid == 1) k1_consumer<1>(shm, wsum, stg, lane, sgn );
    else if (wid == 2) k1_consumer<2>(shm, wsum, stg, lane, sgn );
    else k1_consumer<3>(shm, wsum, stg, lane, sgn );
  }
  lg_bar();
  u16* ob = abT + (size_t)(te & 7) * 80 * 4096 + half * 2048 + ts * 128;
#pragma unroll
  for (int i = 0; i < 3; ++i) {
    int c = threadIdx.x + i * 512;
    if (i < 2 || c < 1280) {
      int row = c >> 4, ch = c & 15;
      *(u32x4*)(ob + (size_t)row * 4096 + ch * 8) = *(const u32x4*)(stg + row * 272 + ch * 16);
    }
  }
}

DEVI u32x4 add8(f32x4 a0, f32x4 a1, u32x4 r) {
  u32x4 o;
  o.x = pack2(a0[0] + bflo(r.x), a0[1] + bfhi(r.x)); o.y = pack2(a0[2] + bflo(r.y), a0[3] + bfhi(r.y));
  o.z = pack2(a1[0] + bflo(r.z), a1[1] + bfhi(r.z)); o.w = pack2(a1[2] + bflo(r.w), a1[3] + bfhi(r.w));
  return o;
}
DEVI u32x4 addbf8(u32x4 a, u32x4 b) {
  u32x4 o;
  o.x = pack2(bflo(a.x) + bflo(b.x), bfhi(a.x) + bfhi(b.x)); o.y = pack2(bflo(a.y) + bflo(b.y), bfhi(a.y) + bfhi(b.y));
  o.z = pack2(bflo(a.z) + bflo(b.z), bfhi(a.z) + bfhi(b.z)); o.w = pack2(bflo(a.w) + bflo(b.w), bfhi(a.w) + bfhi(b.w));
  return o;
}

#define K2_PRE 6
#define K2_NST 6
using C2 = Cfg<80, 128, 1, 2, 2, 64, K2_NST, 32, K2_PRE>;
__global__ __launch_bounds__(512, 2) void k2_seq(const u16* __restrict__ x, const u16* __restrict__ Cr, const u16* __restrict__ Ci,
                                                 const u16* __restrict__ abT, const float* __restrict__ csp,
                                                 u16* __restrict__ P, float* __restrict__ csum) {
  using C = C2;
  extern __shared__ __attribute__((aligned(16))) char shm[];
  const int bid = blockIdx.x, xcd = bid & 7, idx = bid >> 3;
  const int half = xcd & 1, tt = (xcd >> 1) * 4 + (idx >> 3), te = idx & 7;
  const int wid = __builtin_amdgcn_readfirstlane(threadIdx.x >> 6), lane = threadIdx.x & 63;
  static_assert((C::NST - 1 - C::LASTSLOT) * C::STG >= 2 * 128 * 336, "");
  char* red = shm + (C::LASTSLOT + 1) * C::STG;
  u32x4 rx[3] = {{0, 0, 0, 0}, {0, 0, 0, 0}, {0, 0, 0, 0}};
  auto xload = [&]() {
    if (half == 0) {
#pragma unroll
      for (int i = 0; i < 3; ++i) { int c = threadIdx.x + i * 512; if (c > 1279) c = 1279;
        rx[i] = *(const u32x4*)(x + (size_t)(tt * 128 + c / 10) * DM + te * 80 + (c % 10) * 8); }
    }
  };
  if (wid >= 4) {
    const int lw = wid - 4, j = lane >> 3;
    const char* ab = (const char*)abT + (size_t)te * 80 * 8192 + half * 4096;
    const char* cc = (const char*)(half ? Ci : Cr) + (size_t)tt * 128 * 4096;
    auto sA = [&](int p) { return (unsigned)(p * 8 * 8192); };
    auto sB = [&](int p) { return (unsigned)(p * 8 * 4096); };
    auto ptrs = [&](int k, const char*& pa, const char*& pb) { pa = ab + k * 128; pb = cc + k * 128; };
    if (lw < 2) loader<C, 3, 4>(shm, lw * 3, lw * 4, lane, sA, (unsigned)(j * 8192), sB, (unsigned)(j * 4096), ptrs, []() {});
    else loader<C, 2, 4>(shm, 2 + lw * 2, lw * 4, lane, sA, (unsigned)(j * 8192), sB, (unsigned)(j * 4096), ptrs, []() {});
    xload();
  } else {
    if (wid == 0 && bid < 48) {
      const int col = bid * 64 + lane; float c = 0;
#pragma unroll
      for (int i = 0; i < 20; ++i) c += csp[(size_t)i * (2 * FF) + col];
      csum[col] = c;
    }
    xload();
    f32x4 acc[5][4];
    consumer<C>(acc, shm, wid, lane, [](auto&) {} );
    const int fr = lane & 15, fq = lane >> 4, kg = wid >> 1, wb = wid & 1;
#pragma unroll
    for (int n = 0; n < 4; ++n)
#pragma unroll
      for (int j = 0; j < 5; ++j) *(f32x4*)(red + kg * (128 * 336) + ((wb * 4 + n) * 16 + fr) * 336 + j * 64 + fq * 16) = acc[j][n];
  }
  lg_bar();
  u16* op = P + (size_t)half * SEQ * DM + (size_t)tt * 128 * DM + te * 80;
#pragma unroll
  for (int i = 0; i < 3; ++i) {
    const int c = threadIdx.x + i * 512;
    if (i < 2 || c < 1280) {
      const int row = c / 10, ch = c % 10;
      const char* p = red + row * 336 + ch * 32;
      f32x4 a0 = *(const f32x4*)p + *(const f32x4*)(p + 128 * 336), a1 = *(const f32x4*)(p + 16) + *(const f32x4*)(p + 16 + 128 * 336);
      *(u32x4*)(op + (size_t)row * DM + ch * 8) = add8(a0, a1, rx[i]);
    }
  }
}

#define K3_PRE 2
using C3 = Cfg<192, 128, 2, 2, 1, 64, 4, 10, K3_PRE>;
DEVI float fadd1(float a, float b) { float r; asm("v_add_f32 %0, %1, %2" : "=v"(r) : "v"(a), "v"(b)); return r; }
DEVI void facc1(float& s, float a) { asm("v_add_f32 %0, %0, %1" : "+v"(s) : "v"(a)); }
DEVI void fsq1(float& s, float a) { asm("v_fmac_f32 %0, %1, %1" : "+v"(s) : "v"(a)); }
DEVI void gload16(u32x4& d, unsigned voff, const char* sbase) { asm volatile("global_load_dwordx4 %0, %1, %2" : "=v"(d) : "v"(voff), "s"(sbase)); }
template <int WA> DEVI void k3_consumer(char* shm, f32x2* rowst, char* stg, const float* csum, int wid, int lane ) {
  using C = C3;
  const int fr = lane & 15, fq = lane >> 4, wb = wid & 1;
  f32x4 cs[6];
#pragma unroll
  for (int j = 0; j < 6; ++j) cs[j] = *(const f32x4*)(csum + (j & 1) * FF + WA * 48 + (j >> 1) * 16 + 4 * fq);
  f32x4 acc[6][4];
  f32x4 sh[2] = {{0, 0, 0, 0}, {0, 0, 0, 0}}, sq[2] = {{0, 0, 0, 0}, {0, 0, 0, 0}};
  bf16x8 ones = {0x3f80, 0x3f80, 0x3f80, 0x3f80, 0x3f80, 0x3f80, 0x3f80, 0x3f80};
  asm volatile("" : "+v"(ones));
  consumer<C>(acc, shm, wid, lane, [&](bf16x8 (&R)[10]) {
    sh[0] = MFMA(ones, R[6 + 2 * WA], sh[0]); sq[0] = MFMA(R[6 + 2 * WA], R[6 + 2 * WA], sq[0]);
    sh[1] = MFMA(ones, R[7 + 2 * WA], sh[1]); sq[1] = MFMA(R[7 + 2 * WA], R[7 + 2 * WA], sq[1]);
  } );
#pragma unroll
  for (int i = 0; i < 2; ++i) {
    const float d01 = (fr & 1) ? sq[i][1] : sq[i][0], d23 = (fr & 1) ? sq[i][3] : sq[i][2], d = (fr & 2) ? d23 : d01;
    const float mean = sh[i][0] * (1.0f / DM);
    const float var = fmaxf(d * (1.0f / DM) - mean * mean, 0.0f);
    if (fq == (fr >> 2)) rowst[(wb * 4 + 2 * WA + i) * 16 + fr] = f32x2{-mean, var + 1e-5f};
  }
  lg_bar();
#pragma unroll
  for (int n = 0; n < 4; ++n) {
    const int ml = (wb * 4 + n) * 16 + fr;
    const f32x2 st = rowst[ml];
    const float nmean = st[0], ve = st[1];
    const float rstd = __builtin_amdgcn_rsqf(ve);
    const float k1 = -2.3022081985f * rstd;
    const float k2 = k1 * 0.044715f * rstd * rstd;
    f32x4 G[3], U[3], z[3];
#pragma unroll
    for (int p = 0; p < 3; ++p) G[p] = cs[2 * p] * nmean + acc[2 * p][n];
#pragma unroll
    for (int p = 0; p < 3; ++p) z[p] = G[p] * G[p];
#pragma unroll
    for (int p = 0; p < 3; ++p) U[p] = cs[2 * p + 1] * nmean + acc[2 * p + 1][n];
#pragma unroll
    for (int p = 0; p < 3; ++p) z[p] = z[p] * k2 + k1;
#pragma unroll
    for (int p = 0; p < 3; ++p) z[p] = z[p] * G[p];
#pragma unroll
    for (int p = 0; p < 3; ++p) { z[p][0] = __builtin_amdgcn_exp2f(z[p][0]); z[p][1] = __builtin_amdgcn_exp2f(z[p][1]); z[p][2] = __builtin_amdgcn_exp2f(z[p][2]); z[p][3] = __builtin_amdgcn_exp2f(z[p][3]); U[p] = U[p] * G[p]; }
#pragma unroll
    for (int p = 0; p < 3; ++p) z[p] = z[p] * ve + ve;
#pragma unroll
    for (int p = 0; p < 3; ++p) { z[p][0] = __builtin_amdgcn_rcpf(z[p][0]); z[p][1] = __builtin_amdgcn_rcpf(z[p][1]); z[p][2] = __builtin_amdgcn_rcpf(z[p][2]); z[p][3] = __builtin_amdgcn_rcpf(z[p][3]); }
#pragma unroll
    for (int p = 0; p < 3; ++p) {
      f32x4 v = U[p] * z[p];
      u32x2 o; o.x = pack2(v[0], v[1]); o.y = pack2(v[2], v[3]);
      *(u32x2*)(stg + ml * 208 + (WA * 48 + p * 16 + fq * 4) * 2) = o;
    }
  }
}
__global__ __launch_bounds__(512, 2) void k3_ffn1(const u16* __restrict__ P, const u16* __restrict__ WguT, const float* __restrict__ csum,
                                                  u16* __restrict__ act) {
  using C = C3;
  extern __shared__ __attribute__((aligned(16))) char shm[];
  const int bid = blockIdx.x, xcd = bid & 7, idx = bid >> 3;
  const int tm = (xcd >> 2) * 8 + (idx >> 2), tn = (xcd & 3) * 4 + (idx & 3);
  const int wid = __builtin_amdgcn_readfirstlane(threadIdx.x >> 6), lane = threadIdx.x & 63;
  constexpr int FREE = ((C::LASTSLOT + 1) % C::NST) * C::STG;
  f32x2* rowst = (f32x2*)(shm + FREE);
  char* stg = shm + FREE + 4096;
  if (wid >= 4) {
    const int lw = wid - 4, j = lane >> 3;
    const char* wbp = (const char*)WguT + (size_t)tn * 96 * DM * 2;
    const char* p0 = (const char*)P + (size_t)(tm * 128 + lw * 32) * DM * 2;
    const char* p1 = p0 + (size_t)SEQ * DM * 2;
    const unsigned sw0 = (unsigned)(((lane & 7) ^ (lane >> 4)) * 16);
    const unsigned ldsb = (unsigned)(size_t)(__attribute__((address_space(3))) char*)shm;
    unsigned vw[6];
#pragma unroll
    for (int i = 0; i < 6; ++i) { const int p = lw * 6 + i;
      vw[i] = (unsigned)(j * DM * 2) + (sw0 ^ (unsigned)((p & 1) * 64)) + (unsigned)((((p >> 1) & 1) * FF + 16 * (p >> 2) + 8 * (p & 1)) * DM * 2); }
    const unsigned vh = (unsigned)(j * DM * 2 + (lane & 7) * 16);
    char* hw = shm + (24 + lw * 4) * 1024 + j * 128;
    u32x4 buf[3][8];
    auto vm1 = [&](auto S, auto Q) {
      constexpr int s = decltype(S)::value, q = decltype(Q)::value;
      if constexpr (q < 8) gload16(buf[s % 3][q], vh + (q >> 1) * 8 * DM * 2, ((q & 1) ? p1 : p0) + s * 128);
      else glds16(vw[q - 8], wbp + s * 128, ldsb + (s % C::NST) * C::STG + (lw * 6 + q - 8) * 1024);
    };
    static_for<-1, C::NK - 1>([&](auto K) {
      constexpr int k = decltype(K)::value;
      constexpr int c = k + 1;
      constexpr int s0 = C::issued(k - 1) + 1, nop = (C::issued(k) - C::issued(k - 1)) * 14;
      auto ops = [&](auto B, auto E) { static_for<decltype(B)::value, decltype(E)::value>([&](auto O) {
        constexpr int o = decltype(O)::value; vm1(IC(s0 + o / 14), IC(o % 14)); }); };
      if constexpr (k == -1) ops(IC(0), IC(nop));
      u32x4 (&b)[8] = buf[c % 3];
      asm volatile("s_waitcnt vmcnt(%8)" : "+v"(b[0]), "+v"(b[1]), "+v"(b[2]), "+v"(b[3]), "+v"(b[4]), "+v"(b[5]), "+v"(b[6]), "+v"(b[7])
                   : "i"(((k == -1 ? C::issued(k) : C::issued(k - 1)) - c) * 14 + 6) : "memory");
      static_for<0, 4>([&](auto I) {
        constexpr int i = decltype(I)::value;
        if constexpr (k >= 0) { ops(IC(nop * i / 4), IC(nop * (i + 1) / 4)); SB(); }
        const u32x4 u = b[2 * i], v = b[2 * i + 1];
        float hh[8];
        hh[0] = fadd1(bflo(u.x), bflo(v.x)); hh[1] = fadd1(bfhi(u.x), bfhi(v.x)); hh[2] = fadd1(bflo(u.y), bflo(v.y)); hh[3] = fadd1(bfhi(u.y), bfhi(v.y));
        hh[4] = fadd1(bflo(u.z), bflo(v.z)); hh[5] = fadd1(bfhi(u.z), bfhi(v.z)); hh[6] = fadd1(bflo(u.w), bflo(v.w)); hh[7] = fadd1(bfhi(u.w), bfhi(v.w));
        u32x4 o; o.x = pack2(hh[0], hh[1]); o.y = pack2(hh[2], hh[3]); o.z = pack2(hh[4], hh[5]); o.w = pack2(hh[6], hh[7]);
        *(u32x4*)(hw + (c % C::NST) * C::STG + i * 1024 + (sw0 ^ (unsigned)((i & 1) * 64))) = o;
        SB();
      });
      asm volatile("s_waitcnt vmcnt(%0) lgkmcnt(0)\n\ts_barrier" ::"i"((C::issued(k) - c) * 14) : "memory");
    });
    lg_bar();
  } else {
    if (wid < 2) k3_consumer<0>(shm, rowst, stg, csum + tn * 96, wid, lane );
    else k3_consumer<1>(shm, rowst, stg, csum + tn * 96, wid, lane );
  }
  lg_bar();
  u16* ob = act + (size_t)tm * 128 * FF + tn * 96;
#pragma unroll
  for (int i = 0; i < 3; ++i) {
    int c = threadIdx.x + i * 512, row = c / 12, ch = c % 12;
    *(u32x4*)(ob + (size_t)row * FF + ch * 8) = *(const u32x4*)(stg + row * 208 + ch * 16);
  }
}

#define K4_PRE 4
using C4 = Cfg<80, 64, 1, 2, 2, 128, 4, 12, K4_PRE>;
__global__ __launch_bounds__(512, 2) void k4_ffn2(const u16* __restrict__ P, const u16* __restrict__ act, const u16* __restrict__ WdT,
                                                  u16* __restrict__ out) {
  using C = C4;
  extern __shared__ __attribute__((aligned(16))) char shm[];
  const int bid = blockIdx.x, xcd = bid & 7, idx = bid >> 3;
  const int tm = (xcd >> 1) * 8 + (idx >> 2), td = (xcd & 1) * 4 + (idx & 3);
  const int wid = __builtin_amdgcn_readfirstlane(threadIdx.x >> 6), lane = threadIdx.x & 63;
  static_assert(C::LASTSLOT == C::NST - 1 && (C::NST - 1) * C::STG >= 2 * 64 * 336, "");
  u32x4 ra[2], rb[2];
  auto rload = [&]() {
#pragma unroll
    for (int i = 0; i < 2; ++i) { int c = threadIdx.x + i * 512; if (c > 639) c = 639;
      const u16* p = P + (size_t)(tm * 64 + c / 10) * DM + td * 80 + (c % 10) * 8;
      ra[i] = *(const u32x4*)p; rb[i] = *(const u32x4*)(p + (size_t)SEQ * DM); }
  };
  if (wid >= 4) {
    const int lw = wid - 4, j = lane >> 3;
    const char* wp = (const char*)WdT + (size_t)td * 80 * FF * 2;
    const char* ap = (const char*)act + (size_t)tm * 64 * FF * 2;
    auto sA = [&](int p) { return (unsigned)(p * 8 * FF * 2); };
    auto ptrs = [&](int k, const char*& pa, const char*& pb) { pa = wp + k * 256; pb = ap + k * 256; };
    loader<C, 5, 4>(shm, lw * 5, lw * 4, lane, sA, (unsigned)(j * FF * 2), sA, (unsigned)(j * FF * 2), ptrs, []() {});
    rload();
  } else {
    rload();
    f32x4 acc[5][2];
    consumer<C>(acc, shm, wid, lane, [](auto&) {} );
    const int fr = lane & 15, fq = lane >> 4, kg = wid >> 1, wb = wid & 1;
#pragma unroll
    for (int n = 0; n < 2; ++n)
#pragma unroll
      for (int j = 0; j < 5; ++j) *(f32x4*)(shm + kg * (64 * 336) + ((wb * 2 + n) * 16 + fr) * 336 + j * 64 + fq * 16) = acc[j][n];
  }
  lg_bar();
  u16* op = out + (size_t)tm * 64 * DM + td * 80;
#pragma unroll
  for (int i = 0; i < 2; ++i) {
    const int c = threadIdx.x + i * 512;
    if (i == 0 || c < 640) {
      const int row = c / 10, ch = c % 10;
      const char* p = shm + row * 336 + ch * 32;
      f32x4 a0 = *(const f32x4*)p + *(const f32x4*)(p + 64 * 336), a1 = *(const f32x4*)(p + 16) + *(const f32x4*)(p + 16 + 64 * 336);
      *(u32x4*)(op + (size_t)row * DM + ch * 8) = add8(a0, a1, addbf8(ra[i], rb[i]));
    }
  }
}

static void launch_stage(int which, void* const* d_in, void* d_out, void* d_ws, hipStream_t stream) {
  const u16* x = (const u16*)d_in[0];
  const u16* Cr = (const u16*)d_in[1];
  const u16* Ci = (const u16*)d_in[2];
  const u16* Wre = (const u16*)d_in[3];
  const u16* Wim = (const u16*)d_in[4];
  const u16* Wg = (const u16*)d_in[6];
  const u16* Wu = (const u16*)d_in[7];
  const u16* Wd = (const u16*)d_in[8];
  char* ws = (char*)d_ws;
  u16* abT = (u16*)(ws);
  u16* WguT = (u16*)(ws + (10 << 20));
  u16* WdT = (u16*)(ws + (14 << 20));
  u16* act = (u16*)(ws + (16 << 20));
  u16* P = (u16*)(ws + (28 << 20));
  float* csp = (float*)(ws + (25 << 20));
  float* csum = (float*)(ws + (26 << 20));
  u16* out = (u16*)d_out;
  if (which == 0) {
    (void)hipFuncSetAttribute((const void*)k1_hid, hipFuncAttributeMaxDynamicSharedMemorySize, C1::LDS + 1024);
    k1_hid<<<256, 512, C1::LDS + 1024, stream>>>(x, Wre, Wim, Cr, Ci, Wg, Wu, Wd, abT, WguT, WdT, csp);
  } else if (which == 1) {
    (void)hipFuncSetAttribute((const void*)k2_seq, hipFuncAttributeMaxDynamicSharedMemorySize, C2::LDS);
    k2_seq<<<256, 512, C2::LDS, stream>>>(x, Cr, Ci, abT, csp, P, csum);
  } else if (which == 2) {
    (void)hipFuncSetAttribute((const void*)k3_ffn1, hipFuncAttributeMaxDynamicSharedMemorySize, C3::LDS);
    k3_ffn1<<<256, 512, C3::LDS, stream>>>(P, WguT, csum, act);
  } else {
    (void)hipFuncSetAttribute((const void*)k4_ffn2, hipFuncAttributeMaxDynamicSharedMemorySize, C4::LDS);
    k4_ffn2<<<256, 512, C4::LDS, stream>>>(P, act, WdT, out);
  }
}

extern "C" void kernel_launch(void* const* d_in, const int* in_sizes, int n_in, void* d_out, int out_size, void* d_ws, size_t ws_size,
                              hipStream_t stream) {
  for (int i = 0; i < 4; ++i) launch_stage(i, d_in, d_out, d_ws, stream);
}
```

```cpp
#include <hip/hip_runtime.h>
#include <hip/hip_bf16.h>
#include <stdint.h>
#include <type_traits>

typedef unsigned short u16;
typedef __attribute__((ext_vector_type(8))) short bf16x8;
typedef __attribute__((ext_vector_type(4))) float f32x4;
typedef __attribute__((ext_vector_type(2))) float f32x2;
typedef __attribute__((ext_vector_type(2))) __bf16 bf16x2;
typedef __attribute__((ext_vector_type(2))) unsigned u32x2;
typedef __attribute__((ext_vector_type(4))) unsigned u32x4;

#define DEVI __device__ __forceinline__
#define IC(x) std::integral_constant<int, (x)>{}
#define SB() __builtin_amdgcn_sched_barrier(0)
#define MFMA(a, b, c) __builtin_amdgcn_mfma_f32_16x16x32_bf16(a, b, c, 0, 0, 0)

constexpr int SEQ = 2048, DM = 640, FF = 1536;

template <int B, int E, class F> DEVI void static_for(F&& f) {
  if constexpr (B < E) { f(IC(B)); static_for<B + 1, E>(f); }
}
DEVI void glds16(unsigned voff, const char* sbase, unsigned ldsaddr) {
  asm volatile("s_mov_b32 m0, %2\n\ts_nop 0\n\tglobal_load_lds_dwordx4 %0, %1" ::"v"(voff), "s"(sbase), "s"(ldsaddr));
}
template <int N> DEVI void waitvm_bar() { asm volatile("s_waitcnt vmcnt(%0)\n\ts_barrier" ::"i"(N) : "memory"); }
DEVI void bar() { asm volatile("s_barrier" ::: "memory"); }
DEVI void touch(unsigned& sink, unsigned voff, const void* sbase) { asm volatile("global_load_dword %0, %1, %2" : "+v"(sink) : "v"(voff), "s"(sbase)); }
DEVI void touch_done(unsigned& sink) { asm volatile("s_waitcnt vmcnt(0)" : "+v"(sink)::"memory"); }
DEVI void lg_bar() { asm volatile("s_waitcnt lgkmcnt(0)\n\ts_barrier" ::: "memory"); }
DEVI unsigned pack2(float a, float b) { f32x2 p = {a, b}; return __builtin_bit_cast(unsigned, __builtin_convertvector(p, bf16x2)); }
DEVI float bflo(unsigned v) { return __builtin_bit_cast(float, v << 16); }
DEVI float bfhi(unsigned v) { return __builtin_bit_cast(float, v & 0xffff0000u); }
template <int CTRL> DEVI float dppf(float v) {
  return __builtin_bit_cast(float, __builtin_amdgcn_update_dpp(0, __builtin_bit_cast(int, v), CTRL, 0xf, 0xf, true));
}
DEVI float red16(float v) { v += dppf<0xB1>(v); v += dppf<0x4E>(v); v += dppf<0x141>(v); v += dppf<0x140>(v); return v; }

template <int TA_, int TB_, int NWA_, int NWB_, int KS_, int BK_, int NST_, int NK_, int PRE_> struct Cfg {
  static constexpr int TA = TA_, TB = TB_, NWA = NWA_, NWB = NWB_, KS = KS_, BK = BK_, NST = NST_, NK = NK_;
  static constexpr int NSUB = BK / 64, PA = TA / 8, PB = TB / 8, PPS = PA + PB, STG = NSUB * PPS * 1024, SUBB = PPS * 1024;
  static constexpr int WAF = TA / 16 / NWA, WBF = TB / 16 / NWB, KH = (BK / 32) / KS;
  static constexpr int NF = WAF + WBF, NMF = WAF * WBF;
  static constexpr int LDS = NST * STG;
  static constexpr int LASTSLOT = (NK - 1) % NST;
  static constexpr int issued(int k) {
    if (k < -1) return -1;
    int a = 2 * k + 1 + PRE_, b = k - 1 + NST, c = NK - 1;
    if (k == -1) b = NST - 1;
    int m = a < b ? a : b; return m < c ? m : c;
  }
  static constexpr int steady_begin() { int k = 0; while (!(issued(k) == k - 1 + NST && issued(k - 1) == k - 2 + NST) && k < NK) ++k; return k; }
  static constexpr int K0 = steady_begin() < NK - NST + 1 ? steady_begin() : NK - NST + 1;
  static constexpr int K1 = NK - NST + 1 > K0 ? NK - NST + 1 : K0;
};

template <class C, int GA, int GB, int EXTRA = 0, class SA, class SB_, class Ptrs, class Tail>
DEVI void loader(char* shm, int a0, int b0, int lane, SA sA, unsigned vA, SB_ sB, unsigned vB, Ptrs ptrs, Tail tail) {
  constexpr int G = GA + GB;
  const unsigned sw0 = (unsigned)(((lane & 7) ^ (lane >> 4)) * 16);
  unsigned voff[G], lo[G];
  const unsigned ldsb = (unsigned)(size_t)(__attribute__((address_space(3))) char*)shm;
#pragma unroll
  for (int i = 0; i < GA; ++i) {
    int p = a0 + i, sub = p / C::PA, pr = p - sub * C::PA;
    voff[i] = vA + (sw0 ^ (unsigned)((pr & 1) * 64)) + (sA(pr) + (unsigned)sub * 128u);
    lo[i] = ldsb + sub * C::SUBB + pr * 1024;
  }
#pragma unroll
  for (int i = 0; i < GB; ++i) {
    int p = b0 + i, sub = p / C::PB, pr = p - sub * C::PB;
    voff[GA + i] = vB + (sw0 ^ (unsigned)((pr & 1) * 64)) + (sB(pr) + (unsigned)sub * 128u);
    lo[GA + i] = ldsb + sub * C::SUBB + (C::PA + pr) * 1024;
  }
  auto issue = [&](int s, unsigned slot) {
    const char *pa, *pb; ptrs(s, pa, pb);
#pragma unroll
    for (int i = 0; i < G; ++i) glds16(voff[i], i < GA ? pa : pb, lo[i] + slot);
  };
  static_for<-1, C::K0>([&](auto K) {
    constexpr int k = decltype(K)::value;
    static_for<C::issued(k - 1) + 1, C::issued(k) + 1>([&](auto S) { constexpr int s = decltype(S)::value; issue(s, (s % C::NST) * C::STG); });
    waitvm_bar<(C::issued(k) - (k + 1)) * G>();
  });
  unsigned slot = ((C::K0 - 1 + C::NST) % C::NST) * C::STG;
  for (int k = C::K0; k < C::K1; ++k) {
    issue(k - 1 + C::NST, slot);
    slot += C::STG; if (slot == C::NST * C::STG) slot = 0;
    waitvm_bar<(C::NST - 2) * G>();
  }
  static_assert(C::issued(C::K1 - 1) == C::NK - 1, "ring fully issued before the tail");
  tail();
  static_for<C::K1, C::NK - 1>([&](auto K) { constexpr int k = decltype(K)::value; waitvm_bar<(C::NK - 2 - k) * G + EXTRA>(); });
}

template <class C, class Extra> DEVI void consumer(f32x4 (&acc)[C::WAF][C::WBF], char* shm, int wid, int lane, Extra extra ) {
  constexpr int WAF = C::WAF, WBF = C::WBF, KH = C::KH, NF = C::NF, NMF = C::NMF, STG = C::STG, NST = C::NST, NK = C::NK;
  const int kg = wid / (C::NWA * C::NWB), wab = wid % (C::NWA * C::NWB), wa = wab / C::NWB, wb = wab % C::NWB;
#pragma unroll
  for (int j = 0; j < WAF; ++j)
#pragma unroll
    for (int b = 0; b < WBF; ++b) acc[j][b] = f32x4{0, 0, 0, 0};
  const int fr = lane & 15, fq = lane >> 4;
  const int c0 = fr * 128 + ((fq ^ (fr >> 1)) * 16);
  int rda[KH], rdb[KH];
#pragma unroll
  for (int h = 0; h < KH; ++h) {
    int ks = kg * KH + h, o = (c0 ^ ((ks & 1) * 64)) + (ks >> 1) * C::SUBB;
    rda[h] = o + wa * WAF * 2048; rdb[h] = o + C::TA * 128 + wb * WBF * 2048;
  }
  bf16x8 R[2][NF];
  auto rd1 = [&](bf16x8 (&Rn)[NF], unsigned slot, int h, int f) {
    if (f < WAF) Rn[f] = *(const bf16x8*)(shm + slot + rda[h] + f * 2048);
    else Rn[f] = *(const bf16x8*)(shm + slot + rdb[h] + (f - WAF) * 2048);
  };
  auto body = [&](bf16x8 (&Rc)[NF], bf16x8 (&Rn)[NF], unsigned slot, int h, bool dor) {
    int ri = 0;
#pragma unroll
    for (int m = 0; m < NMF; ++m) {
      { int n = m / WAF, j = m % WAF; acc[j][n] = MFMA(Rc[j], Rc[WAF + n], acc[j][n]); }
      int rt = ((m + 1) * NF * 4 + 3 * NMF - 1) / (3 * NMF); if (rt > NF) rt = NF;
#pragma unroll
      for (; ri < rt; ++ri) if (dor) rd1(Rn, slot, h, ri);
      SB();
    }
    extra(Rc);
    SB();
  };
  lg_bar();
#pragma unroll
  for (int f = 0; f < NF; ++f) rd1(R[0], 0, 0, f);
  unsigned slot = 0;
  auto stage = [&](auto P, bool last) {
    constexpr int par = decltype(P)::value;
    unsigned nslot = slot + STG; if (nslot == NST * STG) nslot = 0;
#pragma unroll
    for (int h = 0; h < KH; ++h) {
      const int c = (par + h) & 1;
      if (h == KH - 1) { if (!last) { lg_bar(); body(R[c], R[c ^ 1], nslot, 0, true); } else body(R[c], R[c ^ 1], 0, 0, false); }
      else body(R[c], R[c ^ 1], slot, h + 1, true);
    }
    slot = nslot;
  };
  if constexpr (KH % 2 == 0) {
    for (int k = 0; k < NK - 1; ++k) stage(IC(0), false);
    stage(IC(0), true);
  } else {
    static_assert(NK % 2 == 0, "");
    for (int k = 0; k < NK - 2; k += 2) { stage(IC(0), false); stage(IC(1), false); }
    stage(IC(0), false); stage(IC(1), true);
  }
}

struct TrUnit { u32x2 v[8]; };
DEVI void tr_load(TrUnit& t, const u16* W, int N, int k0, int n0, int lane) {
  const unsigned vo = (unsigned)(((8 * (lane >> 4)) * N + 4 * (lane & 15)) * 2);
  const char* b = (const char*)(W + (size_t)k0 * N + n0);
#pragma unroll
  for (int i = 0; i < 8; ++i) asm volatile("global_load_dwordx2 %0, %1, %2" : "=v"(t.v[i]) : "v"(vo), "s"(b + (size_t)i * N * 2));
}
DEVI void tr_wait(TrUnit& t) {
  asm volatile("s_waitcnt vmcnt(0)" : "+v"(t.v[0]), "+v"(t.v[1]), "+v"(t.v[2]), "+v"(t.v[3]), "+v"(t.v[4]), "+v"(t.v[5]), "+v"(t.v[6]), "+v"(t.v[7])::"memory");
}
DEVI void tr_store(const TrUnit& t, u16* WT, int K, int k0, int n0, int lane) {
  const int src = (((lane & 3) << 4) | (lane >> 2)) << 2;
  u16* p = WT + (size_t)(n0 + 4 * (lane >> 2)) * K + k0 + 8 * (lane & 3);
  u32x4 o;
#define BP(v_) (unsigned)__builtin_amdgcn_ds_bpermute(src, (int)(v_))
#define TRP(W_, SEL) o.x = BP(__builtin_amdgcn_perm(t.v[1].W_, t.v[0].W_, SEL)); o.y = BP(__builtin_amdgcn_perm(t.v[3].W_, t.v[2].W_, SEL)); \
                     o.z = BP(__builtin_amdgcn_perm(t.v[5].W_, t.v[4].W_, SEL)); o.w = BP(__builtin_amdgcn_perm(t.v[7].W_, t.v[6].W_, SEL));
  TRP(x, 0x05040100u) *(u32x4*)(p) = o;
  TRP(x, 0x07060302u) *(u32x4*)(p + K) = o;
  TRP(y, 0x05040100u) *(u32x4*)(p + 2 * K) = o;
  TRP(y, 0x07060302u) *(u32x4*)(p + 3 * K) = o;
#undef TRP
#undef BP
}
DEVI float fadd_(float a, float b) { float r; asm("v_add_f32 %0, %1, %2" : "=v"(r) : "v"(a), "v"(b)); return r; }
template <int H> DEVI void tr_colsum(const TrUnit& t, float (&c)[4]) {
#pragma unroll
  for (int i = 4 * H; i < 4 * H + 4; ++i) {
    c[0] = fadd_(c[0], bflo(t.v[i].x)); c[1] = fadd_(c[1], bfhi(t.v[i].x)); c[2] = fadd_(c[2], bflo(t.v[i].y)); c[3] = fadd_(c[3], bfhi(t.v[i].y)); }
  if constexpr (H == 1) {
#pragma unroll
    for (int i = 0; i < 4; ++i) { c[i] = fadd_(c[i], __shfl_xor(c[i], 16)); c[i] = fadd_(c[i], __shfl_xor(c[i], 32)); }
  }
}
struct TrJob { const u16* src; u16* dst; int lds, ldd, k0, n0, cso; bool ok, cs; };
DEVI TrJob tr_job(int u, const u16* Wg, const u16* Wu, const u16* Wd, u16* WguT, u16* WdT) {
  TrJob j; j.ok = u < 1440; if (!j.ok) u = 1439;
  const int w = u / 480, r = u - w * 480;
  j.cs = w < 2;
  if (w < 2) { const int kb = r / 24; j.src = w ? Wu : Wg; j.dst = WguT + (size_t)w * FF * DM; j.lds = FF; j.ldd = DM; j.k0 = kb * 32; j.n0 = (r - kb * 24) * 64; j.cso = kb * 2 * FF + w * FF + j.n0; }
  else { const int kb = r / 10; j.src = Wd; j.dst = WdT; j.lds = DM; j.ldd = FF; j.k0 = kb * 32; j.n0 = (r - kb * 10) * 64; j.cso = 0; }
  return j;
}

#define K1_NST 6
#define K1_PRE 3
using C1 = Cfg<128, 80, 4, 1, 1, 64, K1_NST, 10, K1_PRE>;
template <int W> DEVI void k1_consumer(char* shm, float* wsum, char* stg, int lane, float sgn ) {
  using C = C1;
  f32x4 acc[2][5];
  f32x4 sx[2] = {{0, 0, 0, 0}, {0, 0, 0, 0}}, sq[2] = {{0, 0, 0, 0}, {0, 0, 0, 0}}, sw = {0, 0, 0, 0}, sw4 = {0, 0, 0, 0};
  bf16x8 ones = {0x3f80, 0x3f80, 0x3f80, 0x3f80, 0x3f80, 0x3f80, 0x3f80, 0x3f80};
  asm volatile("" : "+v"(ones));
  consumer<C>(acc, shm, W, lane, [&](bf16x8 (&R)[7]) {
    sx[0] = MFMA(R[0], ones, sx[0]); sx[1] = MFMA(R[1], ones, sx[1]);
    sq[0] = MFMA(R[0], R[0], sq[0]); sq[1] = MFMA(R[1], R[1], sq[1]);
    sw = MFMA(ones, R[2 + W], sw);
    if constexpr (W == 0) sw4 = MFMA(ones, R[6], sw4);
  } );
  const int fr = lane & 15, fq = lane >> 4;
  if (lane < 16) { wsum[W * 16 + lane] = sw[0]; if constexpr (W == 0) wsum[64 + lane] = sw4[0]; }
  f32x4 rs[2], mr[2];
#pragma unroll
  for (int a = 0; a < 2; ++a)
#pragma unroll
    for (int r = 0; r < 4; ++r) {
      float s2 = __shfl(sq[a][r], 20 * fq + r);
      float mean = sx[a][r] * (1.0f / DM);
      float var = fmaxf(s2 * (1.0f / DM) - mean * mean, 0.0f);
      float rstd = sgn * rsqrtf(var + 1e-5f);
      rs[a][r] = rstd; mr[a][r] = mean * rstd;
    }
  lg_bar();
#pragma unroll
  for (int b = 0; b < 5; ++b) {
    float w = wsum[b * 16 + fr];
    f32x4 v0 = acc[0][b] * rs[0] - w * mr[0], v1 = acc[1][b] * rs[1] - w * mr[1];
    u32x4 o; o.x = pack2(v0[0], v0[1]); o.y = pack2(v0[2], v0[3]); o.z = pack2(v1[0], v1[1]); o.w = pack2(v1[2], v1[3]);
    *(u32x4*)(stg + (b * 16 + fr) * 272 + W * 64 + fq * 16) = o;
  }
}
#define CODE_PF 1
#define K1_PFC 1
extern "C" __global__ __launch_bounds__(512, 2) void k1_hid(const u16* __restrict__ x, const u16* __restrict__ Wre, const u16* __restrict__ Wim,
                                                 const u16* __restrict__ Cr, const u16* __restrict__ Ci, const u16* __restrict__ Wg,
                                                 const u16* __restrict__ Wu, const u16* __restrict__ Wd, u16* __restrict__ abT,
                                                 u16* __restrict__ WguT, u16* __restrict__ WdT, float* __restrict__ csp) {
  using C = C1;
  extern __shared__ __attribute__((aligned(16))) char shm[];
  const int bid = blockIdx.x, xcd = bid & 7, idx = bid >> 3;
  const int ts = (xcd >> 1) * 4 + (idx >> 3), te = (xcd & 1) * 8 + (idx & 7);
  const int wid = __builtin_amdgcn_readfirstlane(threadIdx.x >> 6), lane = threadIdx.x & 63;
  const int half = te >> 3;
  constexpr int FREE = ((C::LASTSLOT + 1) % C::NST) * C::STG;
  float* wsum = (float*)(shm + FREE);
  char* stg = shm + FREE + 1024;
  if (wid >= 4) {
    const int lw = wid - 4, j = lane >> 3;
    const char* Wb = (const char*)(half ? Wim : Wre) + (size_t)(te & 7) * 80 * DM * 2;
    const char* xb = (const char*)x + (size_t)ts * 128 * DM * 2;
    auto sA = [&](int p) { return (unsigned)((32 * (p >> 2) + 16 * (p & 1) + 4 * ((p >> 1) & 1)) * DM * 2); };
    auto sB = [&](int p) { return (unsigned)(p * 8 * DM * 2); };
    auto ptrs = [&](int k, const char*& pa, const char*& pb) { pa = xb + k * 128; pb = Wb + k * 128; };
    const unsigned vA = (unsigned)((8 * (j >> 2) + (j & 3)) * DM * 2), vB = (unsigned)(j * DM * 2);
    TrUnit tu[2];
    const TrJob j0 = tr_job(bid * 4 + lw, Wg, Wu, Wd, WguT, WdT), j1 = tr_job(1024 + bid * 4 + lw, Wg, Wu, Wd, WguT, WdT);
    auto tail = [&]() {
      tr_load(tu[0], j0.src, j0.lds, j0.k0, j0.n0, lane);
      tr_load(tu[1], j1.src, j1.lds, j1.k0, j1.n0, lane);
      const unsigned dummy = (unsigned)(size_t)(__attribute__((address_space(3))) char*)shm + C::LDS;
      const char* cp = (const char*)((bid & 1) ? Ci : Cr) + (size_t)(bid >> 1) * 65536 + lw * 16384;
#pragma unroll
      for (int i = 0; i < 16; ++i) glds16(lane * 16 + i * 1024, cp, dummy);
    };
    constexpr int EX = 16 + (K1_PFC ? 16 : 0);
    if (lw < 2) loader<C, 4, 3, EX>(shm, lw * 4, lw * 3, lane, sA, vA, sB, vB, ptrs, tail);
    else loader<C, 4, 2, EX>(shm, lw * 4, 2 + lw * 2, lane, sA, vA, sB, vB, ptrs, tail);
    tr_wait(tu[0]); tr_wait(tu[1]);
#pragma unroll
    for (int q = 0; q < 2; ++q) {
      const TrJob& jb = q ? j1 : j0;
      if (jb.ok) {
        tr_store(tu[q], jb.dst, jb.ldd, jb.k0, jb.n0, lane);
        if (jb.cs) {
          float c[4] = {0.f, 0.f, 0.f, 0.f}; tr_colsum<0>(tu[q], c); tr_colsum<1>(tu[q], c);
          if (lane < 16) *(f32x4*)(csp + jb.cso + 4 * lane) = f32x4{c[0], c[1], c[2], c[3]};
        }
      }
      if (q == 0) lg_bar();
    }
  } else {
    unsigned csink = 0;
    if (wid == 0) {
      unsigned long long t0, t1; int d0, d1;
      asm volatile("s_getpc_b64 %0\n\ts_mov_b32 %1, k1_hid@rel32@lo+4" : "=s"(t0), "=s"(d0));
      asm volatile("s_getpc_b64 %0\n\ts_mov_b32 %1, kz_end@rel32@lo+4" : "=s"(t1), "=s"(d1));
      t0 += (long long)d0; t1 += (long long)d1;
      const long long len = (long long)(t1 - t0);
      const unsigned off = (unsigned)((bid * 8 + (lane & 7)) * 64);
      if (lane < 8 && (long long)off < len) touch(csink, off, (const void*)t0);
    }
    const float sgn = half ? -1.0f : 1.0f;
    if (wid == 0) k1_consumer<0>(shm, wsum, stg, lane, sgn );
    else if (wid == 1) k1_consumer<1>(shm, wsum, stg, lane, sgn );
    else if (wid == 2) k1_consumer<2>(shm, wsum, stg, lane, sgn );
    else k1_consumer<3>(shm, wsum, stg, lane, sgn );
    touch_done(csink);
  }
  lg_bar();
  u16* ob = abT + (size_t)(te & 7) * 80 * 4096 + half * 2048 + ts * 128;
#pragma unroll
  for (int i = 0; i < 3; ++i) {
    int c = threadIdx.x + i * 512;
    if (i < 2 || c < 1280) {
      int row = c >> 4, ch = c & 15;
      *(u32x4*)(ob + (size_t)row * 4096 + ch * 8) = *(const u32x4*)(stg + row * 272 + ch * 16);
    }
  }
}

DEVI u32x4 add8(f32x4 a0, f32x4 a1, u32x4 r) {
  u32x4 o;
  o.x = pack2(a0[0] + bflo(r.x), a0[1] + bfhi(r.x)); o.y = pack2(a0[2] + bflo(r.y), a0[3] + bfhi(r.y));
  o.z = pack2(a1[0] + bflo(r.z), a1[1] + bfhi(r.z)); o.w = pack2(a1[2] + bflo(r.w), a1[3] + bfhi(r.w));
  return o;
}
DEVI u32x4 addbf8(u32x4 a, u32x4 b) {
  u32x4 o;
  o.x = pack2(bflo(a.x) + bflo(b.x), bfhi(a.x) + bfhi(b.x)); o.y = pack2(bflo(a.y) + bflo(b.y), bfhi(a.y) + bfhi(b.y));
  o.z = pack2(bflo(a.z) + bflo(b.z), bfhi(a.z) + bfhi(b.z)); o.w = pack2(bflo(a.w) + bflo(b.w), bfhi(a.w) + bfhi(b.w));
  return o;
}

#define K2_PRE 6
#define K2_NST 6
using C2 = Cfg<80, 128, 1, 2, 2, 64, K2_NST, 32, K2_PRE>;
extern "C" __global__ __launch_bounds__(512, 2) void k2_seq(const u16* __restrict__ x, const u16* __restrict__ Cr, const u16* __restrict__ Ci,
                                                 const u16* __restrict__ abT, const float* __restrict__ csp,
                                                 u16* __restrict__ P, float* __restrict__ csum) {
  using C = C2;
  extern __shared__ __attribute__((aligned(16))) char shm[];
  const int bid = blockIdx.x, xcd = bid & 7, idx = bid >> 3;
  const int half = xcd & 1, tt = (xcd >> 1) * 4 + (idx >> 3), te = idx & 7;
  const int wid = __builtin_amdgcn_readfirstlane(threadIdx.x >> 6), lane = threadIdx.x & 63;
  static_assert((C::NST - 1 - C::LASTSLOT) * C::STG >= 2 * 128 * 336, "");
  char* red = shm + (C::LASTSLOT + 1) * C::STG;
  u32x4 rx[3] = {{0, 0, 0, 0}, {0, 0, 0, 0}, {0, 0, 0, 0}};
  auto xload = [&]() {
    if (half == 0) {
#pragma unroll
      for (int i = 0; i < 3; ++i) { int c = threadIdx.x + i * 512; if (c > 1279) c = 1279;
        rx[i] = *(const u32x4*)(x + (size_t)(tt * 128 + c / 10) * DM + te * 80 + (c % 10) * 8); }
    }
  };
  if (wid >= 4) {
    const int lw = wid - 4, j = lane >> 3;
    const char* ab = (const char*)abT + (size_t)te * 80 * 8192 + half * 4096;
    const char* cc = (const char*)(half ? Ci : Cr) + (size_t)tt * 128 * 4096;
    auto sA = [&](int p) { return (unsigned)(p * 8 * 8192); };
    auto sB = [&](int p) { return (unsigned)(p * 8 * 4096); };
    auto ptrs = [&](int k, const char*& pa, const char*& pb) { pa = ab + k * 128; pb = cc + k * 128; };
    if (lw < 2) loader<C, 3, 4>(shm, lw * 3, lw * 4, lane, sA, (unsigned)(j * 8192), sB, (unsigned)(j * 4096), ptrs, []() {});
    else loader<C, 2, 4>(shm, 2 + lw * 2, lw * 4, lane, sA, (unsigned)(j * 8192), sB, (unsigned)(j * 4096), ptrs, []() {});
    xload();
  } else {
    if (wid == 0 && bid < 48) {
      const int col = bid * 64 + lane; float c = 0;
#pragma unroll
      for (int i = 0; i < 20; ++i) c += csp[(size_t)i * (2 * FF) + col];
      csum[col] = c;
    }
    xload();
    f32x4 acc[5][4];
    consumer<C>(acc, shm, wid, lane, [](auto&) {} );
    const int fr = lane & 15, fq = lane >> 4, kg = wid >> 1, wb = wid & 1;
#pragma unroll
    for (int n = 0; n < 4; ++n)
#pragma unroll
      for (int j = 0; j < 5; ++j) *(f32x4*)(red + kg * (128 * 336) + ((wb * 4 + n) * 16 + fr) * 336 + j * 64 + fq * 16) = acc[j][n];
  }
  lg_bar();
  u16* op = P + (size_t)half * SEQ * DM + (size_t)tt * 128 * DM + te * 80;
#pragma unroll
  for (int i = 0; i < 3; ++i) {
    const int c = threadIdx.x + i * 512;
    if (i < 2 || c < 1280) {
      const int row = c / 10, ch = c % 10;
      const char* p = red + row * 336 + ch * 32;
      f32x4 a0 = *(const f32x4*)p + *(const f32x4*)(p + 128 * 336), a1 = *(const f32x4*)(p + 16) + *(const f32x4*)(p + 16 + 128 * 336);
      *(u32x4*)(op + (size_t)row * DM + ch * 8) = add8(a0, a1, rx[i]);
    }
  }
}

#define K3_PRE 2
using C3 = Cfg<192, 128, 2, 2, 1, 64, 4, 10, K3_PRE>;
DEVI float fadd1(float a, float b) { float r; asm("v_add_f32 %0, %1, %2" : "=v"(r) : "v"(a), "v"(b)); return r; }
DEVI void facc1(float& s, float a) { asm("v_add_f32 %0, %0, %1" : "+v"(s) : "v"(a)); }
DEVI void fsq1(float& s, float a) { asm("v_fmac_f32 %0, %1, %1" : "+v"(s) : "v"(a)); }
DEVI void gload16(u32x4& d, unsigned voff, const char* sbase) { asm volatile("global_load_dwordx4 %0, %1, %2" : "=v"(d) : "v"(voff), "s"(sbase)); }
template <int WA> DEVI void k3_consumer(char* shm, f32x2* rowst, char* stg, const float* csum, int wid, int lane ) {
  using C = C3;
  const int fr = lane & 15, fq = lane >> 4, wb = wid & 1;
  f32x4 cs[6];
#pragma unroll
  for (int j = 0; j < 6; ++j) cs[j] = *(const f32x4*)(csum + (j & 1) * FF + WA * 48 + (j >> 1) * 16 + 4 * fq);
  f32x4 acc[6][4];
  f32x4 sh[2] = {{0, 0, 0, 0}, {0, 0, 0, 0}}, sq[2] = {{0, 0, 0, 0}, {0, 0, 0, 0}};
  bf16x8 ones = {0x3f80, 0x3f80, 0x3f80, 0x3f80, 0x3f80, 0x3f80, 0x3f80, 0x3f80};
  asm volatile("" : "+v"(ones));
  consumer<C>(acc, shm, wid, lane, [&](bf16x8 (&R)[10]) {
    sh[0] = MFMA(ones, R[6 + 2 * WA], sh[0]); sq[0] = MFMA(R[6 + 2 * WA], R[6 + 2 * WA], sq[0]);
    sh[1] = MFMA(ones, R[7 + 2 * WA], sh[1]); sq[1] = MFMA(R[7 + 2 * WA], R[7 + 2 * WA], sq[1]);
  } );
#pragma unroll
  for (int i = 0; i < 2; ++i) {
    const float d01 = (fr & 1) ? sq[i][1] : sq[i][0], d23 = (fr & 1) ? sq[i][3] : sq[i][2], d = (fr & 2) ? d23 : d01;
    const float mean = sh[i][0] * (1.0f / DM);
    const float var = fmaxf(d * (1.0f / DM) - mean * mean, 0.0f);
    if (fq == (fr >> 2)) rowst[(wb * 4 + 2 * WA + i) * 16 + fr] = f32x2{-mean, var + 1e-5f};
  }
  lg_bar();
#pragma unroll
  for (int n = 0; n < 4; ++n) {
    const int ml = (wb * 4 + n) * 16 + fr;
    const f32x2 st = rowst[ml];
    const float nmean = st[0], ve = st[1];
    const float rstd = __builtin_amdgcn_rsqf(ve);
    const float k1 = -2.3022081985f * rstd;
    const float k2 = k1 * 0.044715f * rstd * rstd;
    f32x4 G[3], U[3], z[3];
#pragma unroll
    for (int p = 0; p < 3; ++p) G[p] = cs[2 * p] * nmean + acc[2 * p][n];
#pragma unroll
    for (int p = 0; p < 3; ++p) z[p] = G[p] * G[p];
#pragma unroll
    for (int p = 0; p < 3; ++p) U[p] = cs[2 * p + 1] * nmean + acc[2 * p + 1][n];
#pragma unroll
    for (int p = 0; p < 3; ++p) z[p] = z[p] * k2 + k1;
#pragma unroll
    for (int p = 0; p < 3; ++p) z[p] = z[p] * G[p];
#pragma unroll
    for (int p = 0; p < 3; ++p) { z[p][0] = __builtin_amdgcn_exp2f(z[p][0]); z[p][1] = __builtin_amdgcn_exp2f(z[p][1]); z[p][2] = __builtin_amdgcn_exp2f(z[p][2]); z[p][3] = __builtin_amdgcn_exp2f(z[p][3]); U[p] = U[p] * G[p]; }
#pragma unroll
    for (int p = 0; p < 3; ++p) z[p] = z[p] * ve + ve;
#pragma unroll
    for (int p = 0; p < 3; ++p) { z[p][0] = __builtin_amdgcn_rcpf(z[p][0]); z[p][1] = __builtin_amdgcn_rcpf(z[p][1]); z[p][2] = __builtin_amdgcn_rcpf(z[p][2]); z[p][3] = __builtin_amdgcn_rcpf(z[p][3]); }
#pragma unroll
    for (int p = 0; p < 3; ++p) {
      f32x4 v = U[p] * z[p];
      u32x2 o; o.x = pack2(v[0], v[1]); o.y = pack2(v[2], v[3]);
      *(u32x2*)(stg + ml * 208 + (WA * 48 + p * 16 + fq * 4) * 2) = o;
    }
  }
}
extern "C" __global__ __launch_bounds__(512, 2) void k3_ffn1(const u16* __restrict__ P, const u16* __restrict__ WguT, const float* __restrict__ csum,
                                                  u16* __restrict__ act) {
  using C = C3;
  extern __shared__ __attribute__((aligned(16))) char shm[];
  const int bid = blockIdx.x, xcd = bid & 7, idx = bid >> 3;
  const int tm = (xcd >> 2) * 8 + (idx >> 2), tn = (xcd & 3) * 4 + (idx & 3);
  const int wid = __builtin_amdgcn_readfirstlane(threadIdx.x >> 6), lane = threadIdx.x & 63;
  constexpr int FREE = ((C::LASTSLOT + 1) % C::NST) * C::STG;
  f32x2* rowst = (f32x2*)(shm + FREE);
  char* stg = shm + FREE + 4096;
  if (wid >= 4) {
    const int lw = wid - 4, j = lane >> 3;
    const char* wbp = (const char*)WguT + (size_t)tn * 96 * DM * 2;
    const char* p0 = (const char*)P + (size_t)(tm * 128 + lw * 32) * DM * 2;
    const char* p1 = p0 + (size_t)SEQ * DM * 2;
    const unsigned sw0 = (unsigned)(((lane & 7) ^ (lane >> 4)) * 16);
    const unsigned ldsb = (unsigned)(size_t)(__attribute__((address_space(3))) char*)shm;
    unsigned vw[6];
#pragma unroll
    for (int i = 0; i < 6; ++i) { const int p = lw * 6 + i;
      vw[i] = (unsigned)(j * DM * 2) + (sw0 ^ (unsigned)((p & 1) * 64)) + (unsigned)((((p >> 1) & 1) * FF + 16 * (p >> 2) + 8 * (p & 1)) * DM * 2); }
    const unsigned vh = (unsigned)(j * DM * 2 + (lane & 7) * 16);
    char* hw = shm + (24 + lw * 4) * 1024 + j * 128;
    u32x4 buf[3][8];
    auto vm1 = [&](auto S, auto Q) {
      constexpr int s = decltype(S)::value, q = decltype(Q)::value;
      if constexpr (q < 8) gload16(buf[s % 3][q], vh + (q >> 1) * 8 * DM * 2, ((q & 1) ? p1 : p0) + s * 128);
      else glds16(vw[q - 8], wbp + s * 128, ldsb + (s % C::NST) * C::STG + (lw * 6 + q - 8) * 1024);
    };
    static_for<-1, C::NK - 1>([&](auto K) {
      constexpr int k = decltype(K)::value;
      constexpr int c = k + 1;
      constexpr int s0 = C::issued(k - 1) + 1, nop = (C::issued(k) - C::issued(k - 1)) * 14;
      auto ops = [&](auto B, auto E) { static_for<decltype(B)::value, decltype(E)::value>([&](auto O) {
        constexpr int o = decltype(O)::value; vm1(IC(s0 + o / 14), IC(o % 14)); }); };
      if constexpr (k == -1) ops(IC(0), IC(nop));
      u32x4 (&b)[8] = buf[c % 3];
      asm volatile("s_waitcnt vmcnt(%8)" : "+v"(b[0]), "+v"(b[1]), "+v"(b[2]), "+v"(b[3]), "+v"(b[4]), "+v"(b[5]), "+v"(b[6]), "+v"(b[7])
                   : "i"(((k == -1 ? C::issued(k) : C::issued(k - 1)) - c) * 14 + 6) : "memory");
      static_for<0, 4>([&](auto I) {
        constexpr int i = decltype(I)::value;
        if constexpr (k >= 0) { ops(IC(nop * i / 4), IC(nop * (i + 1) / 4)); SB(); }
        const u32x4 u = b[2 * i], v = b[2 * i + 1];
        float hh[8];
        hh[0] = fadd1(bflo(u.x), bflo(v.x)); hh[1] = fadd1(bfhi(u.x), bfhi(v.x)); hh[2] = fadd1(bflo(u.y), bflo(v.y)); hh[3] = fadd1(bfhi(u.y), bfhi(v.y));
        hh[4] = fadd1(bflo(u.z), bflo(v.z)); hh[5] = fadd1(bfhi(u.z), bfhi(v.z)); hh[6] = fadd1(bflo(u.w), bflo(v.w)); hh[7] = fadd1(bfhi(u.w), bfhi(v.w));
        u32x4 o; o.x = pack2(hh[0], hh[1]); o.y = pack2(hh[2], hh[3]); o.z = pack2(hh[4], hh[5]); o.w = pack2(hh[6], hh[7]);
        *(u32x4*)(hw + (c % C::NST) * C::STG + i * 1024 + (sw0 ^ (unsigned)((i & 1) * 64))) = o;
        SB();
      });
      asm volatile("s_waitcnt vmcnt(%0) lgkmcnt(0)\n\ts_barrier" ::"i"((C::issued(k) - c) * 14) : "memory");
    });
    lg_bar();
  } else {
    if (wid < 2) k3_consumer<0>(shm, rowst, stg, csum + tn * 96, wid, lane );
    else k3_consumer<1>(shm, rowst, stg, csum + tn * 96, wid, lane );
  }
  lg_bar();
  u16* ob = act + (size_t)tm * 128 * FF + tn * 96;
#pragma unroll
  for (int i = 0; i < 3; ++i) {
    int c = threadIdx.x + i * 512, row = c / 12, ch = c % 12;
    *(u32x4*)(ob + (size_t)row * FF + ch * 8) = *(const u32x4*)(stg + row * 208 + ch * 16);
  }
}

#define K4_PRE 4
using C4 = Cfg<80, 64, 1, 2, 2, 128, 4, 12, K4_PRE>;
extern "C" __global__ __launch_bounds__(512, 2) void k4_ffn2(const u16* __restrict__ P, const u16* __restrict__ act, const u16* __restrict__ WdT,
                                                  u16* __restrict__ out) {
  using C = C4;
  extern __shared__ __attribute__((aligned(16))) char shm[];
  const int bid = blockIdx.x, xcd = bid & 7, idx = bid >> 3;
  const int tm = (xcd >> 1) * 8 + (idx >> 2), td = (xcd & 1) * 4 + (idx & 3);
  const int wid = __builtin_amdgcn_readfirstlane(threadIdx.x >> 6), lane = threadIdx.x & 63;
  static_assert(C::LASTSLOT == C::NST - 1 && (C::NST - 1) * C::STG >= 2 * 64 * 336, "");
  u32x4 ra[2], rb[2];
  auto rload = [&]() {
#pragma unroll
    for (int i = 0; i < 2; ++i) { int c = threadIdx.x + i * 512; if (c > 639) c = 639;
      const u16* p = P + (size_t)(tm * 64 + c / 10) * DM + td * 80 + (c % 10) * 8;
      ra[i] = *(const u32x4*)p; rb[i] = *(const u32x4*)(p + (size_t)SEQ * DM); }
  };
  if (wid >= 4) {
    const int lw = wid - 4, j = lane >> 3;
    const char* wp = (const char*)WdT + (size_t)td * 80 * FF * 2;
    const char* ap = (const char*)act + (size_t)tm * 64 * FF * 2;
    auto sA = [&](int p) { return (unsigned)(p * 8 * FF * 2); };
    auto ptrs = [&](int k, const char*& pa, const char*& pb) { pa = wp + k * 256; pb = ap + k * 256; };
    loader<C, 5, 4>(shm, lw * 5, lw * 4, lane, sA, (unsigned)(j * FF * 2), sA, (unsigned)(j * FF * 2), ptrs, []() {});
    rload();
  } else {
    rload();
    f32x4 acc[5][2];
    consumer<C>(acc, shm, wid, lane, [](auto&) {} );
    const int fr = lane & 15, fq = lane >> 4, kg = wid >> 1, wb = wid & 1;
#pragma unroll
    for (int n = 0; n < 2; ++n)
#pragma unroll
      for (int j = 0; j < 5; ++j) *(f32x4*)(shm + kg * (64 * 336) + ((wb * 2 + n) * 16 + fr) * 336 + j * 64 + fq * 16) = acc[j][n];
  }
  lg_bar();
  u16* op = out + (size_t)tm * 64 * DM + td * 80;
#pragma unroll
  for (int i = 0; i < 2; ++i) {
    const int c = threadIdx.x + i * 512;
    if (i == 0 || c < 640) {
      const int row = c / 10, ch = c % 10;
      const char* p = shm + row * 336 + ch * 32;
      f32x4 a0 = *(const f32x4*)p + *(const f32x4*)(p + 64 * 336), a1 = *(const f32x4*)(p + 16) + *(const f32x4*)(p + 16 + 64 * 336);
      *(u32x4*)(op + (size_t)row * DM + ch * 8) = add8(a0, a1, addbf8(ra[i], rb[i]));
    }
  }
}

extern "C" __global__ void kz_end() {}

static void launch_stage(int which, void* const* d_in, void* d_out, void* d_ws, hipStream_t stream) {
  const u16* x = (const u16*)d_in[0];
  const u16* Cr = (const u16*)d_in[1];
  const u16* Ci = (const u16*)d_in[2];
  const u16* Wre = (const u16*)d_in[3];
  const u16* Wim = (const u16*)d_in[4];
  const u16* Wg = (const u16*)d_in[6];
  const u16* Wu = (const u16*)d_in[7];
  const u16* Wd = (const u16*)d_in[8];
  char* ws = (char*)d_ws;
  u16* abT = (u16*)(ws);
  u16* WguT = (u16*)(ws + (10 << 20));
  u16* WdT = (u16*)(ws + (14 << 20));
  u16* act = (u16*)(ws + (16 << 20));
  u16* P = (u16*)(ws + (28 << 20));
  float* csp = (float*)(ws + (25 << 20));
  float* csum = (float*)(ws + (26 << 20));
  u16* out = (u16*)d_out;
  if (which == 0) {
    (void)hipFuncSetAttribute((const void*)k1_hid, hipFuncAttributeMaxDynamicSharedMemorySize, C1::LDS + 1024);
    k1_hid<<<256, 512, C1::LDS + 1024, stream>>>(x, Wre, Wim, Cr, Ci, Wg, Wu, Wd, abT, WguT, WdT, csp);
  } else if (which == 1) {
    (void)hipFuncSetAttribute((const void*)k2_seq, hipFuncAttributeMaxDynamicSharedMemorySize, C2::LDS);
    k2_seq<<<256, 512, C2::LDS, stream>>>(x, Cr, Ci, abT, csp, P, csum);
  } else if (which == 2) {
    (void)hipFuncSetAttribute((const void*)k3_ffn1, hipFuncAttributeMaxDynamicSharedMemorySize, C3::LDS);
    k3_ffn1<<<256, 512, C3::LDS, stream>>>(P, WguT, csum, act);
  } else {
    (void)hipFuncSetAttribute((const void*)k4_ffn2, hipFuncAttributeMaxDynamicSharedMemorySize, C4::LDS);
    k4_ffn2<<<256, 512, C4::LDS, stream>>>(P, act, WdT, out);
  }
}

extern "C" void kernel_launch(void* const* d_in, const int* in_sizes, int n_in, void* d_out, int out_size, void* d_ws, size_t ws_size,
                              hipStream_t stream) {
  for (int i = 0; i < 4; ++i) launch_stage(i, d_in, d_out, d_ws, stream);
}
```

```cpp
#include <hip/hip_runtime.h>
#include <hip/hip_bf16.h>
#include <stdint.h>
#include <type_traits>

typedef unsigned short u16;
typedef __attribute__((ext_vector_type(8))) short bf16x8;
typedef __attribute__((ext_vector_type(4))) float f32x4;
typedef __attribute__((ext_vector_type(2))) float f32x2;
typedef __attribute__((ext_vector_type(2))) __bf16 bf16x2;
typedef __attribute__((ext_vector_type(2))) unsigned u32x2;
typedef __attribute__((ext_vector_type(4))) unsigned u32x4;

#define DEVI __device__ __forceinline__
#define IC(x) std::integral_constant<int, (x)>{}
#define SB() __builtin_amdgcn_sched_barrier(0)
#define MFMA(a, b, c) __builtin_amdgcn_mfma_f32_16x16x32_bf16(a, b, c, 0, 0, 0)

constexpr int SEQ = 2048, DM = 640, FF = 1536;

template <int B, int E, class F> DEVI void static_for(F&& f) {
  if constexpr (B < E) { f(IC(B)); static_for<B + 1, E>(f); }
}
DEVI void glds16(unsigned voff, const char* sbase, unsigned ldsaddr) {
  asm volatile("s_mov_b32 m0, %2\n\ts_nop 0\n\tglobal_load_lds_dwordx4 %0, %1" ::"v"(voff), "s"(sbase), "s"(ldsaddr));
}
template <int N> DEVI void waitvm_bar() { asm volatile("s_waitcnt vmcnt(%0)\n\ts_barrier" ::"i"(N) : "memory"); }
DEVI void bar() { asm volatile("s_barrier" ::: "memory"); }
DEVI void touch(unsigned& sink, unsigned voff, const void* sbase) { asm volatile("global_load_dword %0, %1, %2" : "+v"(sink) : "v"(voff), "s"(sbase)); }
DEVI void touch_done(unsigned& sink) { asm volatile("s_waitcnt vmcnt(0)" : "+v"(sink)::"memory"); }
DEVI void lg_bar() { asm volatile("s_waitcnt lgkmcnt(0)\n\ts_barrier" ::: "memory"); }
DEVI unsigned pack2(float a, float b) { f32x2 p = {a, b}; return __builtin_bit_cast(unsigned, __builtin_convertvector(p, bf16x2)); }
DEVI float bflo(unsigned v) { return __builtin_bit_cast(float, v << 16); }
DEVI float bfhi(unsigned v) { return __builtin_bit_cast(float, v & 0xffff0000u); }
template <int CTRL> DEVI float dppf(float v) {
  return __builtin_bit_cast(float, __builtin_amdgcn_update_dpp(0, __builtin_bit_cast(int, v), CTRL, 0xf, 0xf, true));
}
DEVI float red16(float v) { v += dppf<0xB1>(v); v += dppf<0x4E>(v); v += dppf<0x141>(v); v += dppf<0x140>(v); return v; }

template <int TA_, int TB_, int NWA_, int NWB_, int KS_, int BK_, int NST_, int NK_, int PRE_> struct Cfg {
  static constexpr int TA = TA_, TB = TB_, NWA = NWA_, NWB = NWB_, KS = KS_, BK = BK_, NST = NST_, NK = NK_;
  static constexpr int NSUB = BK / 64, PA = TA / 8, PB = TB / 8, PPS = PA + PB, STG = NSUB * PPS * 1024, SUBB = PPS * 1024;
  static constexpr int WAF = TA / 16 / NWA, WBF = TB / 16 / NWB, KH = (BK / 32) / KS;
  static constexpr int NF = WAF + WBF, NMF = WAF * WBF;
  static constexpr int LDS = NST * STG;
  static constexpr int LASTSLOT = (NK - 1) % NST;
  static constexpr int issued(int k) {
    if (k < -1) return -1;
    int a = 2 * k + 1 + PRE_, b = k - 1 + NST, c = NK - 1;
    if (k == -1) b = NST - 1;
    int m = a < b ? a : b; return m < c ? m : c;
  }
  static constexpr int steady_begin() { int k = 0; while (!(issued(k) == k - 1 + NST && issued(k - 1) == k - 2 + NST) && k < NK) ++k; return k; }
  static constexpr int K0 = steady_begin() < NK - NST + 1 ? steady_begin() : NK - NST + 1;
  static constexpr int K1 = NK - NST + 1 > K0 ? NK - NST + 1 : K0;
};

template <class C, int A0, int GA, int B0, int GB, class SA, class SB_, class Ptrs>
DEVI void loader(int lane, SA sA, unsigned vA, SB_ sB, unsigned vB, Ptrs ptrs) {
  constexpr int G = GA + GB;
  const unsigned sw0 = (unsigned)(((lane & 7) ^ (lane >> 4)) * 16);
  const unsigned a_e = vA + sw0, a_o = vA + (sw0 ^ 64u), b_e = vB + sw0, b_o = vB + (sw0 ^ 64u);
  unsigned voff[G];
  static_for<0, G>([&](auto I) {
    constexpr int i = decltype(I)::value;
    if constexpr (i < GA) { constexpr int p = A0 + i, sub = p / C::PA, pr = p % C::PA; voff[i] = ((pr & 1) ? a_o : a_e) + (sA(pr) + sub * 128u); }
    else { constexpr int p = B0 + i - GA, sub = p / C::PB, pr = p % C::PB; voff[i] = ((pr & 1) ? b_o : b_e) + (sB(pr) + sub * 128u); }
  });
  auto issue = [&](int s, unsigned slot) {
    const char *pa, *pb; ptrs(s, pa, pb);
    static_for<0, G>([&](auto I) {
      constexpr int i = decltype(I)::value;
      constexpr int p = i < GA ? A0 + i : B0 + i - GA, np = i < GA ? C::PA : C::PB;
      constexpr unsigned lo = (p / np) * C::SUBB + ((i < GA ? 0 : C::PA) + p % np) * 1024;
      glds16(voff[i], i < GA ? pa : pb, lo + slot);
    });
  };
  auto irregular = [&](auto K) {
    constexpr int k = decltype(K)::value;
    static_for<C::issued(k - 1) + 1, C::issued(k) + 1>([&](auto S) { constexpr int s = decltype(S)::value; issue(s, (s % C::NST) * C::STG); });
    waitvm_bar<(C::issued(k) - (k + 1)) * G>();
  };
  static_for<-1, C::K0>(irregular);
  unsigned slot = ((C::K0 - 1 + C::NST) % C::NST) * C::STG;
  for (int k = C::K0; k < C::K1; ++k) {
    issue(k - 1 + C::NST, slot);
    slot += C::STG; if (slot == C::NST * C::STG) slot = 0;
    waitvm_bar<(C::NST - 2) * G>();
  }
  static_for<C::K1, C::NK - 1>(irregular);
}

template <class C, class Extra> DEVI void consumer(f32x4 (&acc)[C::WAF][C::WBF], char* shm, int wid, int lane, Extra extra ) {
  constexpr int WAF = C::WAF, WBF = C::WBF, KH = C::KH, NF = C::NF, NMF = C::NMF, STG = C::STG, NST = C::NST, NK = C::NK;
  const int kg = wid / (C::NWA * C::NWB), wab = wid % (C::NWA * C::NWB), wa = wab / C::NWB, wb = wab % C::NWB;
#pragma unroll
  for (int j = 0; j < WAF; ++j)
#pragma unroll
    for (int b = 0; b < WBF; ++b) acc[j][b] = f32x4{0, 0, 0, 0};
  const int fr = lane & 15, fq = lane >> 4;
  const int c0 = fr * 128 + ((fq ^ (fr >> 1)) * 16);
  int rda[KH], rdb[KH];
#pragma unroll
  for (int h = 0; h < KH; ++h) {
    int ks = kg * KH + h, o = (c0 ^ ((ks & 1) * 64)) + (ks >> 1) * C::SUBB;
    rda[h] = o + wa * WAF * 2048; rdb[h] = o + C::TA * 128 + wb * WBF * 2048;
  }
  bf16x8 R[2][NF];
  auto rd1 = [&](bf16x8 (&Rn)[NF], unsigned slot, int h, int f) {
    if (f < WAF) Rn[f] = *(const bf16x8*)(shm + slot + rda[h] + f * 2048);
    else Rn[f] = *(const bf16x8*)(shm + slot + rdb[h] + (f - WAF) * 2048);
  };
  auto body = [&](bf16x8 (&Rc)[NF], bf16x8 (&Rn)[NF], unsigned slot, int h, bool dor) {
    int ri = 0;
#pragma unroll
    for (int m = 0; m < NMF; ++m) {
      { int n = m / WAF, j = m % WAF; acc[j][n] = MFMA(Rc[j], Rc[WAF + n], acc[j][n]); }
      int rt = ((m + 1) * NF * 4 + 3 * NMF - 1) / (3 * NMF); if (rt > NF) rt = NF;
#pragma unroll
      for (; ri < rt; ++ri) if (dor) rd1(Rn, slot, h, ri);
      SB();
    }
    extra(Rc);
    SB();
  };
  lg_bar();
#pragma unroll
  for (int f = 0; f < NF; ++f) rd1(R[0], 0, 0, f);
  unsigned slot = 0;
  auto stage = [&](auto P, bool last) {
    constexpr int par = decltype(P)::value;
    unsigned nslot = slot + STG; if (nslot == NST * STG) nslot = 0;
#pragma unroll
    for (int h = 0; h < KH; ++h) {
      const int c = (par + h) & 1;
      if (h == KH - 1) { if (!last) { lg_bar(); body(R[c], R[c ^ 1], nslot, 0, true); } else body(R[c], R[c ^ 1], 0, 0, false); }
      else body(R[c], R[c ^ 1], slot, h + 1, true);
    }
    slot = nslot;
  };
  if constexpr (KH % 2 == 0) {
    for (int k = 0; k < NK - 1; ++k) stage(IC(0), false);
    stage(IC(0), true);
  } else {
    static_assert(NK % 2 == 0, "");
    for (int k = 0; k < NK - 2; k += 2) { stage(IC(0), false); stage(IC(1), false); }
    stage(IC(0), false); stage(IC(1), true);
  }
}

struct TrUnit { u32x2 v[8]; };
DEVI void tr_load(TrUnit& t, const u16* W, int N, int k0, int n0, int lane) {
  const unsigned vo = (unsigned)(((8 * (lane >> 4)) * N + 4 * (lane & 15)) * 2);
  const char* b = (const char*)(W + (size_t)k0 * N + n0);
#pragma unroll
  for (int i = 0; i < 8; ++i) asm volatile("global_load_dwordx2 %0, %1, %2" : "=v"(t.v[i]) : "v"(vo), "s"(b + (size_t)i * N * 2));
}
DEVI void tr_wait(TrUnit& t) {
  asm volatile("s_waitcnt vmcnt(0)" : "+v"(t.v[0]), "+v"(t.v[1]), "+v"(t.v[2]), "+v"(t.v[3]), "+v"(t.v[4]), "+v"(t.v[5]), "+v"(t.v[6]), "+v"(t.v[7])::"memory");
}
DEVI void tr_store(const TrUnit& t, u16* WT, int K, int k0, int n0, int lane) {
  const int src = (((lane & 3) << 4) | (lane >> 2)) << 2;
  u16* p = WT + (size_t)(n0 + 4 * (lane >> 2)) * K + k0 + 8 * (lane & 3);
  u32x4 o;
#define BP(v_) (unsigned)__builtin_amdgcn_ds_bpermute(src, (int)(v_))
#define TRP(W_, SEL) o.x = BP(__builtin_amdgcn_perm(t.v[1].W_, t.v[0].W_, SEL)); o.y = BP(__builtin_amdgcn_perm(t.v[3].W_, t.v[2].W_, SEL)); \
                     o.z = BP(__builtin_amdgcn_perm(t.v[5].W_, t.v[4].W_, SEL)); o.w = BP(__builtin_amdgcn_perm(t.v[7].W_, t.v[6].W_, SEL));
  TRP(x, 0x05040100u) *(u32x4*)(p) = o;
  TRP(x, 0x07060302u) *(u32x4*)(p + K) = o;
  TRP(y, 0x05040100u) *(u32x4*)(p + 2 * K) = o;
  TRP(y, 0x07060302u) *(u32x4*)(p + 3 * K) = o;
#undef TRP
#undef BP
}
DEVI float fadd_(float a, float b) { float r; asm("v_add_f32 %0, %1, %2" : "=v"(r) : "v"(a), "v"(b)); return r; }
template <int H> DEVI void tr_colsum(const TrUnit& t, float (&c)[4]) {
#pragma unroll
  for (int i = 4 * H; i < 4 * H + 4; ++i) {
    c[0] = fadd_(c[0], bflo(t.v[i].x)); c[1] = fadd_(c[1], bfhi(t.v[i].x)); c[2] = fadd_(c[2], bflo(t.v[i].y)); c[3] = fadd_(c[3], bfhi(t.v[i].y)); }
  if constexpr (H == 1) {
#pragma unroll
    for (int i = 0; i < 4; ++i) { c[i] = fadd_(c[i], __shfl_xor(c[i], 16)); c[i] = fadd_(c[i], __shfl_xor(c[i], 32)); }
  }
}
struct TrJob { const u16* src; u16* dst; int lds, ldd, k0, n0, cso; bool ok, cs; };
DEVI TrJob tr_job(int u, const u16* Wg, const u16* Wu, const u16* Wd, u16* WguT, u16* WdT) {
  TrJob j; j.ok = u < 1440; if (!j.ok) u = 1439;
  const int w = u / 480, r = u - w * 480;
  j.cs = w < 2;
  if (w < 2) { const int kb = r / 24; j.src = w ? Wu : Wg; j.dst = WguT + (size_t)w * FF * DM; j.lds = FF; j.ldd = DM; j.k0 = kb * 32; j.n0 = (r - kb * 24) * 64; j.cso = kb * 2 * FF + w * FF + j.n0; }
  else { const int kb = r / 10; j.src = Wd; j.dst = WdT; j.lds = DM; j.ldd = FF; j.k0 = kb * 32; j.n0 = (r - kb * 10) * 64; j.cso = 0; }
  return j;
}

#define K1_PRE 3
using C1 = Cfg<128, 128, 4, 1, 1, 64, 5, 10, K1_PRE>;
template <int W> DEVI void k1_consumer(char* shm, float* wsum, char* stg, int lane, float sgn ) {
  using C = C1;
  f32x4 acc[2][8];
  f32x4 sx[2] = {{0, 0, 0, 0}, {0, 0, 0, 0}}, sq[2] = {{0, 0, 0, 0}, {0, 0, 0, 0}}, sw[2] = {{0, 0, 0, 0}, {0, 0, 0, 0}};
  bf16x8 ones = {0x3f80, 0x3f80, 0x3f80, 0x3f80, 0x3f80, 0x3f80, 0x3f80, 0x3f80};
  asm volatile("" : "+v"(ones));
  consumer<C>(acc, shm, W, lane, [&](bf16x8 (&R)[10]) {
    sx[0] = MFMA(R[0], ones, sx[0]); sx[1] = MFMA(R[1], ones, sx[1]);
    sq[0] = MFMA(R[0], R[0], sq[0]); sq[1] = MFMA(R[1], R[1], sq[1]);
    sw[0] = MFMA(ones, R[2 + 2 * W], sw[0]); sw[1] = MFMA(ones, R[3 + 2 * W], sw[1]);
  } );
  const int fr = lane & 15, fq = lane >> 4;
  if (lane < 16) { wsum[W * 32 + lane] = sw[0][0]; wsum[W * 32 + 16 + lane] = sw[1][0]; }
  f32x4 rs[2], mr[2];
#pragma unroll
  for (int a = 0; a < 2; ++a)
#pragma unroll
    for (int r = 0; r < 4; ++r) {
      float s2 = __shfl(sq[a][r], 20 * fq + r);
      float mean = sx[a][r] * (1.0f / DM);
      float var = fmaxf(s2 * (1.0f / DM) - mean * mean, 0.0f);
      float rstd = sgn * __builtin_amdgcn_rsqf(var + 1e-5f);
      rs[a][r] = rstd; mr[a][r] = mean * rstd;
    }
  lg_bar();
#pragma unroll
  for (int b = 0; b < 8; ++b) {
    float w = wsum[b * 16 + fr];
    f32x4 v0 = acc[0][b] * rs[0] - w * mr[0], v1 = acc[1][b] * rs[1] - w * mr[1];
    u32x4 o; o.x = pack2(v0[0], v0[1]); o.y = pack2(v0[2], v0[3]); o.z = pack2(v1[0], v1[1]); o.w = pack2(v1[2], v1[3]);
    *(u32x4*)(stg + (b * 16 + fr) * 272 + W * 64 + fq * 16) = o;
  }
}
__global__ __launch_bounds__(512, 2) void k1_hid(const u16* __restrict__ x, const u16* __restrict__ Wre, const u16* __restrict__ Wim,
                                                 const u16* __restrict__ Cr, const u16* __restrict__ Ci, const u16* __restrict__ Wg,
                                                 const u16* __restrict__ Wu, const u16* __restrict__ Wd, u16* __restrict__ abT,
                                                 u16* __restrict__ WguT, u16* __restrict__ WdT, float* __restrict__ csp) {
  using C = C1;
  extern __shared__ __attribute__((aligned(16))) char shm[];
  const int bid = blockIdx.x, xcd = bid & 7, idx = bid >> 3;
  const int wid = __builtin_amdgcn_readfirstlane(threadIdx.x >> 6), lane = threadIdx.x & 63;
  const int ts = (xcd >> 1) * 4 + idx / 5, tl = idx % 5, half = xcd & 1;
  const char* Wb = (const char*)(half ? Wim : Wre) + (size_t)tl * 128 * DM * 2;
  const char* xb = (const char*)x + (size_t)ts * 128 * DM * 2;
  asm volatile("" ::"s"(Wb), "s"(xb));
  if (idx >= 20) {
    const int w = ((idx - 20) * 8 + xcd) * 8 + wid;
    TrUnit tu[2];
    const TrJob j0 = tr_job(w, Wg, Wu, Wd, WguT, WdT), j1 = tr_job(768 + w, Wg, Wu, Wd, WguT, WdT);
    tr_load(tu[0], j0.src, j0.lds, j0.k0, j0.n0, lane);
    tr_load(tu[1], j1.src, j1.lds, j1.k0, j1.n0, lane);
#pragma unroll
    for (int i = 0; i < 22; ++i) {
      int c = w + 768 * i; if (c > 16383) c = 16383;
      glds16(lane * 16, (const char*)(c < 8192 ? Cr : Ci) + (size_t)(c & 8191) * 1024, wid * 1024);
    }
    asm volatile("s_waitcnt vmcnt(22)" : "+v"(tu[0].v[0]), "+v"(tu[0].v[1]), "+v"(tu[0].v[2]), "+v"(tu[0].v[3]), "+v"(tu[0].v[4]), "+v"(tu[0].v[5]),
                 "+v"(tu[0].v[6]), "+v"(tu[0].v[7]), "+v"(tu[1].v[0]), "+v"(tu[1].v[1]), "+v"(tu[1].v[2]), "+v"(tu[1].v[3]), "+v"(tu[1].v[4]),
                 "+v"(tu[1].v[5]), "+v"(tu[1].v[6]), "+v"(tu[1].v[7])::"memory");
#pragma unroll
    for (int q = 0; q < 2; ++q) {
      const TrJob& jb = q ? j1 : j0;
      if (jb.ok) {
        tr_store(tu[q], jb.dst, jb.ldd, jb.k0, jb.n0, lane);
        if (jb.cs) {
          float c[4] = {0.f, 0.f, 0.f, 0.f}; tr_colsum<0>(tu[q], c); tr_colsum<1>(tu[q], c);
          if (lane < 16) *(f32x4*)(csp + jb.cso + 4 * lane) = f32x4{c[0], c[1], c[2], c[3]};
        }
      }
    }
    asm volatile("s_waitcnt vmcnt(0)" ::: "memory");
    return;
  }
  static_assert(C::LASTSLOT == C::NST - 1, "");
  float* wsum = (float*)shm;
  char* stg = shm + 1024;
  if (__builtin_expect(wid >= 4, 1)) {
    const int j = lane >> 3;
    auto sA = [](int p) { return (unsigned)((32 * (p >> 2) + 16 * (p & 1) + 4 * ((p >> 1) & 1)) * DM * 2); };
    auto sB = [](int p) { return (unsigned)(p * 8 * DM * 2); };
    auto ptrs = [&](int k, const char*& pa, const char*& pb) { pa = xb + k * 128; pb = Wb + k * 128; };
    const unsigned vA = (unsigned)((8 * (j >> 2) + (j & 3)) * DM * 2), vB = (unsigned)(j * DM * 2);
    if (wid == 4) loader<C, 0, 4, 0, 4>(lane, sA, vA, sB, vB, ptrs);
    else if (wid == 5) loader<C, 4, 4, 4, 4>(lane, sA, vA, sB, vB, ptrs);
    else if (wid == 6) loader<C, 8, 4, 8, 4>(lane, sA, vA, sB, vB, ptrs);
    else loader<C, 12, 4, 12, 4>(lane, sA, vA, sB, vB, ptrs);
    lg_bar();
  } else {
    const float sgn = half ? -1.0f : 1.0f;
    if (wid == 0) k1_consumer<0>(shm, wsum, stg, lane, sgn );
    else if (wid == 1) k1_consumer<1>(shm, wsum, stg, lane, sgn );
    else if (wid == 2) k1_consumer<2>(shm, wsum, stg, lane, sgn );
    else k1_consumer<3>(shm, wsum, stg, lane, sgn );
  }
  lg_bar();
  u16* ob = abT + (size_t)tl * 128 * 4096 + half * 2048 + ts * 128;
#pragma unroll
  for (int i = 0; i < 4; ++i) {
    const int c = threadIdx.x + i * 512, row = c >> 4, ch = c & 15;
    *(u32x4*)(ob + (size_t)row * 4096 + ch * 8) = *(const u32x4*)(stg + row * 272 + ch * 16);
  }
}

DEVI u32x4 add8(f32x4 a0, f32x4 a1, u32x4 r) {
  u32x4 o;
  o.x = pack2(a0[0] + bflo(r.x), a0[1] + bfhi(r.x)); o.y = pack2(a0[2] + bflo(r.y), a0[3] + bfhi(r.y));
  o.z = pack2(a1[0] + bflo(r.z), a1[1] + bfhi(r.z)); o.w = pack2(a1[2] + bflo(r.w), a1[3] + bfhi(r.w));
  return o;
}
DEVI u32x4 addbf8(u32x4 a, u32x4 b) {
  u32x4 o;
  o.x = pack2(bflo(a.x) + bflo(b.x), bfhi(a.x) + bfhi(b.x)); o.y = pack2(bflo(a.y) + bflo(b.y), bfhi(a.y) + bfhi(b.y));
  o.z = pack2(bflo(a.z) + bflo(b.z), bfhi(a.z) + bfhi(b.z)); o.w = pack2(bflo(a.w) + bflo(b.w), bfhi(a.w) + bfhi(b.w));
  return o;
}

#define K2_PRE 4
#define K2_NST 6
using C2 = Cfg<80, 128, 1, 2, 2, 64, K2_NST, 32, K2_PRE>;
__global__ __launch_bounds__(512, 2) void k2_seq(const u16* __restrict__ x, const u16* __restrict__ Cr, const u16* __restrict__ Ci,
                                                 const u16* __restrict__ abT, const float* __restrict__ csp,
                                                 u16* __restrict__ P, float* __restrict__ csum) {
  using C = C2;
  extern __shared__ __attribute__((aligned(16))) char shm[];
  const int bid = blockIdx.x, xcd = bid & 7, idx = bid >> 3;
  const int half = xcd & 1, tt = (xcd >> 1) * 4 + (idx >> 3), te = idx & 7;
  const int wid = __builtin_amdgcn_readfirstlane(threadIdx.x >> 6), lane = threadIdx.x & 63;
  static_assert((C::NST - 1 - C::LASTSLOT) * C::STG >= 2 * 128 * 336, "");
  char* red = shm + (C::LASTSLOT + 1) * C::STG;
  u32x4 rx[3] = {{0, 0, 0, 0}, {0, 0, 0, 0}, {0, 0, 0, 0}};
  auto xload = [&]() {
    if (half == 0) {
#pragma unroll
      for (int i = 0; i < 3; ++i) { int c = threadIdx.x + i * 512; if (c > 1279) c = 1279;
        rx[i] = *(const u32x4*)(x + (size_t)(tt * 128 + c / 10) * DM + te * 80 + (c % 10) * 8); }
    }
  };
  const char* ab = (const char*)abT + (size_t)te * 80 * 8192 + half * 4096;
  const char* cc = (const char*)(half ? Ci : Cr) + (size_t)tt * 128 * 4096;
  asm volatile("" ::"s"(ab), "s"(cc));
  if (__builtin_expect(wid >= 4, 1)) {
    const int j = lane >> 3;
    auto sA = [](int p) { return (unsigned)(p * 8 * 8192); };
    auto sB = [](int p) { return (unsigned)(p * 8 * 4096); };
    auto ptrs = [&](int k, const char*& pa, const char*& pb) { pa = ab + k * 128; pb = cc + k * 128; };
    const unsigned vA = (unsigned)(j * 8192), vB = (unsigned)(j * 4096);
    if (wid == 4) loader<C, 0, 3, 0, 4>(lane, sA, vA, sB, vB, ptrs);
    else if (wid == 5) loader<C, 3, 3, 4, 4>(lane, sA, vA, sB, vB, ptrs);
    else if (wid == 6) loader<C, 6, 2, 8, 4>(lane, sA, vA, sB, vB, ptrs);
    else loader<C, 8, 2, 12, 4>(lane, sA, vA, sB, vB, ptrs);
    xload();
  } else {
    if (wid == 0 && bid < 48) {
      const int col = bid * 64 + lane; float c = 0;
#pragma unroll
      for (int i = 0; i < 20; ++i) c += csp[(size_t)i * (2 * FF) + col];
      csum[col] = c;
    }
    xload();
    f32x4 acc[5][4];
    consumer<C>(acc, shm, wid, lane, [](auto&) {} );
    const int fr = lane & 15, fq = lane >> 4, kg = wid >> 1, wb = wid & 1;
#pragma unroll
    for (int n = 0; n < 4; ++n)
#pragma unroll
      for (int j = 0; j < 5; ++j) *(f32x4*)(red + kg * (128 * 336) + ((wb * 4 + n) * 16 + fr) * 336 + j * 64 + fq * 16) = acc[j][n];
  }
  lg_bar();
  u16* op = P + (size_t)half * SEQ * DM + (size_t)tt * 128 * DM + te * 80;
#pragma unroll
  for (int i = 0; i < 3; ++i) {
    const int c = threadIdx.x + i * 512;
    if (i < 2 || c < 1280) {
      const int row = c / 10, ch = c % 10;
      const char* p = red + row * 336 + ch * 32;
      f32x4 a0 = *(const f32x4*)p + *(const f32x4*)(p + 128 * 336), a1 = *(const f32x4*)(p + 16) + *(const f32x4*)(p + 16 + 128 * 336);
      *(u32x4*)(op + (size_t)row * DM + ch * 8) = add8(a0, a1, rx[i]);
    }
  }
}

#define K3_PRE 2
using C3 = Cfg<192, 128, 2, 2, 1, 64, 4, 10, K3_PRE>;
DEVI float fadd1(float a, float b) { float r; asm("v_add_f32 %0, %1, %2" : "=v"(r) : "v"(a), "v"(b)); return r; }
DEVI void facc1(float& s, float a) { asm("v_add_f32 %0, %0, %1" : "+v"(s) : "v"(a)); }
DEVI void fsq1(float& s, float a) { asm("v_fmac_f32 %0, %1, %1" : "+v"(s) : "v"(a)); }
DEVI void gload16(u32x4& d, unsigned voff, const char* sbase) { asm volatile("global_load_dwordx4 %0, %1, %2" : "=v"(d) : "v"(voff), "s"(sbase)); }
template <int WA> DEVI void k3_consumer(char* shm, f32x2* rowst, char* stg, const float* csum, int wid, int lane ) {
  using C = C3;
  const int fr = lane & 15, fq = lane >> 4, wb = wid & 1;
  f32x4 cs[6];
#pragma unroll
  for (int j = 0; j < 6; ++j) cs[j] = *(const f32x4*)(csum + (j & 1) * FF + WA * 48 + (j >> 1) * 16 + 4 * fq);
  f32x4 acc[6][4];
  f32x4 sh[2] = {{0, 0, 0, 0}, {0, 0, 0, 0}}, sq[2] = {{0, 0, 0, 0}, {0, 0, 0, 0}};
  bf16x8 ones = {0x3f80, 0x3f80, 0x3f80, 0x3f80, 0x3f80, 0x3f80, 0x3f80, 0x3f80};
  asm volatile("" : "+v"(ones));
  consumer<C>(acc, shm, wid, lane, [&](bf16x8 (&R)[10]) {
    sh[0] = MFMA(ones, R[6 + 2 * WA], sh[0]); sq[0] = MFMA(R[6 + 2 * WA], R[6 + 2 * WA], sq[0]);
    sh[1] = MFMA(ones, R[7 + 2 * WA], sh[1]); sq[1] = MFMA(R[7 + 2 * WA], R[7 + 2 * WA], sq[1]);
  } );
#pragma unroll
  for (int i = 0; i < 2; ++i) {
    const float d01 = (fr & 1) ? sq[i][1] : sq[i][0], d23 = (fr & 1) ? sq[i][3] : sq[i][2], d = (fr & 2) ? d23 : d01;
    const float mean = sh[i][0] * (1.0f / DM);
    const float var = fmaxf(d * (1.0f / DM) - mean * mean, 0.0f);
    if (fq == (fr >> 2)) rowst[(wb * 4 + 2 * WA + i) * 16 + fr] = f32x2{-mean, var + 1e-5f};
  }
  lg_bar();
#pragma unroll
  for (int n = 0; n < 4; ++n) {
    const int ml = (wb * 4 + n) * 16 + fr;
    const f32x2 st = rowst[ml];
    const float nmean = st[0], ve = st[1];
    const float rstd = __builtin_amdgcn_rsqf(ve);
    const float k1 = -2.3022081985f * rstd;
    const float k2 = k1 * 0.044715f * rstd * rstd;
    f32x4 G[3], U[3], z[3];
#pragma unroll
    for (int p = 0; p < 3; ++p) G[p] = cs[2 * p] * nmean + acc[2 * p][n];
#pragma unroll
    for (int p = 0; p < 3; ++p) z[p] = G[p] * G[p];
#pragma unroll
    for (int p = 0; p < 3; ++p) U[p] = cs[2 * p + 1] * nmean + acc[2 * p + 1][n];
#pragma unroll
    for (int p = 0; p < 3; ++p) z[p] = z[p] * k2 + k1;
#pragma unroll
    for (int p = 0; p < 3; ++p) z[p] = z[p] * G[p];
#pragma unroll
    for (int p = 0; p < 3; ++p) { z[p][0] = __builtin_amdgcn_exp2f(z[p][0]); z[p][1] = __builtin_amdgcn_exp2f(z[p][1]); z[p][2] = __builtin_amdgcn_exp2f(z[p][2]); z[p][3] = __builtin_amdgcn_exp2f(z[p][3]); U[p] = U[p] * G[p]; }
#pragma unroll
    for (int p = 0; p < 3; ++p) z[p] = z[p] * ve + ve;
#pragma unroll
    for (int p = 0; p < 3; ++p) { z[p][0] = __builtin_amdgcn_rcpf(z[p][0]); z[p][1] = __builtin_amdgcn_rcpf(z[p][1]); z[p][2] = __builtin_amdgcn_rcpf(z[p][2]); z[p][3] = __builtin_amdgcn_rcpf(z[p][3]); }
#pragma unroll
    for (int p = 0; p < 3; ++p) {
      f32x4 v = U[p] * z[p];
      u32x2 o; o.x = pack2(v[0], v[1]); o.y = pack2(v[2], v[3]);
      *(u32x2*)(stg + ml * 208 + (WA * 48 + p * 16 + fq * 4) * 2) = o;
    }
  }
}
__global__ __launch_bounds__(512, 2) void k3_ffn1(const u16* __restrict__ P, const u16* __restrict__ WguT, const float* __restrict__ csum,
                                                  u16* __restrict__ act) {
  using C = C3;
  extern __shared__ __attribute__((aligned(16))) char shm[];
  const int bid = blockIdx.x, xcd = bid & 7, idx = bid >> 3;
  const int tm = (xcd >> 2) * 8 + (idx >> 2), tn = (xcd & 3) * 4 + (idx & 3);
  const int wid = __builtin_amdgcn_readfirstlane(threadIdx.x >> 6), lane = threadIdx.x & 63;
  constexpr int FREE = ((C::LASTSLOT + 1) % C::NST) * C::STG;
  f32x2* rowst = (f32x2*)(shm + FREE);
  char* stg = shm + FREE + 4096;
  const char* wbp = (const char*)WguT + (size_t)tn * 96 * DM * 2;
  const char* pb0 = (const char*)P + (size_t)tm * 128 * DM * 2;
  asm volatile("" ::"s"(wbp), "s"(pb0));
  if (__builtin_expect(wid >= 4, 1)) {
    const int lw = wid - 4, j = lane >> 3;
    const char* p0 = pb0 + lw * 32 * DM * 2;
    const char* p1 = p0 + (size_t)SEQ * DM * 2;
    const unsigned sw0 = (unsigned)(((lane & 7) ^ (lane >> 4)) * 16);
    const unsigned ldsb = (unsigned)(size_t)(__attribute__((address_space(3))) char*)shm;
    unsigned vw[6];
#pragma unroll
    for (int i = 0; i < 6; ++i) { const int p = lw * 6 + i;
      vw[i] = (unsigned)(j * DM * 2) + (sw0 ^ (unsigned)((p & 1) * 64)) + (unsigned)((((p >> 1) & 1) * FF + 16 * (p >> 2) + 8 * (p & 1)) * DM * 2); }
    const unsigned vh = (unsigned)(j * DM * 2 + (lane & 7) * 16);
    char* hw = shm + (24 + lw * 4) * 1024 + j * 128;
    u32x4 buf[3][8];
    auto vm1 = [&](auto S, auto Q) {
      constexpr int s = decltype(S)::value, q = decltype(Q)::value;
      if constexpr (q < 8) gload16(buf[s % 3][q], vh + (q >> 1) * 8 * DM * 2, ((q & 1) ? p1 : p0) + s * 128);
      else glds16(vw[q - 8], wbp + s * 128, ldsb + (s % C::NST) * C::STG + (lw * 6 + q - 8) * 1024);
    };
    static_for<-1, C::NK - 1>([&](auto K) {
      constexpr int k = decltype(K)::value;
      constexpr int c = k + 1;
      constexpr int s0 = C::issued(k - 1) + 1, nop = (C::issued(k) - C::issued(k - 1)) * 14;
      auto ops = [&](auto B, auto E) { static_for<decltype(B)::value, decltype(E)::value>([&](auto O) {
        constexpr int o = decltype(O)::value; vm1(IC(s0 + o / 14), IC(o % 14)); }); };
      if constexpr (k == -1) ops(IC(0), IC(nop));
      u32x4 (&b)[8] = buf[c % 3];
      asm volatile("s_waitcnt vmcnt(%8)" : "+v"(b[0]), "+v"(b[1]), "+v"(b[2]), "+v"(b[3]), "+v"(b[4]), "+v"(b[5]), "+v"(b[6]), "+v"(b[7])
                   : "i"(((k == -1 ? C::issued(k) : C::issued(k - 1)) - c) * 14 + 6) : "memory");
      static_for<0, 4>([&](auto I) {
        constexpr int i = decltype(I)::value;
        if constexpr (k >= 0) { ops(IC(nop * i / 4), IC(nop * (i + 1) / 4)); SB(); }
        const u32x4 u = b[2 * i], v = b[2 * i + 1];
        float hh[8];
        hh[0] = fadd1(bflo(u.x), bflo(v.x)); hh[1] = fadd1(bfhi(u.x), bfhi(v.x)); hh[2] = fadd1(bflo(u.y), bflo(v.y)); hh[3] = fadd1(bfhi(u.y), bfhi(v.y));
        hh[4] = fadd1(bflo(u.z), bflo(v.z)); hh[5] = fadd1(bfhi(u.z), bfhi(v.z)); hh[6] = fadd1(bflo(u.w), bflo(v.w)); hh[7] = fadd1(bfhi(u.w), bfhi(v.w));
        u32x4 o; o.x = pack2(hh[0], hh[1]); o.y = pack2(hh[2], hh[3]); o.z = pack2(hh[4], hh[5]); o.w = pack2(hh[6], hh[7]);
        *(u32x4*)(hw + (c % C::NST) * C::STG + i * 1024 + (sw0 ^ (unsigned)((i & 1) * 64))) = o;
        SB();
      });
      asm volatile("s_waitcnt vmcnt(%0) lgkmcnt(0)\n\ts_barrier" ::"i"((C::issued(k) - c) * 14) : "memory");
    });
    lg_bar();
  } else {
    if (wid < 2) k3_consumer<0>(shm, rowst, stg, csum + tn * 96, wid, lane );
    else k3_consumer<1>(shm, rowst, stg, csum + tn * 96, wid, lane );
  }
  lg_bar();
  u16* ob = act + (size_t)tm * 128 * FF + tn * 96;
#pragma unroll
  for (int i = 0; i < 3; ++i) {
    int c = threadIdx.x + i * 512, row = c / 12, ch = c % 12;
    *(u32x4*)(ob + (size_t)row * FF + ch * 8) = *(const u32x4*)(stg + row * 208 + ch * 16);
  }
}

#define K4_PRE 3
using C4 = Cfg<80, 64, 1, 2, 2, 128, 4, 12, K4_PRE>;
__global__ __launch_bounds__(512, 2) void k4_ffn2(const u16* __restrict__ P, const u16* __restrict__ act, const u16* __restrict__ WdT,
                                                  u16* __restrict__ out) {
  using C = C4;
  extern __shared__ __attribute__((aligned(16))) char shm[];
  const int bid = blockIdx.x, xcd = bid & 7, idx = bid >> 3;
  const int tm = (xcd >> 1) * 8 + (idx >> 2), td = (xcd & 1) * 4 + (idx & 3);
  const int wid = __builtin_amdgcn_readfirstlane(threadIdx.x >> 6), lane = threadIdx.x & 63;
  static_assert(C::LASTSLOT == C::NST - 1 && (C::NST - 1) * C::STG >= 2 * 64 * 336, "");
  u32x4 ra[2], rb[2];
  auto rload = [&]() {
#pragma unroll
    for (int i = 0; i < 2; ++i) { int c = threadIdx.x + i * 512; if (c > 639) c = 639;
      const u16* p = P + (size_t)(tm * 64 + c / 10) * DM + td * 80 + (c % 10) * 8;
      ra[i] = *(const u32x4*)p; rb[i] = *(const u32x4*)(p + (size_t)SEQ * DM); }
  };
  const char* wp = (const char*)WdT + (size_t)td * 80 * FF * 2;
  const char* ap = (const char*)act + (size_t)tm * 64 * FF * 2;
  asm volatile("" ::"s"(wp), "s"(ap));
  if (__builtin_expect(wid >= 4, 1)) {
    const unsigned v = (unsigned)((lane >> 3) * FF * 2);
    auto sA = [](int p) { return (unsigned)(p * 8 * FF * 2); };
    auto ptrs = [&](int k, const char*& pa, const char*& pb) { pa = wp + k * 256; pb = ap + k * 256; };
    if (wid == 4) loader<C, 0, 5, 0, 4>(lane, sA, v, sA, v, ptrs);
    else if (wid == 5) loader<C, 5, 5, 4, 4>(lane, sA, v, sA, v, ptrs);
    else if (wid == 6) loader<C, 10, 5, 8, 4>(lane, sA, v, sA, v, ptrs);
    else loader<C, 15, 5, 12, 4>(lane, sA, v, sA, v, ptrs);
    rload();
  } else {
    rload();
    f32x4 acc[5][2];
    consumer<C>(acc, shm, wid, lane, [](auto&) {} );
    const int fr = lane & 15, fq = lane >> 4, kg = wid >> 1, wb = wid & 1;
#pragma unroll
    for (int n = 0; n < 2; ++n)
#pragma unroll
      for (int j = 0; j < 5; ++j) *(f32x4*)(shm + kg * (64 * 336) + ((wb * 2 + n) * 16 + fr) * 336 + j * 64 + fq * 16) = acc[j][n];
  }
  lg_bar();
  u16* op = out + (size_t)tm * 64 * DM + td * 80;
#pragma unroll
  for (int i = 0; i < 2; ++i) {
    const int c = threadIdx.x + i * 512;
    if (i == 0 || c < 640) {
      const int row = c / 10, ch = c % 10;
      const char* p = shm + row * 336 + ch * 32;
      f32x4 a0 = *(const f32x4*)p + *(const f32x4*)(p + 64 * 336), a1 = *(const f32x4*)(p + 16) + *(const f32x4*)(p + 16 + 64 * 336);
      *(u32x4*)(op + (size_t)row * DM + ch * 8) = add8(a0, a1, addbf8(ra[i], rb[i]));
    }
  }
}

static void launch_stage(int which, void* const* d_in, void* d_out, void* d_ws, hipStream_t stream) {
  const u16* x = (const u16*)d_in[0];
  const u16* Cr = (const u16*)d_in[1];
  const u16* Ci = (const u16*)d_in[2];
  const u16* Wre = (const u16*)d_in[3];
  const u16* Wim = (const u16*)d_in[4];
  const u16* Wg = (const u16*)d_in[6];
  const u16* Wu = (const u16*)d_in[7];
  const u16* Wd = (const u16*)d_in[8];
  char* ws = (char*)d_ws;
  u16* abT = (u16*)(ws);
  u16* WguT = (u16*)(ws + (10 << 20));
  u16* WdT = (u16*)(ws + (14 << 20));
  u16* act = (u16*)(ws + (16 << 20));
  u16* P = (u16*)(ws + (28 << 20));
  float* csp = (float*)(ws + (25 << 20));
  float* csum = (float*)(ws + (26 << 20));
  u16* out = (u16*)d_out;
  if (which == 0) {
    (void)hipFuncSetAttribute((const void*)k1_hid, hipFuncAttributeMaxDynamicSharedMemorySize, C1::LDS);
    k1_hid<<<256, 512, C1::LDS, stream>>>(x, Wre, Wim, Cr, Ci, Wg, Wu, Wd, abT, WguT, WdT, csp);
  } else if (which == 1) {
    (void)hipFuncSetAttribute((const void*)k2_seq, hipFuncAttributeMaxDynamicSharedMemorySize, C2::LDS);
    k2_seq<<<256, 512, C2::LDS, stream>>>(x, Cr, Ci, abT, csp, P, csum);
  } else if (which == 2) {
    (void)hipFuncSetAttribute((const void*)k3_ffn1, hipFuncAttributeMaxDynamicSharedMemorySize, C3::LDS);
    k3_ffn1<<<256, 512, C3::LDS, stream>>>(P, WguT, csum, act);
  } else {
    (void)hipFuncSetAttribute((const void*)k4_ffn2, hipFuncAttributeMaxDynamicSharedMemorySize, C4::LDS);
    k4_ffn2<<<256, 512, C4::LDS, stream>>>(P, act, WdT, out);
  }
}

extern "C" void kernel_launch(void* const* d_in, const int* in_sizes, int n_in, void* d_out, int out_size, void* d_ws, size_t ws_size,
                              hipStream_t stream) {
  for (int i = 0; i < 4; ++i) launch_stage(i, d_in, d_out, d_ws, stream);
}
```

```cpp
#include <hip/hip_runtime.h>
#include <hip/hip_bf16.h>
#include <stdint.h>
#include <type_traits>

typedef unsigned short u16;
typedef __attribute__((ext_vector_type(8))) short bf16x8;
typedef __attribute__((ext_vector_type(4))) float f32x4;
typedef __attribute__((ext_vector_type(2))) float f32x2;
typedef __attribute__((ext_vector_type(2))) __bf16 bf16x2;
typedef __attribute__((ext_vector_type(2))) unsigned u32x2;
typedef __attribute__((ext_vector_type(4))) unsigned u32x4;

#define DEVI __device__ __forceinline__
#define IC(x) std::integral_constant<int, (x)>{}
#define SB() __builtin_amdgcn_sched_barrier(0)
#define MFMA(a, b, c) __builtin_amdgcn_mfma_f32_16x16x32_bf16(a, b, c, 0, 0, 0)

constexpr int SEQ = 2048, DM = 640, FF = 1536;

template <int B, int E, class F> DEVI void static_for(F&& f) {
  if constexpr (B < E) { f(IC(B)); static_for<B + 1, E>(f); }
}
DEVI void glds16(unsigned voff, const char* sbase, unsigned ldsaddr) {
  asm volatile("s_mov_b32 m0, %2\n\ts_nop 0\n\tglobal_load_lds_dwordx4 %0, %1" ::"v"(voff), "s"(sbase), "s"(ldsaddr));
}
DEVI void glds16c(unsigned voff, const char* sbase, unsigned ldsaddr) {
  asm volatile("s_mov_b32 m0, %2\n\ts_nop 0\n\tglobal_load_lds_dwordx4 %0, %1 sc1" ::"v"(voff), "s"(sbase), "s"(ldsaddr));
}
DEVI void gstore16c(void* p, u32x4 v) { asm volatile("global_store_dwordx4 %0, %1, off sc1\n\ts_nop 1" ::"v"(p), "v"(v) : "memory"); }
typedef unsigned long long u64;
DEVI void flag_set(u64* f, u64 tag) { __hip_atomic_store(f, tag, __ATOMIC_RELAXED, __HIP_MEMORY_SCOPE_AGENT); }
DEVI void flag_wait16(const u64* f, u64 tag, int lane) {
  for (unsigned spin = 0; spin < (1u << 20); ++spin) {
    const u64 v = __hip_atomic_load(f + (lane & 15), __ATOMIC_RELAXED, __HIP_MEMORY_SCOPE_AGENT);
    if (__all(v == tag)) break;
    __builtin_amdgcn_s_sleep(2);
  }
  asm volatile("" ::: "memory");
}
template <int N> DEVI void waitvm_bar() { asm volatile("s_waitcnt vmcnt(%0)\n\ts_barrier" ::"i"(N) : "memory"); }
DEVI void bar() { asm volatile("s_barrier" ::: "memory"); }
DEVI void touch(unsigned& sink, unsigned voff, const void* sbase) { asm volatile("global_load_dword %0, %1, %2" : "+v"(sink) : "v"(voff), "s"(sbase)); }
DEVI void touch_done(unsigned& sink) { asm volatile("s_waitcnt vmcnt(0)" : "+v"(sink)::"memory"); }
DEVI void lg_bar() { asm volatile("s_waitcnt lgkmcnt(0)\n\ts_barrier" ::: "memory"); }
DEVI unsigned pack2(float a, float b) { f32x2 p = {a, b}; return __builtin_bit_cast(unsigned, __builtin_convertvector(p, bf16x2)); }
DEVI float bflo(unsigned v) { return __builtin_bit_cast(float, v << 16); }
DEVI float bfhi(unsigned v) { return __builtin_bit_cast(float, v & 0xffff0000u); }
template <int CTRL> DEVI float dppf(float v) {
  return __builtin_bit_cast(float, __builtin_amdgcn_update_dpp(0, __builtin_bit_cast(int, v), CTRL, 0xf, 0xf, true));
}
DEVI float red16(float v) { v += dppf<0xB1>(v); v += dppf<0x4E>(v); v += dppf<0x141>(v); v += dppf<0x140>(v); return v; }

template <int TA_, int TB_, int NWA_, int NWB_, int KS_, int BK_, int NST_, int NK_, int PRE_> struct Cfg {
  static constexpr int TA = TA_, TB = TB_, NWA = NWA_, NWB = NWB_, KS = KS_, BK = BK_, NST = NST_, NK = NK_;
  static constexpr int NSUB = BK / 64, PA = TA / 8, PB = TB / 8, PPS = PA + PB, STG = NSUB * PPS * 1024, SUBB = PPS * 1024;
  static constexpr int WAF = TA / 16 / NWA, WBF = TB / 16 / NWB, KH = (BK / 32) / KS;
  static constexpr int NF = WAF + WBF, NMF = WAF * WBF;
  static constexpr int LDS = NST * STG;
  static constexpr int LASTSLOT = (NK - 1) % NST;
  static constexpr int issued(int k) {
    if (k < -1) return -1;
    int a = 2 * k + 1 + PRE_, b = k - 1 + NST, c = NK - 1;
    if (k == -1) b = NST - 1;
    int m = a < b ? a : b; return m < c ? m : c;
  }
  static constexpr int steady_begin() { int k = 0; while (!(issued(k) == k - 1 + NST && issued(k - 1) == k - 2 + NST) && k < NK) ++k; return k; }
  static constexpr int K0 = steady_begin() < NK - NST + 1 ? steady_begin() : NK - NST + 1;
  static constexpr int K1 = NK - NST + 1 > K0 ? NK - NST + 1 : K0;
};

template <class C, int A0, int GA, int B0, int GB> struct Pieces {
  static constexpr int G = GA + GB;
  unsigned voff[G];
  template <class SA, class SB_> DEVI Pieces(int lane, SA sA, unsigned vA, SB_ sB, unsigned vB) {
    const unsigned sw0 = (unsigned)(((lane & 7) ^ (lane >> 4)) * 16);
    const unsigned a_e = vA + sw0, a_o = vA + (sw0 ^ 64u), b_e = vB + sw0, b_o = vB + (sw0 ^ 64u);
    static_for<0, G>([&](auto I) {
      constexpr int i = decltype(I)::value;
      if constexpr (i < GA) { constexpr int p = A0 + i, sub = p / C::PA, pr = p % C::PA; voff[i] = ((pr & 1) ? a_o : a_e) + (sA(pr) + sub * 128u); }
      else { constexpr int p = B0 + i - GA, sub = p / C::PB, pr = p % C::PB; voff[i] = ((pr & 1) ? b_o : b_e) + (sB(pr) + sub * 128u); }
    });
  }
  template <int DEP, int B, int E> DEVI void issue(const char* pa, const char* pb, unsigned slot) const {
    static_for<B, E>([&](auto I) {
      constexpr int i = decltype(I)::value;
      constexpr int p = i < GA ? A0 + i : B0 + i - GA, np = i < GA ? C::PA : C::PB;
      constexpr unsigned lo = (p / np) * C::SUBB + ((i < GA ? 0 : C::PA) + p % np) * 1024;
      if constexpr ((i < GA) == (DEP == 1) && DEP != 0) glds16c(voff[i], i < GA ? pa : pb, lo + slot);
      else glds16(voff[i], i < GA ? pa : pb, lo + slot);
    });
  }
};
template <class C, int X, int NX> struct XSched {
  static constexpr int xdone(int k) { int n = (k + 2) * X; return n < 0 ? 0 : (n > NX ? NX : n); }
  static constexpr int it(int c) { int i = -1; while (C::issued(i) < c) ++i; return i; }
  static constexpr int younger(int k, int G) { return (C::issued(k) - (k + 1)) * G + xdone(k) - xdone(it(k + 1) - 1); }
};
template <class C, int A0, int GA, int B0, int GB, int X, int NX, class SA, class SB_, class Ptrs, class Extra>
DEVI void loader_x(int lane, SA sA, unsigned vA, SB_ sB, unsigned vB, Ptrs ptrs, Extra extra) {
  constexpr int G = GA + GB;
  using XS = XSched<C, X, NX>;
  const Pieces<C, A0, GA, B0, GB> pc(lane, sA, vA, sB, vB);
  static_for<-1, C::NK - 1>([&](auto K) {
    constexpr int k = decltype(K)::value;
    static_for<C::issued(k - 1) + 1, C::issued(k) + 1>([&](auto S) {
      constexpr int s = decltype(S)::value;
      const char *pa, *pb; ptrs(s, pa, pb);
      pc.template issue<0, 0, G>(pa, pb, (s % C::NST) * C::STG);
    });
    static_for<XS::xdone(k - 1), XS::xdone(k)>(extra);
    static_assert(XS::younger(k, G) <= 63, "vmcnt range");
    waitvm_bar<XS::younger(k, G)>();
  });
  static_assert(XS::xdone(C::NK - 2) == NX, "all extras issued");
}
template <class C, int DEP, int A0, int GA, int B0, int GB, class SA, class SB_, class Ptrs>
DEVI void loader_dep(int lane, SA sA, unsigned vA, SB_ sB, unsigned vB, Ptrs ptrs) {
  constexpr int G = GA + GB, NST = C::NST, GD = DEP == 1 ? GA : GB;
  constexpr int DB = DEP == 1 ? 0 : GA, DE = DEP == 1 ? GA : G, IB = DEP == 1 ? GA : 0, IE = DEP == 1 ? G : GA;
  const Pieces<C, A0, GA, B0, GB> pc(lane, sA, vA, sB, vB);
  static_for<0, NST>([&](auto S) { constexpr int s = decltype(S)::value; const char *pa, *pb; ptrs(s, pa, pb); pc.template issue<DEP, IB, IE>(pa, pb, s * C::STG); });
  bar();
  constexpr int KS0a = C::K0 > NST - 2 ? C::K0 : NST - 2, KS0 = KS0a > 1 ? KS0a : 1;
  static_for<-1, KS0>([&](auto K) {
    constexpr int k = decltype(K)::value;
    static_for<C::issued(k - 1) + 1, C::issued(k) + 1>([&](auto S) {
      constexpr int s = decltype(S)::value;
      const char *pa, *pb; ptrs(s, pa, pb);
      if constexpr (s < NST) pc.template issue<DEP, DB, DE>(pa, pb, s * C::STG); else pc.template issue<DEP, 0, G>(pa, pb, (s % NST) * C::STG);
    });
    constexpr int hi = C::issued(k), lo = k + 2;
    constexpr int nd = (hi < NST - 1 ? hi : NST - 1) - lo + 1 > 0 ? (hi < NST - 1 ? hi : NST - 1) - lo + 1 : 0;
    constexpr int nf = hi - lo + 1 - nd;
    waitvm_bar<nd * GD + nf * G>();
  });
  unsigned slot = ((KS0 - 1 + NST) % NST) * C::STG;
  for (int k = KS0; k < C::K1; ++k) {
    const char *pa, *pb; ptrs(k - 1 + NST, pa, pb);
    pc.template issue<DEP, 0, G>(pa, pb, slot);
    slot += C::STG; if (slot == NST * C::STG) slot = 0;
    waitvm_bar<(NST - 2) * G>();
  }
  static_for<C::K1, C::NK - 1>([&](auto K) { constexpr int k = decltype(K)::value; waitvm_bar<(C::NK - 2 - k) * G>(); });
}

template <class C, class Extra> DEVI void consumer(f32x4 (&acc)[C::WAF][C::WBF], char* shm, int wid, int lane, Extra extra ) {
  constexpr int WAF = C::WAF, WBF = C::WBF, KH = C::KH, NF = C::NF, NMF = C::NMF, STG = C::STG, NST = C::NST, NK = C::NK;
  const int kg = wid / (C::NWA * C::NWB), wab = wid % (C::NWA * C::NWB), wa = wab / C::NWB, wb = wab % C::NWB;
#pragma unroll
  for (int j = 0; j < WAF; ++j)
#pragma unroll
    for (int b = 0; b < WBF; ++b) acc[j][b] = f32x4{0, 0, 0, 0};
  const int fr = lane & 15, fq = lane >> 4;
  const int c0 = fr * 128 + ((fq ^ (fr >> 1)) * 16);
  int rda[KH], rdb[KH];
#pragma unroll
  for (int h = 0; h < KH; ++h) {
    int ks = kg * KH + h, o = (c0 ^ ((ks & 1) * 64)) + (ks >> 1) * C::SUBB;
    rda[h] = o + wa * WAF * 2048; rdb[h] = o + C::TA * 128 + wb * WBF * 2048;
  }
  bf16x8 R[2][NF];
  auto rd1 = [&](bf16x8 (&Rn)[NF], unsigned slot, int h, int f) {
    if (f < WAF) Rn[f] = *(const bf16x8*)(shm + slot + rda[h] + f * 2048);
    else Rn[f] = *(const bf16x8*)(shm + slot + rdb[h] + (f - WAF) * 2048);
  };
  auto body = [&](bf16x8 (&Rc)[NF], bf16x8 (&Rn)[NF], unsigned slot, int h, bool dor) {
    int ri = 0;
#pragma unroll
    for (int m = 0; m < NMF; ++m) {
      { int n = m / WAF, j = m % WAF; acc[j][n] = MFMA(Rc[j], Rc[WAF + n], acc[j][n]); }
      int rt = ((m + 1) * NF * 4 + 3 * NMF - 1) / (3 * NMF); if (rt > NF) rt = NF;
#pragma unroll
      for (; ri < rt; ++ri) if (dor) rd1(Rn, slot, h, ri);
      SB();
    }
    extra(Rc);
    SB();
  };
  lg_bar();
#pragma unroll
  for (int f = 0; f < NF; ++f) rd1(R[0], 0, 0, f);
  unsigned slot = 0;
  auto stage = [&](auto P, bool last) {
    constexpr int par = decltype(P)::value;
    unsigned nslot = slot + STG; if (nslot == NST * STG) nslot = 0;
#pragma unroll
    for (int h = 0; h < KH; ++h) {
      const int c = (par + h) & 1;
      if (h == KH - 1) { if (!last) { lg_bar(); body(R[c], R[c ^ 1], nslot, 0, true); } else body(R[c], R[c ^ 1], 0, 0, false); }
      else body(R[c], R[c ^ 1], slot, h + 1, true);
    }
    slot = nslot;
  };
  if constexpr (KH % 2 == 0) {
    for (int k = 0; k < NK - 1; ++k) stage(IC(0), false);
    stage(IC(0), true);
  } else {
    static_assert(NK % 2 == 0, "");
    for (int k = 0; k < NK - 2; k += 2) { stage(IC(0), false); stage(IC(1), false); }
    stage(IC(0), false); stage(IC(1), true);
  }
}

struct TrUnit { u32x2 v[8]; };
DEVI void tr_load(TrUnit& t, const u16* W, int N, int k0, int n0, int lane) {
  const unsigned vo = (unsigned)(((8 * (lane >> 4)) * N + 4 * (lane & 15)) * 2);
  const char* b = (const char*)(W + (size_t)k0 * N + n0);
#pragma unroll
  for (int i = 0; i < 8; ++i) asm volatile("global_load_dwordx2 %0, %1, %2" : "=v"(t.v[i]) : "v"(vo), "s"(b + (size_t)i * N * 2));
}
DEVI void tr_load1(TrUnit& t, int i, const u16* W, int N, int k0, int n0, int lane) {
  const unsigned vo = (unsigned)(((8 * (lane >> 4)) * N + 4 * (lane & 15)) * 2);
  asm volatile("global_load_dwordx2 %0, %1, %2" : "=v"(t.v[i]) : "v"(vo), "s"((const char*)(W + (size_t)(k0 + i) * N + n0)));
}
DEVI void tr_wait(TrUnit& t) {
  asm volatile("s_waitcnt vmcnt(0)" : "+v"(t.v[0]), "+v"(t.v[1]), "+v"(t.v[2]), "+v"(t.v[3]), "+v"(t.v[4]), "+v"(t.v[5]), "+v"(t.v[6]), "+v"(t.v[7])::"memory");
}
DEVI void tr_store(const TrUnit& t, u16* WT, int K, int k0, int n0, int lane) {
  const int src = (((lane & 3) << 4) | (lane >> 2)) << 2;
  u16* p = WT + (size_t)(n0 + 4 * (lane >> 2)) * K + k0 + 8 * (lane & 3);
  u32x4 o;
#define BP(v_) (unsigned)__builtin_amdgcn_ds_bpermute(src, (int)(v_))
#define TRP(W_, SEL) o.x = BP(__builtin_amdgcn_perm(t.v[1].W_, t.v[0].W_, SEL)); o.y = BP(__builtin_amdgcn_perm(t.v[3].W_, t.v[2].W_, SEL)); \
                     o.z = BP(__builtin_amdgcn_perm(t.v[5].W_, t.v[4].W_, SEL)); o.w = BP(__builtin_amdgcn_perm(t.v[7].W_, t.v[6].W_, SEL));
  TRP(x, 0x05040100u) gstore16c(p, o);
  TRP(x, 0x07060302u) gstore16c(p + K, o);
  TRP(y, 0x05040100u) gstore16c(p + 2 * K, o);
  TRP(y, 0x07060302u) gstore16c(p + 3 * K, o);
#undef TRP
#undef BP
}
DEVI float fadd_(float a, float b) { float r; asm("v_add_f32 %0, %1, %2" : "=v"(r) : "v"(a), "v"(b)); return r; }
template <int H> DEVI void tr_colsum(const TrUnit& t, float (&c)[4]) {
#pragma unroll
  for (int i = 4 * H; i < 4 * H + 4; ++i) {
    c[0] = fadd_(c[0], bflo(t.v[i].x)); c[1] = fadd_(c[1], bfhi(t.v[i].x)); c[2] = fadd_(c[2], bflo(t.v[i].y)); c[3] = fadd_(c[3], bfhi(t.v[i].y)); }
  if constexpr (H == 1) {
#pragma unroll
    for (int i = 0; i < 4; ++i) { c[i] = fadd_(c[i], __shfl_xor(c[i], 16)); c[i] = fadd_(c[i], __shfl_xor(c[i], 32)); }
  }
}
struct TrJob { const u16* src; u16* dst; int lds, ldd, k0, n0, cso; bool ok, cs; };
DEVI TrJob tr_job(int u, const u16* Wg, const u16* Wu, const u16* Wd, u16* WguT, u16* WdT) {
  TrJob j; j.ok = u < 1440; if (!j.ok) u = 1439;
  const int w = u / 480, r = u - w * 480;
  j.cs = w < 2;
  if (w < 2) { const int kb = r / 24; j.src = w ? Wu : Wg; j.dst = WguT + (size_t)w * FF * DM; j.lds = FF; j.ldd = DM; j.k0 = kb * 32; j.n0 = (r - kb * 24) * 64; j.cso = kb * 2 * FF + w * FF + j.n0; }
  else { const int kb = r / 10; j.src = Wd; j.dst = WdT; j.lds = DM; j.ldd = FF; j.k0 = kb * 32; j.n0 = (r - kb * 10) * 64; j.cso = 0; }
  return j;
}

#define K1_PRE 3
#define K2_PRE 2
#define K3_PRE 2
#define K4_PRE 2
using C1 = Cfg<128, 80, 4, 1, 1, 64, 6, 10, K1_PRE>;
using C2 = Cfg<80, 128, 1, 2, 2, 64, 6, 32, K2_PRE>;
using C3 = Cfg<192, 128, 2, 2, 1, 64, 4, 10, K3_PRE>;
using C4 = Cfg<80, 64, 1, 2, 2, 128, 4, 12, K4_PRE>;
constexpr int LDS_BYTES = 160 * 1024;
constexpr size_t WS_ABT = 0, WS_WGU = 6u << 20, WS_WD = 10u << 20, WS_ACT = 12u << 20, WS_P = 18u << 20, WS_CSP = 24u << 20, WS_SYNC = 25u << 20;

template <int W> DEVI void k1_consumer(char* shm, float* wsum, char* stg, int lane, float sgn ) {
  using C = C1;
  f32x4 acc[2][5];
  f32x4 sx[2] = {{0, 0, 0, 0}, {0, 0, 0, 0}}, sq[2] = {{0, 0, 0, 0}, {0, 0, 0, 0}}, sw = {0, 0, 0, 0}, sw4 = {0, 0, 0, 0};
  bf16x8 ones = {0x3f80, 0x3f80, 0x3f80, 0x3f80, 0x3f80, 0x3f80, 0x3f80, 0x3f80};
  asm volatile("" : "+v"(ones));
  consumer<C>(acc, shm, W, lane, [&](bf16x8 (&R)[7]) {
    sx[0] = MFMA(R[0], ones, sx[0]); sx[1] = MFMA(R[1], ones, sx[1]);
    sq[0] = MFMA(R[0], R[0], sq[0]); sq[1] = MFMA(R[1], R[1], sq[1]);
    sw = MFMA(ones, R[2 + W], sw);
    if constexpr (W == 0) sw4 = MFMA(ones, R[6], sw4);
  } );
    const int fr = lane & 15, fq = lane >> 4;
  if (lane < 16) { wsum[W * 16 + lane] = sw[0]; if constexpr (W == 0) wsum[64 + lane] = sw4[0]; }
  f32x4 rs[2], mr[2];
#pragma unroll
  for (int a = 0; a < 2; ++a)
#pragma unroll
    for (int r = 0; r < 4; ++r) {
      float s2 = __shfl(sq[a][r], 20 * fq + r);
      float mean = sx[a][r] * (1.0f / DM);
      float var = fmaxf(s2 * (1.0f / DM) - mean * mean, 0.0f);
      float rstd = sgn * __builtin_amdgcn_rsqf(var + 1e-5f);
      rs[a][r] = rstd; mr[a][r] = mean * rstd;
    }
  lg_bar();
  #pragma unroll
  for (int b = 0; b < 5; ++b) {
    float w = wsum[b * 16 + fr];
    f32x4 v0 = acc[0][b] * rs[0] - w * mr[0], v1 = acc[1][b] * rs[1] - w * mr[1];
    u32x4 o; o.x = pack2(v0[0], v0[1]); o.y = pack2(v0[2], v0[3]); o.z = pack2(v1[0], v1[1]); o.w = pack2(v1[2], v1[3]);
    *(u32x4*)(stg + (b * 16 + fr) * 272 + W * 64 + fq * 16) = o;
  }
}
DEVI u32x4 add8(f32x4 a0, f32x4 a1, u32x4 r) {
  u32x4 o;
  o.x = pack2(a0[0] + bflo(r.x), a0[1] + bfhi(r.x)); o.y = pack2(a0[2] + bflo(r.y), a0[3] + bfhi(r.y));
  o.z = pack2(a1[0] + bflo(r.z), a1[1] + bfhi(r.z)); o.w = pack2(a1[2] + bflo(r.w), a1[3] + bfhi(r.w));
  return o;
}
DEVI u32x4 addbf8(u32x4 a, u32x4 b) {
  u32x4 o;
  o.x = pack2(bflo(a.x) + bflo(b.x), bfhi(a.x) + bfhi(b.x)); o.y = pack2(bflo(a.y) + bflo(b.y), bfhi(a.y) + bfhi(b.y));
  o.z = pack2(bflo(a.z) + bflo(b.z), bfhi(a.z) + bfhi(b.z)); o.w = pack2(bflo(a.w) + bflo(b.w), bfhi(a.w) + bfhi(b.w));
  return o;
}

DEVI float fadd1(float a, float b) { float r; asm("v_add_f32 %0, %1, %2" : "=v"(r) : "v"(a), "v"(b)); return r; }
DEVI void facc1(float& s, float a) { asm("v_add_f32 %0, %0, %1" : "+v"(s) : "v"(a)); }
DEVI void fsq1(float& s, float a) { asm("v_fmac_f32 %0, %1, %1" : "+v"(s) : "v"(a)); }
DEVI void gload16c(u32x4& d, unsigned voff, const char* sbase) { asm volatile("global_load_dwordx4 %0, %1, %2 sc1" : "=v"(d) : "v"(voff), "s"(sbase)); }
template <int WA> DEVI void k3_consumer(char* shm, f32x2* rowst, float* lcs, char* stg, float cpart, int wid, int lane ) {
  using C = C3;
  const int fr = lane & 15, fq = lane >> 4, wb = wid & 1;
  f32x4 acc[6][4];
  f32x4 sh[2] = {{0, 0, 0, 0}, {0, 0, 0, 0}}, sq[2] = {{0, 0, 0, 0}, {0, 0, 0, 0}};
  bf16x8 ones = {0x3f80, 0x3f80, 0x3f80, 0x3f80, 0x3f80, 0x3f80, 0x3f80, 0x3f80};
  asm volatile("" : "+v"(ones));
  consumer<C>(acc, shm, wid, lane, [&](bf16x8 (&R)[10]) {
    sh[0] = MFMA(ones, R[6 + 2 * WA], sh[0]); sq[0] = MFMA(R[6 + 2 * WA], R[6 + 2 * WA], sq[0]);
    sh[1] = MFMA(ones, R[7 + 2 * WA], sh[1]); sq[1] = MFMA(R[7 + 2 * WA], R[7 + 2 * WA], sq[1]);
  } );
  #pragma unroll
  for (int i = 0; i < 2; ++i) {
    const float d01 = (fr & 1) ? sq[i][1] : sq[i][0], d23 = (fr & 1) ? sq[i][3] : sq[i][2], d = (fr & 2) ? d23 : d01;
    const float mean = sh[i][0] * (1.0f / DM);
    const float var = fmaxf(d * (1.0f / DM) - mean * mean, 0.0f);
    if (fq == (fr >> 2)) rowst[(wb * 4 + 2 * WA + i) * 16 + fr] = f32x2{-mean, var + 1e-5f};
  }
  if (wid < 3) lcs[wid * 64 + lane] = cpart;
  lg_bar();
  f32x4 cs[6];
#pragma unroll
  for (int j = 0; j < 6; ++j) cs[j] = *(const f32x4*)(lcs + WA * 96 + j * 16 + 4 * fq);
#pragma unroll
  for (int n = 0; n < 4; ++n) {
    const int ml = (wb * 4 + n) * 16 + fr;
    const f32x2 st = rowst[ml];
    const float nmean = st[0], ve = st[1];
    const float rstd = __builtin_amdgcn_rsqf(ve);
    const float k1 = -2.3022081985f * rstd;
    const float k2 = k1 * 0.044715f * rstd * rstd;
    f32x4 G[3], U[3], z[3];
#pragma unroll
    for (int p = 0; p < 3; ++p) G[p] = cs[2 * p] * nmean + acc[2 * p][n];
#pragma unroll
    for (int p = 0; p < 3; ++p) z[p] = G[p] * G[p];
#pragma unroll
    for (int p = 0; p < 3; ++p) U[p] = cs[2 * p + 1] * nmean + acc[2 * p + 1][n];
#pragma unroll
    for (int p = 0; p < 3; ++p) z[p] = z[p] * k2 + k1;
#pragma unroll
    for (int p = 0; p < 3; ++p) z[p] = z[p] * G[p];
#pragma unroll
    for (int p = 0; p < 3; ++p) { z[p][0] = __builtin_amdgcn_exp2f(z[p][0]); z[p][1] = __builtin_amdgcn_exp2f(z[p][1]); z[p][2] = __builtin_amdgcn_exp2f(z[p][2]); z[p][3] = __builtin_amdgcn_exp2f(z[p][3]); U[p] = U[p] * G[p]; }
#pragma unroll
    for (int p = 0; p < 3; ++p) z[p] = z[p] * ve + ve;
#pragma unroll
    for (int p = 0; p < 3; ++p) { z[p][0] = __builtin_amdgcn_rcpf(z[p][0]); z[p][1] = __builtin_amdgcn_rcpf(z[p][1]); z[p][2] = __builtin_amdgcn_rcpf(z[p][2]); z[p][3] = __builtin_amdgcn_rcpf(z[p][3]); }
#pragma unroll
    for (int p = 0; p < 3; ++p) {
      f32x4 v = U[p] * z[p];
      u32x2 o; o.x = pack2(v[0], v[1]); o.y = pack2(v[2], v[3]);
      *(u32x2*)(stg + ml * 208 + (WA * 48 + p * 16 + fq * 4) * 2) = o;
    }
  }
}
DEVI void flag_wait_all(const u64* f, u64 tag, int lane) {
  for (unsigned spin = 0; spin < (1u << 16); ++spin) {
    bool ok = true;
#pragma unroll
    for (int i = 0; i < 4; ++i) ok &= __hip_atomic_load(f + i * 64 + lane, __ATOMIC_RELAXED, __HIP_MEMORY_SCOPE_AGENT) == tag;
    if (__all(ok)) break;
    __builtin_amdgcn_s_sleep(2);
  }
  asm volatile("" ::: "memory");
}
DEVI void drain_bar() { asm volatile("s_waitcnt vmcnt(0) lgkmcnt(0)\n\ts_barrier" ::: "memory"); }

__global__ __launch_bounds__(512, 2) void fnet_geglu_fused(const u16* __restrict__ x, const u16* __restrict__ Cr, const u16* __restrict__ Ci,
                                                           const u16* __restrict__ Wre, const u16* __restrict__ Wim, const u16* __restrict__ Wg,
                                                           const u16* __restrict__ Wu, const u16* __restrict__ Wd, char* ws, u16* __restrict__ out) {
  extern __shared__ __attribute__((aligned(16))) char shm[];
  const int bid = blockIdx.x, xcd = bid & 7, idx = bid >> 3;
  const int wid = __builtin_amdgcn_readfirstlane(threadIdx.x >> 6), lane = threadIdx.x & 63;
  u16* abT = (u16*)(ws + WS_ABT); u16* WguT = (u16*)(ws + WS_WGU); u16* WdT = (u16*)(ws + WS_WD); u16* act = (u16*)(ws + WS_ACT);
  u16* P = (u16*)(ws + WS_P); float* csp = (float*)(ws + WS_CSP);
  u64* gen = (u64*)(ws + WS_SYNC); u64* flags1 = gen + 64; u64* flags2 = flags1 + 256; u64* flags3 = flags2 + 256;
  u64 tag;
  asm volatile("s_load_dwordx2 %0, %1, 0x0" : "=s"(tag) : "s"(gen));

  {
    using C = C1;
    const int ts = (xcd >> 1) * 4 + (idx >> 3), te = (xcd & 1) * 8 + (idx & 7), half = te >> 3;
    constexpr int FREE = ((C::LASTSLOT + 1) % C::NST) * C::STG;
    float* wsum = (float*)(shm + FREE);
    char* stg = shm + FREE + 1024;
    if (__builtin_expect(wid >= 4, 1)) {
      const int lw = wid - 4, j = lane >> 3;
      const char* Wb = (const char*)(half ? Wim : Wre) + (size_t)(te & 7) * 80 * DM * 2;
      const char* xb = (const char*)x + (size_t)ts * 128 * DM * 2;
      auto sA = [](int p) { return (unsigned)((32 * (p >> 2) + 16 * (p & 1) + 4 * ((p >> 1) & 1)) * DM * 2); };
      auto sB = [](int p) { return (unsigned)(p * 8 * DM * 2); };
      auto ptrs = [&](int k, const char*& pa, const char*& pb) { pa = xb + k * 128; pb = Wb + k * 128; };
      const unsigned vA = (unsigned)((8 * (j >> 2) + (j & 3)) * DM * 2), vB = (unsigned)(j * DM * 2);
      TrUnit tu[2];
      const TrJob j0 = tr_job(bid * 4 + lw, Wg, Wu, Wd, WguT, WdT), j1 = tr_job(1024 + bid * 4 + lw, Wg, Wu, Wd, WguT, WdT);
      const char* cp = (const char*)((bid & 1) ? Ci : Cr) + (size_t)(bid >> 1) * 65536 + lw * 16384;
      auto extra = [&](auto I) {
        constexpr int i = decltype(I)::value;
        if constexpr (i < 8) tr_load1(tu[0], i, j0.src, j0.lds, j0.k0, j0.n0, lane);
        else if constexpr (i < 16) tr_load1(tu[1], i - 8, j1.src, j1.lds, j1.k0, j1.n0, lane);
        else glds16(lane * 16 + ((i - 16) & 3) * 4096 + ((i - 16) >> 2) * 1024, cp, C::LDS);
      };
      if (wid == 4) loader_x<C, 0, 4, 0, 3, 4, 32>(lane, sA, vA, sB, vB, ptrs, extra);
      else if (wid == 5) loader_x<C, 4, 4, 3, 3, 4, 32>(lane, sA, vA, sB, vB, ptrs, extra);
      else if (wid == 6) loader_x<C, 8, 4, 6, 2, 4, 32>(lane, sA, vA, sB, vB, ptrs, extra);
      else loader_x<C, 12, 4, 8, 2, 4, 32>(lane, sA, vA, sB, vB, ptrs, extra);
      tr_wait(tu[0]); tr_wait(tu[1]);
#pragma unroll
      for (int q = 0; q < 2; ++q) {
        const TrJob& jb = q ? j1 : j0;
        if (jb.ok) {
          tr_store(tu[q], jb.dst, jb.ldd, jb.k0, jb.n0, lane);
          if (jb.cs) {
            float c[4] = {0.f, 0.f, 0.f, 0.f}; tr_colsum<0>(tu[q], c); tr_colsum<1>(tu[q], c);
            if (lane < 16) gstore16c(csp + jb.cso + 4 * lane, __builtin_bit_cast(u32x4, f32x4{c[0], c[1], c[2], c[3]}));
          }
        }
        if (q == 0) lg_bar();
      }
    } else {
      const float sgn = half ? -1.0f : 1.0f;
      if (wid == 0) k1_consumer<0>(shm, wsum, stg, lane, sgn );
      else if (wid == 1) k1_consumer<1>(shm, wsum, stg, lane, sgn );
      else if (wid == 2) k1_consumer<2>(shm, wsum, stg, lane, sgn );
      else k1_consumer<3>(shm, wsum, stg, lane, sgn );
    }
    lg_bar();
    u16* ob = abT + (size_t)(te & 7) * 80 * 4096 + half * 2048 + ts * 128;
#pragma unroll
    for (int i = 0; i < 3; ++i) {
      int c = threadIdx.x + i * 512;
      if (i < 2 || c < 1280) { int row = c >> 4, ch = c & 15; gstore16c(ob + (size_t)row * 4096 + ch * 8, *(const u32x4*)(stg + row * 272 + ch * 16)); }
    }
    asm volatile("s_waitcnt vmcnt(0) lgkmcnt(0)\n\ts_barrier" : "+s"(tag)::"memory");
    tag += 1;
    if (threadIdx.x == 0) flag_set(flags1 + te * 16 + ts, tag);
  }
  {
    using C = C2;
    const int half = xcd & 1, tt = (xcd >> 1) * 4 + (idx >> 3), te = idx & 7;
    static_assert((C::NST - 1 - C::LASTSLOT) * C::STG >= 2 * 128 * 336, "");
    char* red = shm + (C::LASTSLOT + 1) * C::STG;
    u32x4 rx[3] = {{0, 0, 0, 0}, {0, 0, 0, 0}, {0, 0, 0, 0}};
    auto xload = [&]() {
      if (half == 0) {
#pragma unroll
        for (int i = 0; i < 3; ++i) { int c = threadIdx.x + i * 512; if (c > 1279) c = 1279;
          rx[i] = *(const u32x4*)(x + (size_t)(tt * 128 + c / 10) * DM + te * 80 + (c % 10) * 8); }
      }
    };
    if (wid >= 4) {
      const int j = lane >> 3;
      const char* ab = (const char*)abT + (size_t)te * 80 * 8192 + half * 4096;
      const char* cc = (const char*)(half ? Ci : Cr) + (size_t)tt * 128 * 4096;
      auto sA = [](int p) { return (unsigned)(p * 8 * 8192); };
      auto sB = [](int p) { return (unsigned)(p * 8 * 4096); };
      auto ptrs = [&](int k, const char*& pa, const char*& pb) { pa = ab + k * 128; pb = cc + k * 128; };
      const unsigned vA = (unsigned)(j * 8192), vB = (unsigned)(j * 4096);
      if (wid == 4) loader_dep<C, 1, 0, 3, 0, 4>(lane, sA, vA, sB, vB, ptrs);
      else if (wid == 5) loader_dep<C, 1, 3, 3, 4, 4>(lane, sA, vA, sB, vB, ptrs);
      else if (wid == 6) loader_dep<C, 1, 6, 2, 8, 4>(lane, sA, vA, sB, vB, ptrs);
      else loader_dep<C, 1, 8, 2, 12, 4>(lane, sA, vA, sB, vB, ptrs);
      xload();
      if (wid == 4) flag_wait_all(flags1, tag, lane);
    } else {
      if (wid == 0) flag_wait16(flags1 + (half * 8 + te) * 16, tag, lane);
      bar();
      xload();
      f32x4 acc[5][4];
      consumer<C>(acc, shm, wid, lane, [](auto&) {} );
      const int fr = lane & 15, fq = lane >> 4, kg = wid >> 1, wb = wid & 1;
#pragma unroll
      for (int n = 0; n < 4; ++n)
#pragma unroll
        for (int j = 0; j < 5; ++j) *(f32x4*)(red + kg * (128 * 336) + ((wb * 4 + n) * 16 + fr) * 336 + j * 64 + fq * 16) = acc[j][n];
    }
    lg_bar();
    u16* op = P + (size_t)half * SEQ * DM + (size_t)tt * 128 * DM + te * 80;
#pragma unroll
    for (int i = 0; i < 3; ++i) {
      const int c = threadIdx.x + i * 512;
      if (i < 2 || c < 1280) {
        const int row = c / 10, ch = c % 10;
        const char* p = red + row * 336 + ch * 32;
        f32x4 a0 = *(const f32x4*)p + *(const f32x4*)(p + 128 * 336), a1 = *(const f32x4*)(p + 16) + *(const f32x4*)(p + 16 + 128 * 336);
        gstore16c(op + (size_t)row * DM + ch * 8, add8(a0, a1, rx[i]));
      }
    }
    drain_bar();
    if (threadIdx.x == 0) flag_set(flags2 + tt * 16 + half * 8 + te, tag);
  }
  {
    using C = C3;
    const int tm = (xcd >> 2) * 8 + (idx >> 2), tn = (xcd & 3) * 4 + (idx & 3);
    constexpr int FREE = ((C::LASTSLOT + 1) % C::NST) * C::STG;
    f32x2* rowst = (f32x2*)(shm + FREE);
    float* lcs = (float*)(shm + FREE + 1024);
    char* stg = shm + FREE + 4096;
    if (wid >= 4) {
      const int lw = wid - 4, j = lane >> 3;
      const char* wbp = (const char*)WguT + (size_t)tn * 96 * DM * 2;
      const char* p0 = (const char*)P + (size_t)(tm * 128 + lw * 32) * DM * 2;
      const char* p1 = p0 + (size_t)SEQ * DM * 2;
      const unsigned sw0 = (unsigned)(((lane & 7) ^ (lane >> 4)) * 16);
      unsigned vw[6];
#pragma unroll
      for (int i = 0; i < 6; ++i) { const int p = lw * 6 + i;
        vw[i] = (unsigned)(j * DM * 2) + (sw0 ^ (unsigned)((p & 1) * 64)) + (unsigned)((((p >> 1) & 1) * FF + 16 * (p >> 2) + 8 * (p & 1)) * DM * 2); }
      const unsigned vh = (unsigned)(j * DM * 2 + (lane & 7) * 16);
      char* hw = shm + (24 + lw * 4) * 1024 + j * 128;
      u32x4 buf[3][8];
      auto vm1 = [&](auto S, auto Q) {
        constexpr int s = decltype(S)::value, q = decltype(Q)::value;
        if constexpr (q < 8) gload16c(buf[s % 3][q], vh + (q >> 1) * 8 * DM * 2, ((q & 1) ? p1 : p0) + s * 128);
        else glds16c(vw[q - 8], wbp + s * 128, (s % C::NST) * C::STG + (lw * 6 + q - 8) * 1024);
      };
      static_for<0, C::NST>([&](auto S) { static_for<8, 14>([&](auto Q) { vm1(S, Q); }); });
      bar();
      struct N { static constexpr int n(int s) { return s < C3::NST ? 8 : 14; }
                 static constexpr int sum(int a, int b) { int t = 0; for (int s = a; s <= b; ++s) t += n(s); return t; }
                 static constexpr int st(int s0, int o) { int s = s0; while (o >= n(s)) { o -= n(s); ++s; } return s; }
                 static constexpr int qq(int s0, int o) { int s = s0; while (o >= n(s)) { o -= n(s); ++s; } return o; } };
      static_for<-1, C::NK - 1>([&](auto K) {
        constexpr int k = decltype(K)::value;
        constexpr int c = k + 1;
        constexpr int s0 = C::issued(k - 1) + 1, nop = N::sum(s0, C::issued(k));
        auto ops = [&](auto B, auto E) { static_for<decltype(B)::value, decltype(E)::value>([&](auto O) {
          constexpr int o = decltype(O)::value; vm1(IC(N::st(s0, o)), IC(N::qq(s0, o))); }); };
        if constexpr (k == -1) ops(IC(0), IC(nop));
        u32x4 (&b)[8] = buf[c % 3];
        asm volatile("s_waitcnt vmcnt(%8)" : "+v"(b[0]), "+v"(b[1]), "+v"(b[2]), "+v"(b[3]), "+v"(b[4]), "+v"(b[5]), "+v"(b[6]), "+v"(b[7])
                     : "i"(N::n(c) - 8 + N::sum(c + 1, k == -1 ? C::issued(k) : C::issued(k - 1))) : "memory");
        static_for<0, 4>([&](auto I) {
          constexpr int i = decltype(I)::value;
          if constexpr (k >= 0) { ops(IC(nop * i / 4), IC(nop * (i + 1) / 4)); SB(); }
          const u32x4 u = b[2 * i], v = b[2 * i + 1];
          float hh[8];
          hh[0] = fadd1(bflo(u.x), bflo(v.x)); hh[1] = fadd1(bfhi(u.x), bfhi(v.x)); hh[2] = fadd1(bflo(u.y), bflo(v.y)); hh[3] = fadd1(bfhi(u.y), bfhi(v.y));
          hh[4] = fadd1(bflo(u.z), bflo(v.z)); hh[5] = fadd1(bfhi(u.z), bfhi(v.z)); hh[6] = fadd1(bflo(u.w), bflo(v.w)); hh[7] = fadd1(bfhi(u.w), bfhi(v.w));
          u32x4 o; o.x = pack2(hh[0], hh[1]); o.y = pack2(hh[2], hh[3]); o.z = pack2(hh[4], hh[5]); o.w = pack2(hh[6], hh[7]);
          *(u32x4*)(hw + (c % C::NST) * C::STG + i * 1024 + (sw0 ^ (unsigned)((i & 1) * 64))) = o;
          SB();
        });
        asm volatile("s_waitcnt vmcnt(%0) lgkmcnt(0)\n\ts_barrier" ::"i"(N::sum(c + 1, C::issued(k))) : "memory");
      });
      lg_bar();
    } else {
      float cpart = 0;
      if (wid < 3) {
        const int r = wid * 64 + lane;
        const float* cq = csp + ((r >> 4) & 1) * FF + tn * 96 + 16 * (r >> 5) + (r & 15);
        float t[20];
#pragma unroll
        for (int i = 0; i < 20; ++i) t[i] = __hip_atomic_load(cq + i * 2 * FF, __ATOMIC_RELAXED, __HIP_MEMORY_SCOPE_AGENT);
#pragma unroll
        for (int i = 0; i < 20; ++i) cpart += t[i];
        asm volatile("" : "+v"(cpart));
      } else {
        flag_wait16(flags2 + tm * 16, tag, lane);
      }
      bar();
      if (bid == 0 && threadIdx.x == 0) *(volatile u64*)gen = tag;
      if (wid < 2) k3_consumer<0>(shm, rowst, lcs, stg, cpart, wid, lane );
      else k3_consumer<1>(shm, rowst, lcs, stg, cpart, wid, lane );
    }
    lg_bar();
    u16* ob = act + (size_t)tm * 128 * FF + tn * 96;
#pragma unroll
    for (int i = 0; i < 3; ++i) {
      int c = threadIdx.x + i * 512, row = c / 12, ch = c % 12;
      gstore16c(ob + (size_t)row * FF + ch * 8, *(const u32x4*)(stg + row * 208 + ch * 16));
    }
    drain_bar();
    if (threadIdx.x == 0) flag_set(flags3 + tm * 16 + tn, tag);
  }
  {
    using C = C4;
    const int tm4 = (xcd >> 1) * 8 + (idx >> 2), td = (xcd & 1) * 4 + (idx & 3);
    static_assert(C::LASTSLOT == C::NST - 1 && (C::NST - 1) * C::STG >= 2 * 64 * 336, "");
    u32x4 ra[2], rb[2];
    auto rload = [&]() {
#pragma unroll
      for (int i = 0; i < 2; ++i) { int c = threadIdx.x + i * 512; if (c > 639) c = 639;
        const unsigned o = (unsigned)(((tm4 * 64 + c / 10) * DM + td * 80 + (c % 10) * 8) * 2);
        gload16c(ra[i], o, (const char*)P); gload16c(rb[i], o, (const char*)P + (size_t)SEQ * DM * 2); }
    };
    if (wid >= 4) {
      const char* wp = (const char*)WdT + (size_t)td * 80 * FF * 2;
      const char* ap = (const char*)act + (size_t)tm4 * 64 * FF * 2;
      const unsigned v = (unsigned)((lane >> 3) * FF * 2);
      auto sA = [](int p) { return (unsigned)(p * 8 * FF * 2); };
      auto ptrs = [&](int k, const char*& pa, const char*& pb) { pa = wp + k * 256; pb = ap + k * 256; };
      if (wid == 4) loader_dep<C, 2, 0, 5, 0, 4>(lane, sA, v, sA, v, ptrs);
      else if (wid == 5) loader_dep<C, 2, 5, 5, 4, 4>(lane, sA, v, sA, v, ptrs);
      else if (wid == 6) loader_dep<C, 2, 10, 5, 8, 4>(lane, sA, v, sA, v, ptrs);
      else loader_dep<C, 2, 15, 5, 12, 4>(lane, sA, v, sA, v, ptrs);
      rload();
    } else {
      if (wid == 0) flag_wait16(flags3 + (tm4 >> 1) * 16, tag, lane);
      bar();
      rload();
      f32x4 acc[5][2];
      consumer<C>(acc, shm, wid, lane, [](auto&) {} );
      const int fr = lane & 15, fq = lane >> 4, kg = wid >> 1, wb = wid & 1;
#pragma unroll
      for (int n = 0; n < 2; ++n)
#pragma unroll
        for (int j = 0; j < 5; ++j) *(f32x4*)(shm + kg * (64 * 336) + ((wb * 2 + n) * 16 + fr) * 336 + j * 64 + fq * 16) = acc[j][n];
    }
    asm volatile("s_waitcnt vmcnt(0) lgkmcnt(0)\n\ts_barrier" : "+v"(ra[0]), "+v"(ra[1]), "+v"(rb[0]), "+v"(rb[1])::"memory");
    u16* op = out + (size_t)tm4 * 64 * DM + td * 80;
#pragma unroll
    for (int i = 0; i < 2; ++i) {
      const int c = threadIdx.x + i * 512;
      if (i == 0 || c < 640) {
        const int row = c / 10, ch = c % 10;
        const char* p = shm + row * 336 + ch * 32;
        f32x4 a0 = *(const f32x4*)p + *(const f32x4*)(p + 64 * 336), a1 = *(const f32x4*)(p + 16) + *(const f32x4*)(p + 16 + 64 * 336);
        *(u32x4*)(op + (size_t)row * DM + ch * 8) = add8(a0, a1, addbf8(ra[i], rb[i]));
      }
    }
  }
}

static void launch_stage(int which, void* const* d_in, void* d_out, void* d_ws, hipStream_t stream) {
  if (which != 0) return;
  (void)hipFuncSetAttribute((const void*)fnet_geglu_fused, hipFuncAttributeMaxDynamicSharedMemorySize, LDS_BYTES);
  fnet_geglu_fused<<<256, 512, LDS_BYTES, stream>>>((const u16*)d_in[0], (const u16*)d_in[1], (const u16*)d_in[2], (const u16*)d_in[3],
                                                    (const u16*)d_in[4], (const u16*)d_in[6], (const u16*)d_in[7], (const u16*)d_in[8],
                                                    (char*)d_ws, (u16*)d_out);
}

extern "C" void kernel_launch(void* const* d_in, const int* in_sizes, int n_in, void* d_out, int out_size, void* d_ws, size_t ws_size,
                              hipStream_t stream) {
  launch_stage(0, d_in, d_out, d_ws, stream);
}
```

```cpp
#define TLBG 65536u
#include <hip/hip_runtime.h>
#include <hip/hip_bf16.h>
#include <stdint.h>
#include <type_traits>

typedef unsigned short u16;
typedef __attribute__((ext_vector_type(8))) short bf16x8;
typedef __attribute__((ext_vector_type(4))) float f32x4;
typedef __attribute__((ext_vector_type(2))) float f32x2;
typedef __attribute__((ext_vector_type(2))) __bf16 bf16x2;
typedef __attribute__((ext_vector_type(2))) unsigned u32x2;
typedef __attribute__((ext_vector_type(4))) unsigned u32x4;

#define DEVI __device__ __forceinline__
#define IC(x) std::integral_constant<int, (x)>{}
#define SB() __builtin_amdgcn_sched_barrier(0)
#define MFMA(a, b, c) __builtin_amdgcn_mfma_f32_16x16x32_bf16(a, b, c, 0, 0, 0)

constexpr int SEQ = 2048, DM = 640, FF = 1536;

template <int B, int E, class F> DEVI void static_for(F&& f) {
  if constexpr (B < E) { f(IC(B)); static_for<B + 1, E>(f); }
}
DEVI void glds16(unsigned voff, const char* sbase, unsigned ldsaddr) {
  asm volatile("s_mov_b32 m0, %2\n\ts_nop 0\n\tglobal_load_lds_dwordx4 %0, %1" ::"v"(voff), "s"(sbase), "s"(ldsaddr));
}
template <int N> DEVI void waitvm_bar() { asm volatile("s_waitcnt vmcnt(%0)\n\ts_barrier" ::"i"(N) : "memory"); }
DEVI void bar() { asm volatile("s_barrier" ::: "memory"); }
DEVI void touch(unsigned& sink, unsigned voff, const void* sbase) { asm volatile("global_load_dword %0, %1, %2" : "+v"(sink) : "v"(voff), "s"(sbase)); }
DEVI void touch_done(unsigned& sink) { asm volatile("s_waitcnt vmcnt(0)" : "+v"(sink)::"memory"); }
DEVI void lg_bar() { asm volatile("s_waitcnt lgkmcnt(0)\n\ts_barrier" ::: "memory"); }
DEVI unsigned pack2(float a, float b) { f32x2 p = {a, b}; return __builtin_bit_cast(unsigned, __builtin_convertvector(p, bf16x2)); }
DEVI float bflo(unsigned v) { return __builtin_bit_cast(float, v << 16); }
DEVI float bfhi(unsigned v) { return __builtin_bit_cast(float, v & 0xffff0000u); }
template <int CTRL> DEVI float dppf(float v) {
  return __builtin_bit_cast(float, __builtin_amdgcn_update_dpp(0, __builtin_bit_cast(int, v), CTRL, 0xf, 0xf, true));
}
DEVI float red16(float v) { v += dppf<0xB1>(v); v += dppf<0x4E>(v); v += dppf<0x141>(v); v += dppf<0x140>(v); return v; }

template <int TA_, int TB_, int NWA_, int NWB_, int KS_, int BK_, int NST_, int NK_, int PRE_> struct Cfg {
  static constexpr int TA = TA_, TB = TB_, NWA = NWA_, NWB = NWB_, KS = KS_, BK = BK_, NST = NST_, NK = NK_;
  static constexpr int NSUB = BK / 64, PA = TA / 8, PB = TB / 8, PPS = PA + PB, STG = NSUB * PPS * 1024, SUBB = PPS * 1024;
  static constexpr int WAF = TA / 16 / NWA, WBF = TB / 16 / NWB, KH = (BK / 32) / KS;
  static constexpr int NF = WAF + WBF, NMF = WAF * WBF;
  static constexpr int LDS = NST * STG;
  static constexpr int LASTSLOT = (NK - 1) % NST;
  static constexpr int issued(int k) {
    if (k < -1) return -1;
    int a = 2 * k + 1 + PRE_, b = k - 1 + NST, c = NK - 1;
    if (k == -1) b = NST - 1;
    int m = a < b ? a : b; return m < c ? m : c;
  }
  static constexpr int steady_begin() { int k = 0; while (!(issued(k) == k - 1 + NST && issued(k - 1) == k - 2 + NST) && k < NK) ++k; return k; }
  static constexpr int K0 = steady_begin() < NK - NST + 1 ? steady_begin() : NK - NST + 1;
  static constexpr int K1 = NK - NST + 1 > K0 ? NK - NST + 1 : K0;
};

template <class C, int GA, int GB, int EXTRA = 0, class SA, class SB_, class Ptrs, class Tail>
DEVI void loader(char* shm, int a0, int b0, int lane, SA sA, unsigned vA, SB_ sB, unsigned vB, Ptrs ptrs, Tail tail) {
  constexpr int G = GA + GB;
  const unsigned sw0 = (unsigned)(((lane & 7) ^ (lane >> 4)) * 16);
  unsigned voff[G], lo[G];
  const unsigned ldsb = (unsigned)(size_t)(__attribute__((address_space(3))) char*)shm;
#pragma unroll
  for (int i = 0; i < GA; ++i) {
    int p = a0 + i, sub = p / C::PA, pr = p - sub * C::PA;
    voff[i] = vA + (sw0 ^ (unsigned)((pr & 1) * 64)) + (sA(pr) + (unsigned)sub * 128u);
    lo[i] = ldsb + sub * C::SUBB + pr * 1024;
  }
#pragma unroll
  for (int i = 0; i < GB; ++i) {
    int p = b0 + i, sub = p / C::PB, pr = p - sub * C::PB;
    voff[GA + i] = vB + (sw0 ^ (unsigned)((pr & 1) * 64)) + (sB(pr) + (unsigned)sub * 128u);
    lo[GA + i] = ldsb + sub * C::SUBB + (C::PA + pr) * 1024;
  }
  auto issue = [&](int s, unsigned slot) {
    const char *pa, *pb; ptrs(s, pa, pb);
#pragma unroll
    for (int i = 0; i < G; ++i) glds16(voff[i], i < GA ? pa : pb, lo[i] + slot);
  };
  static_for<-1, C::K0>([&](auto K) {
    constexpr int k = decltype(K)::value;
    static_for<C::issued(k - 1) + 1, C::issued(k) + 1>([&](auto S) { constexpr int s = decltype(S)::value; issue(s, (s % C::NST) * C::STG); });
    waitvm_bar<(C::issued(k) - (k + 1)) * G>();
  });
  unsigned slot = ((C::K0 - 1 + C::NST) % C::NST) * C::STG;
  for (int k = C::K0; k < C::K1; ++k) {
    issue(k - 1 + C::NST, slot);
    slot += C::STG; if (slot == C::NST * C::STG) slot = 0;
    waitvm_bar<(C::NST - 2) * G>();
  }
  static_assert(C::issued(C::K1 - 1) == C::NK - 1, "ring fully issued before the tail");
  tail();
  static_for<C::K1, C::NK - 1>([&](auto K) { constexpr int k = decltype(K)::value; waitvm_bar<(C::NK - 2 - k) * G + EXTRA>(); });
}

template <class C, class Extra> DEVI void consumer(f32x4 (&acc)[C::WAF][C::WBF], char* shm, int wid, int lane, Extra extra ) {
  constexpr int WAF = C::WAF, WBF = C::WBF, KH = C::KH, NF = C::NF, NMF = C::NMF, STG = C::STG, NST = C::NST, NK = C::NK;
  const int kg = wid / (C::NWA * C::NWB), wab = wid % (C::NWA * C::NWB), wa = wab / C::NWB, wb = wab % C::NWB;
#pragma unroll
  for (int j = 0; j < WAF; ++j)
#pragma unroll
    for (int b = 0; b < WBF; ++b) acc[j][b] = f32x4{0, 0, 0, 0};
  const int fr = lane & 15, fq = lane >> 4;
  const int c0 = fr * 128 + ((fq ^ (fr >> 1)) * 16);
  int rda[KH], rdb[KH];
#pragma unroll
  for (int h = 0; h < KH; ++h) {
    int ks = kg * KH + h, o = (c0 ^ ((ks & 1) * 64)) + (ks >> 1) * C::SUBB;
    rda[h] = o + wa * WAF * 2048; rdb[h] = o + C::TA * 128 + wb * WBF * 2048;
  }
  bf16x8 R[2][NF];
  auto rd1 = [&](bf16x8 (&Rn)[NF], unsigned slot, int h, int f) {
    if (f < WAF) Rn[f] = *(const bf16x8*)(shm + slot + rda[h] + f * 2048);
    else Rn[f] = *(const bf16x8*)(shm + slot + rdb[h] + (f - WAF) * 2048);
  };
  auto body = [&](bf16x8 (&Rc)[NF], bf16x8 (&Rn)[NF], unsigned slot, int h, bool dor) {
    int ri = 0;
#pragma unroll
    for (int m = 0; m < NMF; ++m) {
      { int n = m / WAF, j = m % WAF; acc[j][n] = MFMA(Rc[j], Rc[WAF + n], acc[j][n]); }
      int rt = ((m + 1) * NF * 4 + 3 * NMF - 1) / (3 * NMF); if (rt > NF) rt = NF;
#pragma unroll
      for (; ri < rt; ++ri) if (dor) rd1(Rn, slot, h, ri);
      SB();
    }
    extra(Rc);
    SB();
  };
  lg_bar();
#pragma unroll
  for (int f = 0; f < NF; ++f) rd1(R[0], 0, 0, f);
  unsigned slot = 0;
  auto stage = [&](auto P, bool last) {
    constexpr int par = decltype(P)::value;
    unsigned nslot = slot + STG; if (nslot == NST * STG) nslot = 0;
#pragma unroll
    for (int h = 0; h < KH; ++h) {
      const int c = (par + h) & 1;
      if (h == KH - 1) { if (!last) { lg_bar(); body(R[c], R[c ^ 1], nslot, 0, true); } else body(R[c], R[c ^ 1], 0, 0, false); }
      else body(R[c], R[c ^ 1], slot, h + 1, true);
    }
    slot = nslot;
  };
  if constexpr (KH % 2 == 0) {
    for (int k = 0; k < NK - 1; ++k) stage(IC(0), false);
    stage(IC(0), true);
  } else {
    static_assert(NK % 2 == 0, "");
    for (int k = 0; k < NK - 2; k += 2) { stage(IC(0), false); stage(IC(1), false); }
    stage(IC(0), false); stage(IC(1), true);
  }
}

struct TrUnit { u32x2 v[8]; };
DEVI void tr_load(TrUnit& t, const u16* W, int N, int k0, int n0, int lane) {
  const unsigned vo = (unsigned)(((8 * (lane >> 4)) * N + 4 * (lane & 15)) * 2);
  const char* b = (const char*)(W + (size_t)k0 * N + n0);
#pragma unroll
  for (int i = 0; i < 8; ++i) asm volatile("global_load_dwordx2 %0, %1, %2" : "=v"(t.v[i]) : "v"(vo), "s"(b + (size_t)i * N * 2));
}
DEVI void tr_wait(TrUnit& t) {
  asm volatile("s_waitcnt vmcnt(0)" : "+v"(t.v[0]), "+v"(t.v[1]), "+v"(t.v[2]), "+v"(t.v[3]), "+v"(t.v[4]), "+v"(t.v[5]), "+v"(t.v[6]), "+v"(t.v[7])::"memory");
}
DEVI void tr_store(const TrUnit& t, u16* WT, int K, int k0, int n0, int lane) {
  const int src = (((lane & 3) << 4) | (lane >> 2)) << 2;
  u16* p = WT + (size_t)(n0 + 4 * (lane >> 2)) * K + k0 + 8 * (lane & 3);
  u32x4 o;
#define BP(v_) (unsigned)__builtin_amdgcn_ds_bpermute(src, (int)(v_))
#define TRP(W_, SEL) o.x = BP(__builtin_amdgcn_perm(t.v[1].W_, t.v[0].W_, SEL)); o.y = BP(__builtin_amdgcn_perm(t.v[3].W_, t.v[2].W_, SEL)); \
                     o.z = BP(__builtin_amdgcn_perm(t.v[5].W_, t.v[4].W_, SEL)); o.w = BP(__builtin_amdgcn_perm(t.v[7].W_, t.v[6].W_, SEL));
  TRP(x, 0x05040100u) *(u32x4*)(p) = o;
  TRP(x, 0x07060302u) *(u32x4*)(p + K) = o;
  TRP(y, 0x05040100u) *(u32x4*)(p + 2 * K) = o;
  TRP(y, 0x07060302u) *(u32x4*)(p + 3 * K) = o;
#undef TRP
#undef BP
}
DEVI float fadd_(float a, float b) { float r; asm("v_add_f32 %0, %1, %2" : "=v"(r) : "v"(a), "v"(b)); return r; }
template <int H> DEVI void tr_colsum(const TrUnit& t, float (&c)[4]) {
#pragma unroll
  for (int i = 4 * H; i < 4 * H + 4; ++i) {
    c[0] = fadd_(c[0], bflo(t.v[i].x)); c[1] = fadd_(c[1], bfhi(t.v[i].x)); c[2] = fadd_(c[2], bflo(t.v[i].y)); c[3] = fadd_(c[3], bfhi(t.v[i].y)); }
  if constexpr (H == 1) {
#pragma unroll
    for (int i = 0; i < 4; ++i) { c[i] = fadd_(c[i], __shfl_xor(c[i], 16)); c[i] = fadd_(c[i], __shfl_xor(c[i], 32)); }
  }
}
struct TrJob { const u16* src; u16* dst; int lds, ldd, k0, n0, cso; bool ok, cs; };
DEVI TrJob tr_job(int u, const u16* Wg, const u16* Wu, const u16* Wd, u16* WguT, u16* WdT) {
  TrJob j; j.ok = u < 1440; if (!j.ok) u = 1439;
  const int w = u / 480, r = u - w * 480;
  j.cs = w < 2;
  if (w < 2) { const int kb = r / 24; j.src = w ? Wu : Wg; j.dst = WguT + (size_t)w * FF * DM; j.lds = FF; j.ldd = DM; j.k0 = kb * 32; j.n0 = (r - kb * 24) * 64; j.cso = kb * 2 * FF + w * FF + j.n0; }
  else { const int kb = r / 10; j.src = Wd; j.dst = WdT; j.lds = DM; j.ldd = FF; j.k0 = kb * 32; j.n0 = (r - kb * 10) * 64; j.cso = 0; }
  return j;
}

#define K1_NST 6
#define K1_PRE 3
using C1 = Cfg<128, 80, 4, 1, 1, 64, K1_NST, 10, K1_PRE>;
template <int W> DEVI void k1_consumer(char* shm, float* wsum, char* stg, int lane, float sgn ) {
  using C = C1;
  f32x4 acc[2][5];
  f32x4 sx[2] = {{0, 0, 0, 0}, {0, 0, 0, 0}}, sq[2] = {{0, 0, 0, 0}, {0, 0, 0, 0}}, sw = {0, 0, 0, 0}, sw4 = {0, 0, 0, 0};
  bf16x8 ones = {0x3f80, 0x3f80, 0x3f80, 0x3f80, 0x3f80, 0x3f80, 0x3f80, 0x3f80};
  asm volatile("" : "+v"(ones));
  consumer<C>(acc, shm, W, lane, [&](bf16x8 (&R)[7]) {
    sx[0] = MFMA(R[0], ones, sx[0]); sx[1] = MFMA(R[1], ones, sx[1]);
    sq[0] = MFMA(R[0], R[0], sq[0]); sq[1] = MFMA(R[1], R[1], sq[1]);
    sw = MFMA(ones, R[2 + W], sw);
    if constexpr (W == 0) sw4 = MFMA(ones, R[6], sw4);
  } );
  const int fr = lane & 15, fq = lane >> 4;
  if (lane < 16) { wsum[W * 16 + lane] = sw[0]; if constexpr (W == 0) wsum[64 + lane] = sw4[0]; }
  f32x4 rs[2], mr[2];
#pragma unroll
  for (int a = 0; a < 2; ++a)
#pragma unroll
    for (int r = 0; r < 4; ++r) {
      float s2 = __shfl(sq[a][r], 20 * fq + r);
      float mean = sx[a][r] * (1.0f / DM);
      float var = fmaxf(s2 * (1.0f / DM) - mean * mean, 0.0f);
      float rstd = sgn * rsqrtf(var + 1e-5f);
      rs[a][r] = rstd; mr[a][r] = mean * rstd;
    }
  lg_bar();
#pragma unroll
  for (int b = 0; b < 5; ++b) {
    float w = wsum[b * 16 + fr];
    f32x4 v0 = acc[0][b] * rs[0] - w * mr[0], v1 = acc[1][b] * rs[1] - w * mr[1];
    u32x4 o; o.x = pack2(v0[0], v0[1]); o.y = pack2(v0[2], v0[3]); o.z = pack2(v1[0], v1[1]); o.w = pack2(v1[2], v1[3]);
    *(u32x4*)(stg + (b * 16 + fr) * 272 + W * 64 + fq * 16) = o;
  }
}
#define K1_PFC 1
__global__ __launch_bounds__(512, 2) void k1_hid(const u16* __restrict__ x, const u16* __restrict__ Wre, const u16* __restrict__ Wim,
                                                 const u16* __restrict__ Cr, const u16* __restrict__ Ci, const u16* __restrict__ Wg,
                                                 const u16* __restrict__ Wu, const u16* __restrict__ Wd, u16* __restrict__ abT,
                                                 u16* __restrict__ WguT, u16* __restrict__ WdT, float* __restrict__ csp, const u16* outp) {
  using C = C1;
  extern __shared__ __attribute__((aligned(16))) char shm[];
  const int bid = blockIdx.x, xcd = bid & 7, idx = bid >> 3;
  const int ts = (xcd >> 1) * 4 + (idx >> 3), te = (xcd & 1) * 8 + (idx & 7);
  const int wid = __builtin_amdgcn_readfirstlane(threadIdx.x >> 6), lane = threadIdx.x & 63;
  const int half = te >> 3;
  constexpr int FREE = ((C::LASTSLOT + 1) % C::NST) * C::STG;
  float* wsum = (float*)(shm + FREE);
  char* stg = shm + FREE + 1024;
  if (wid >= 4) {
    const int lw = wid - 4, j = lane >> 3;
    const char* Wb = (const char*)(half ? Wim : Wre) + (size_t)(te & 7) * 80 * DM * 2;
    const char* xb = (const char*)x + (size_t)ts * 128 * DM * 2;
    auto sA = [&](int p) { return (unsigned)((32 * (p >> 2) + 16 * (p & 1) + 4 * ((p >> 1) & 1)) * DM * 2); };
    auto sB = [&](int p) { return (unsigned)(p * 8 * DM * 2); };
    auto ptrs = [&](int k, const char*& pa, const char*& pb) { pa = xb + k * 128; pb = Wb + k * 128; };
    const unsigned vA = (unsigned)((8 * (j >> 2) + (j & 3)) * DM * 2), vB = (unsigned)(j * DM * 2);
    TrUnit tu[2];
    const TrJob j0 = tr_job(bid * 4 + lw, Wg, Wu, Wd, WguT, WdT), j1 = tr_job(1024 + bid * 4 + lw, Wg, Wu, Wd, WguT, WdT);
    auto tail = [&]() {
      tr_load(tu[0], j0.src, j0.lds, j0.k0, j0.n0, lane);
      tr_load(tu[1], j1.src, j1.lds, j1.k0, j1.n0, lane);
      const unsigned dummy = (unsigned)(size_t)(__attribute__((address_space(3))) char*)shm + C::LDS;
      const char* cp = (const char*)((bid & 1) ? Ci : Cr) + (size_t)(bid >> 1) * 65536 + lw * 16384;
#pragma unroll
      for (int i = 0; i < 16; ++i) glds16(lane * 16 + i * 1024, cp, dummy);
    };
    constexpr int EX = 16 + (K1_PFC ? 16 : 0);
    if (lw < 2) loader<C, 4, 3, EX>(shm, lw * 4, lw * 3, lane, sA, vA, sB, vB, ptrs, tail);
    else loader<C, 4, 2, EX>(shm, lw * 4, 2 + lw * 2, lane, sA, vA, sB, vB, ptrs, tail);
    tr_wait(tu[0]); tr_wait(tu[1]);
#pragma unroll
    for (int q = 0; q < 2; ++q) {
      const TrJob& jb = q ? j1 : j0;
      if (jb.ok) {
        tr_store(tu[q], jb.dst, jb.ldd, jb.k0, jb.n0, lane);
        if (jb.cs) {
          float c[4] = {0.f, 0.f, 0.f, 0.f}; tr_colsum<0>(tu[q], c); tr_colsum<1>(tu[q], c);
          if (lane < 16) *(f32x4*)(csp + jb.cso + 4 * lane) = f32x4{c[0], c[1], c[2], c[3]};
        }
      }
      if (q == 0) lg_bar();
    }
  } else {
    unsigned tsink = 0;
    { const unsigned g = idx * 256 + threadIdx.x;
      const unsigned off = g * TLBG;
      if (off < (34u << 20)) touch(tsink, off, abT);
      if (off < 2621440u) touch(tsink, off, outp);
      if (off < (8u << 20)) { touch(tsink, off, Cr); touch(tsink, off, Ci); }
    }
    const float sgn = half ? -1.0f : 1.0f;
    if (wid == 0) k1_consumer<0>(shm, wsum, stg, lane, sgn );
    else if (wid == 1) k1_consumer<1>(shm, wsum, stg, lane, sgn );
    else if (wid == 2) k1_consumer<2>(shm, wsum, stg, lane, sgn );
    else k1_consumer<3>(shm, wsum, stg, lane, sgn );
    touch_done(tsink);
  }
  lg_bar();
  u16* ob = abT + (size_t)(te & 7) * 80 * 4096 + half * 2048 + ts * 128;
#pragma unroll
  for (int i = 0; i < 3; ++i) {
    int c = threadIdx.x + i * 512;
    if (i < 2 || c < 1280) {
      int row = c >> 4, ch = c & 15;
      *(u32x4*)(ob + (size_t)row * 4096 + ch * 8) = *(const u32x4*)(stg + row * 272 + ch * 16);
    }
  }
}

DEVI u32x4 add8(f32x4 a0, f32x4 a1, u32x4 r) {
  u32x4 o;
  o.x = pack2(a0[0] + bflo(r.x), a0[1] + bfhi(r.x)); o.y = pack2(a0[2] + bflo(r.y), a0[3] + bfhi(r.y));
  o.z = pack2(a1[0] + bflo(r.z), a1[1] + bfhi(r.z)); o.w = pack2(a1[2] + bflo(r.w), a1[3] + bfhi(r.w));
  return o;
}
DEVI u32x4 addbf8(u32x4 a, u32x4 b) {
  u32x4 o;
  o.x = pack2(bflo(a.x) + bflo(b.x), bfhi(a.x) + bfhi(b.x)); o.y = pack2(bflo(a.y) + bflo(b.y), bfhi(a.y) + bfhi(b.y));
  o.z = pack2(bflo(a.z) + bflo(b.z), bfhi(a.z) + bfhi(b.z)); o.w = pack2(bflo(a.w) + bflo(b.w), bfhi(a.w) + bfhi(b.w));
  return o;
}

#define K2_PRE 6
#define K2_NST 6
using C2 = Cfg<80, 128, 1, 2, 2, 64, K2_NST, 32, K2_PRE>;
__global__ __launch_bounds__(512, 2) void k2_seq(const u16* __restrict__ x, const u16* __restrict__ Cr, const u16* __restrict__ Ci,
                                                 const u16* __restrict__ abT, const float* __restrict__ csp,
                                                 u16* __restrict__ P, float* __restrict__ csum) {
  using C = C2;
  extern __shared__ __attribute__((aligned(16))) char shm[];
  const int bid = blockIdx.x, xcd = bid & 7, idx = bid >> 3;
  const int half = xcd & 1, tt = (xcd >> 1) * 4 + (idx >> 3), te = idx & 7;
  const int wid = __builtin_amdgcn_readfirstlane(threadIdx.x >> 6), lane = threadIdx.x & 63;
  static_assert((C::NST - 1 - C::LASTSLOT) * C::STG >= 2 * 128 * 336, "");
  char* red = shm + (C::LASTSLOT + 1) * C::STG;
  u32x4 rx[3] = {{0, 0, 0, 0}, {0, 0, 0, 0}, {0, 0, 0, 0}};
  auto xload = [&]() {
    if (half == 0) {
#pragma unroll
      for (int i = 0; i < 3; ++i) { int c = threadIdx.x + i * 512; if (c > 1279) c = 1279;
        rx[i] = *(const u32x4*)(x + (size_t)(tt * 128 + c / 10) * DM + te * 80 + (c % 10) * 8); }
    }
  };
  if (wid >= 4) {
    const int lw = wid - 4, j = lane >> 3;
    const char* ab = (const char*)abT + (size_t)te * 80 * 8192 + half * 4096;
    const char* cc = (const char*)(half ? Ci : Cr) + (size_t)tt * 128 * 4096;
    auto sA = [&](int p) { return (unsigned)(p * 8 * 8192); };
    auto sB = [&](int p) { return (unsigned)(p * 8 * 4096); };
    auto ptrs = [&](int k, const char*& pa, const char*& pb) { pa = ab + k * 128; pb = cc + k * 128; };
    if (lw < 2) loader<C, 3, 4>(shm, lw * 3, lw * 4, lane, sA, (unsigned)(j * 8192), sB, (unsigned)(j * 4096), ptrs, []() {});
    else loader<C, 2, 4>(shm, 2 + lw * 2, lw * 4, lane, sA, (unsigned)(j * 8192), sB, (unsigned)(j * 4096), ptrs, []() {});
    xload();
  } else {
    if (wid == 0 && bid < 48) {
      const int col = bid * 64 + lane; float c = 0;
#pragma unroll
      for (int i = 0; i < 20; ++i) c += csp[(size_t)i * (2 * FF) + col];
      csum[col] = c;
    }
    xload();
    f32x4 acc[5][4];
    consumer<C>(acc, shm, wid, lane, [](auto&) {} );
    const int fr = lane & 15, fq = lane >> 4, kg = wid >> 1, wb = wid & 1;
#pragma unroll
    for (int n = 0; n < 4; ++n)
#pragma unroll
      for (int j = 0; j < 5; ++j) *(f32x4*)(red + kg * (128 * 336) + ((wb * 4 + n) * 16 + fr) * 336 + j * 64 + fq * 16) = acc[j][n];
  }
  lg_bar();
  u16* op = P + (size_t)half * SEQ * DM + (size_t)tt * 128 * DM + te * 80;
#pragma unroll
  for (int i = 0; i < 3; ++i) {
    const int c = threadIdx.x + i * 512;
    if (i < 2 || c < 1280) {
      const int row = c / 10, ch = c % 10;
      const char* p = red + row * 336 + ch * 32;
      f32x4 a0 = *(const f32x4*)p + *(const f32x4*)(p + 128 * 336), a1 = *(const f32x4*)(p + 16) + *(const f32x4*)(p + 16 + 128 * 336);
      *(u32x4*)(op + (size_t)row * DM + ch * 8) = add8(a0, a1, rx[i]);
    }
  }
}

#define K3_PRE 2
using C3 = Cfg<192, 128, 2, 2, 1, 64, 4, 10, K3_PRE>;
DEVI float fadd1(float a, float b) { float r; asm("v_add_f32 %0, %1, %2" : "=v"(r) : "v"(a), "v"(b)); return r; }
DEVI void facc1(float& s, float a) { asm("v_add_f32 %0, %0, %1" : "+v"(s) : "v"(a)); }
DEVI void fsq1(float& s, float a) { asm("v_fmac_f32 %0, %1, %1" : "+v"(s) : "v"(a)); }
DEVI void gload16(u32x4& d, unsigned voff, const char* sbase) { asm volatile("global_load_dwordx4 %0, %1, %2" : "=v"(d) : "v"(voff), "s"(sbase)); }
template <int WA> DEVI void k3_consumer(char* shm, f32x2* rowst, char* stg, const float* csum, int wid, int lane ) {
  using C = C3;
  const int fr = lane & 15, fq = lane >> 4, wb = wid & 1;
  f32x4 cs[6];
#pragma unroll
  for (int j = 0; j < 6; ++j) cs[j] = *(const f32x4*)(csum + (j & 1) * FF + WA * 48 + (j >> 1) * 16 + 4 * fq);
  f32x4 acc[6][4];
  f32x4 sh[2] = {{0, 0, 0, 0}, {0, 0, 0, 0}}, sq[2] = {{0, 0, 0, 0}, {0, 0, 0, 0}};
  bf16x8 ones = {0x3f80, 0x3f80, 0x3f80, 0x3f80, 0x3f80, 0x3f80, 0x3f80, 0x3f80};
  asm volatile("" : "+v"(ones));
  consumer<C>(acc, shm, wid, lane, [&](bf16x8 (&R)[10]) {
    sh[0] = MFMA(ones, R[6 + 2 * WA], sh[0]); sq[0] = MFMA(R[6 + 2 * WA], R[6 + 2 * WA], sq[0]);
    sh[1] = MFMA(ones, R[7 + 2 * WA], sh[1]); sq[1] = MFMA(R[7 + 2 * WA], R[7 + 2 * WA], sq[1]);
  } );
#pragma unroll
  for (int i = 0; i < 2; ++i) {
    const float d01 = (fr & 1) ? sq[i][1] : sq[i][0], d23 = (fr & 1) ? sq[i][3] : sq[i][2], d = (fr & 2) ? d23 : d01;
    const float mean = sh[i][0] * (1.0f / DM);
    const float var = fmaxf(d * (1.0f / DM) - mean * mean, 0.0f);
    if (fq == (fr >> 2)) rowst[(wb * 4 + 2 * WA + i) * 16 + fr] = f32x2{-mean, var + 1e-5f};
  }
  lg_bar();
#pragma unroll
  for (int n = 0; n < 4; ++n) {
    const int ml = (wb * 4 + n) * 16 + fr;
    const f32x2 st = rowst[ml];
    const float nmean = st[0], ve = st[1];
    const float rstd = __builtin_amdgcn_rsqf(ve);
    const float k1 = -2.3022081985f * rstd;
    const float k2 = k1 * 0.044715f * rstd * rstd;
    f32x4 G[3], U[3], z[3];
#pragma unroll
    for (int p = 0; p < 3; ++p) G[p] = cs[2 * p] * nmean + acc[2 * p][n];
#pragma unroll
    for (int p = 0; p < 3; ++p) z[p] = G[p] * G[p];
#pragma unroll
    for (int p = 0; p < 3; ++p) U[p] = cs[2 * p + 1] * nmean + acc[2 * p + 1][n];
#pragma unroll
    for (int p = 0; p < 3; ++p) z[p] = z[p] * k2 + k1;
#pragma unroll
    for (int p = 0; p < 3; ++p) z[p] = z[p] * G[p];
#pragma unroll
    for (int p = 0; p < 3; ++p) { z[p][0] = __builtin_amdgcn_exp2f(z[p][0]); z[p][1] = __builtin_amdgcn_exp2f(z[p][1]); z[p][2] = __builtin_amdgcn_exp2f(z[p][2]); z[p][3] = __builtin_amdgcn_exp2f(z[p][3]); U[p] = U[p] * G[p]; }
#pragma unroll
    for (int p = 0; p < 3; ++p) z[p] = z[p] * ve + ve;
#pragma unroll
    for (int p = 0; p < 3; ++p) { z[p][0] = __builtin_amdgcn_rcpf(z[p][0]); z[p][1] = __builtin_amdgcn_rcpf(z[p][1]); z[p][2] = __builtin_amdgcn_rcpf(z[p][2]); z[p][3] = __builtin_amdgcn_rcpf(z[p][3]); }
#pragma unroll
    for (int p = 0; p < 3; ++p) {
      f32x4 v = U[p] * z[p];
      u32x2 o; o.x = pack2(v[0], v[1]); o.y = pack2(v[2], v[3]);
      *(u32x2*)(stg + ml * 208 + (WA * 48 + p * 16 + fq * 4) * 2) = o;
    }
  }
}
__global__ __launch_bounds__(512, 2) void k3_ffn1(const u16* __restrict__ P, const u16* __restrict__ WguT, const float* __restrict__ csum,
                                                  u16* __restrict__ act) {
  using C = C3;
  extern __shared__ __attribute__((aligned(16))) char shm[];
  const int bid = blockIdx.x, xcd = bid & 7, idx = bid >> 3;
  const int tm = (xcd >> 2) * 8 + (idx >> 2), tn = (xcd & 3) * 4 + (idx & 3);
  const int wid = __builtin_amdgcn_readfirstlane(threadIdx.x >> 6), lane = threadIdx.x & 63;
  constexpr int FREE = ((C::LASTSLOT + 1) % C::NST) * C::STG;
  f32x2* rowst = (f32x2*)(shm + FREE);
  char* stg = shm + FREE + 4096;
  if (wid >= 4) {
    const int lw = wid - 4, j = lane >> 3;
    const char* wbp = (const char*)WguT + (size_t)tn * 96 * DM * 2;
    const char* p0 = (const char*)P + (size_t)(tm * 128 + lw * 32) * DM * 2;
    const char* p1 = p0 + (size_t)SEQ * DM * 2;
    const unsigned sw0 = (unsigned)(((lane & 7) ^ (lane >> 4)) * 16);
    const unsigned ldsb = (unsigned)(size_t)(__attribute__((address_space(3))) char*)shm;
    unsigned vw[6];
#pragma unroll
    for (int i = 0; i < 6; ++i) { const int p = lw * 6 + i;
      vw[i] = (unsigned)(j * DM * 2) + (sw0 ^ (unsigned)((p & 1) * 64)) + (unsigned)((((p >> 1) & 1) * FF + 16 * (p >> 2) + 8 * (p & 1)) * DM * 2); }
    const unsigned vh = (unsigned)(j * DM * 2 + (lane & 7) * 16);
    char* hw = shm + (24 + lw * 4) * 1024 + j * 128;
    u32x4 buf[3][8];
    auto vm1 = [&](auto S, auto Q) {
      constexpr int s = decltype(S)::value, q = decltype(Q)::value;
      if constexpr (q < 8) gload16(buf[s % 3][q], vh + (q >> 1) * 8 * DM * 2, ((q & 1) ? p1 : p0) + s * 128);
      else glds16(vw[q - 8], wbp + s * 128, ldsb + (s % C::NST) * C::STG + (lw * 6 + q - 8) * 1024);
    };
    static_for<-1, C::NK - 1>([&](auto K) {
      constexpr int k = decltype(K)::value;
      constexpr int c = k + 1;
      constexpr int s0 = C::issued(k - 1) + 1, nop = (C::issued(k) - C::issued(k - 1)) * 14;
      auto ops = [&](auto B, auto E) { static_for<decltype(B)::value, decltype(E)::value>([&](auto O) {
        constexpr int o = decltype(O)::value; vm1(IC(s0 + o / 14), IC(o % 14)); }); };
      if constexpr (k == -1) ops(IC(0), IC(nop));
      u32x4 (&b)[8] = buf[c % 3];
      asm volatile("s_waitcnt vmcnt(%8)" : "+v"(b[0]), "+v"(b[1]), "+v"(b[2]), "+v"(b[3]), "+v"(b[4]), "+v"(b[5]), "+v"(b[6]), "+v"(b[7])
                   : "i"(((k == -1 ? C::issued(k) : C::issued(k - 1)) - c) * 14 + 6) : "memory");
      static_for<0, 4>([&](auto I) {
        constexpr int i = decltype(I)::value;
        if constexpr (k >= 0) { ops(IC(nop * i / 4), IC(nop * (i + 1) / 4)); SB(); }
        const u32x4 u = b[2 * i], v = b[2 * i + 1];
        float hh[8];
        hh[0] = fadd1(bflo(u.x), bflo(v.x)); hh[1] = fadd1(bfhi(u.x), bfhi(v.x)); hh[2] = fadd1(bflo(u.y), bflo(v.y)); hh[3] = fadd1(bfhi(u.y), bfhi(v.y));
        hh[4] = fadd1(bflo(u.z), bflo(v.z)); hh[5] = fadd1(bfhi(u.z), bfhi(v.z)); hh[6] = fadd1(bflo(u.w), bflo(v.w)); hh[7] = fadd1(bfhi(u.w), bfhi(v.w));
        u32x4 o; o.x = pack2(hh[0], hh[1]); o.y = pack2(hh[2], hh[3]); o.z = pack2(hh[4], hh[5]); o.w = pack2(hh[6], hh[7]);
        *(u32x4*)(hw + (c % C::NST) * C::STG + i * 1024 + (sw0 ^ (unsigned)((i & 1) * 64))) = o;
        SB();
      });
      asm volatile("s_waitcnt vmcnt(%0) lgkmcnt(0)\n\ts_barrier" ::"i"((C::issued(k) - c) * 14) : "memory");
    });
    lg_bar();
  } else {
    if (wid < 2) k3_consumer<0>(shm, rowst, stg, csum + tn * 96, wid, lane );
    else k3_consumer<1>(shm, rowst, stg, csum + tn * 96, wid, lane );
  }
  lg_bar();
  u16* ob = act + (size_t)tm * 128 * FF + tn * 96;
#pragma unroll
  for (int i = 0; i < 3; ++i) {
    int c = threadIdx.x + i * 512, row = c / 12, ch = c % 12;
    *(u32x4*)(ob + (size_t)row * FF + ch * 8) = *(const u32x4*)(stg + row * 208 + ch * 16);
  }
}

#define K4_PRE 4
using C4 = Cfg<80, 64, 1, 2, 2, 128, 4, 12, K4_PRE>;
__global__ __launch_bounds__(512, 2) void k4_ffn2(const u16* __restrict__ P, const u16* __restrict__ act, const u16* __restrict__ WdT,
                                                  u16* __restrict__ out) {
  using C = C4;
  extern __shared__ __attribute__((aligned(16))) char shm[];
  const int bid = blockIdx.x, xcd = bid & 7, idx = bid >> 3;
  const int tm = (xcd >> 1) * 8 + (idx >> 2), td = (xcd & 1) * 4 + (idx & 3);
  const int wid = __builtin_amdgcn_readfirstlane(threadIdx.x >> 6), lane = threadIdx.x & 63;
  static_assert(C::LASTSLOT == C::NST - 1 && (C::NST - 1) * C::STG >= 2 * 64 * 336, "");
  u32x4 ra[2], rb[2];
  auto rload = [&]() {
#pragma unroll
    for (int i = 0; i < 2; ++i) { int c = threadIdx.x + i * 512; if (c > 639) c = 639;
      const u16* p = P + (size_t)(tm * 64 + c / 10) * DM + td * 80 + (c % 10) * 8;
      ra[i] = *(const u32x4*)p; rb[i] = *(const u32x4*)(p + (size_t)SEQ * DM); }
  };
  if (wid >= 4) {
    const int lw = wid - 4, j = lane >> 3;
    const char* wp = (const char*)WdT + (size_t)td * 80 * FF * 2;
    const char* ap = (const char*)act + (size_t)tm * 64 * FF * 2;
    auto sA = [&](int p) { return (unsigned)(p * 8 * FF * 2); };
    auto ptrs = [&](int k, const char*& pa, const char*& pb) { pa = wp + k * 256; pb = ap + k * 256; };
    loader<C, 5, 4>(shm, lw * 5, lw * 4, lane, sA, (unsigned)(j * FF * 2), sA, (unsigned)(j * FF * 2), ptrs, []() {});
    rload();
  } else {
    rload();
    f32x4 acc[5][2];
    consumer<C>(acc, shm, wid, lane, [](auto&) {} );
    const int fr = lane & 15, fq = lane >> 4, kg = wid >> 1, wb = wid & 1;
#pragma unroll
    for (int n = 0; n < 2; ++n)
#pragma unroll
      for (int j = 0; j < 5; ++j) *(f32x4*)(shm + kg * (64 * 336) + ((wb * 2 + n) * 16 + fr) * 336 + j * 64 + fq * 16) = acc[j][n];
  }
  lg_bar();
  u16* op = out + (size_t)tm * 64 * DM + td * 80;
#pragma unroll
  for (int i = 0; i < 2; ++i) {
    const int c = threadIdx.x + i * 512;
    if (i == 0 || c < 640) {
      const int row = c / 10, ch = c % 10;
      const char* p = shm + row * 336 + ch * 32;
      f32x4 a0 = *(const f32x4*)p + *(const f32x4*)(p + 64 * 336), a1 = *(const f32x4*)(p + 16) + *(const f32x4*)(p + 16 + 64 * 336);
      *(u32x4*)(op + (size_t)row * DM + ch * 8) = add8(a0, a1, addbf8(ra[i], rb[i]));
    }
  }
}

static void launch_stage(int which, void* const* d_in, void* d_out, void* d_ws, hipStream_t stream) {
  const u16* x = (const u16*)d_in[0];
  const u16* Cr = (const u16*)d_in[1];
  const u16* Ci = (const u16*)d_in[2];
  const u16* Wre = (const u16*)d_in[3];
  const u16* Wim = (const u16*)d_in[4];
  const u16* Wg = (const u16*)d_in[6];
  const u16* Wu = (const u16*)d_in[7];
  const u16* Wd = (const u16*)d_in[8];
  char* ws = (char*)d_ws;
  u16* abT = (u16*)(ws);
  u16* WguT = (u16*)(ws + (10 << 20));
  u16* WdT = (u16*)(ws + (14 << 20));
  u16* act = (u16*)(ws + (16 << 20));
  u16* P = (u16*)(ws + (28 << 20));
  float* csp = (float*)(ws + (25 << 20));
  float* csum = (float*)(ws + (26 << 20));
  u16* out = (u16*)d_out;
  if (which == 0) {
    (void)hipFuncSetAttribute((const void*)k1_hid, hipFuncAttributeMaxDynamicSharedMemorySize, C1::LDS + 1024);
    k1_hid<<<256, 512, C1::LDS + 1024, stream>>>(x, Wre, Wim, Cr, Ci, Wg, Wu, Wd, abT, WguT, WdT, csp, out);
  } else if (which == 1) {
    (void)hipFuncSetAttribute((const void*)k2_seq, hipFuncAttributeMaxDynamicSharedMemorySize, C2::LDS);
    k2_seq<<<256, 512, C2::LDS, stream>>>(x, Cr, Ci, abT, csp, P, csum);
  } else if (which == 2) {
    (void)hipFuncSetAttribute((const void*)k3_ffn1, hipFuncAttributeMaxDynamicSharedMemorySize, C3::LDS);
    k3_ffn1<<<256, 512, C3::LDS, stream>>>(P, WguT, csum, act);
  } else {
    (void)hipFuncSetAttribute((const void*)k4_ffn2, hipFuncAttributeMaxDynamicSharedMemorySize, C4::LDS);
    k4_ffn2<<<256, 512, C4::LDS, stream>>>(P, act, WdT, out);
  }
}

extern "C" void kernel_launch(void* const* d_in, const int* in_sizes, int n_in, void* d_out, int out_size, void* d_ws, size_t ws_size,
                              hipStream_t stream) {
  for (int i = 0; i < 4; ++i) launch_stage(i, d_in, d_out, d_ws, stream);
}
```

```cpp
#include <hip/hip_runtime.h>
#include <hip/hip_bf16.h>
#include <stdint.h>
#include <type_traits>

typedef unsigned short u16;
typedef __attribute__((ext_vector_type(8))) short bf16x8;
typedef __attribute__((ext_vector_type(4))) float f32x4;
typedef __attribute__((ext_vector_type(2))) float f32x2;
typedef __attribute__((ext_vector_type(2))) __bf16 bf16x2;
typedef __attribute__((ext_vector_type(2))) unsigned u32x2;
typedef __attribute__((ext_vector_type(4))) unsigned u32x4;

#define DEVI __device__ __forceinline__
#define IC(x) std::integral_constant<int, (x)>{}
#define SB() __builtin_amdgcn_sched_barrier(0)
#define MFMA(a, b, c) __builtin_amdgcn_mfma_f32_16x16x32_bf16(a, b, c, 0, 0, 0)

constexpr int SJ_AT = 4;
constexpr int PFX0 = 5, PFX = 4;
constexpr int SEQ = 2048, DM = 640, FF = 1536;

template <int B, int E, class F> DEVI void static_for(F&& f) {
  if constexpr (B < E) { f(IC(B)); static_for<B + 1, E>(f); }
}
DEVI void glds16(unsigned voff, const char* sbase, unsigned ldsaddr) {
  asm volatile("s_mov_b32 m0, %2\n\ts_nop 0\n\tglobal_load_lds_dwordx4 %0, %1" ::"v"(voff), "s"(sbase), "s"(ldsaddr));
}
DEVI void glds16c(unsigned voff, const char* sbase, unsigned ldsaddr) {
  asm volatile("s_mov_b32 m0, %2\n\ts_nop 0\n\tglobal_load_lds_dwordx4 %0, %1 sc1" ::"v"(voff), "s"(sbase), "s"(ldsaddr));
}
DEVI void gstore16c(void* p, u32x4 v) { asm volatile("global_store_dwordx4 %0, %1, off sc1\n\ts_nop 1" ::"v"(p), "v"(v) : "memory"); }
typedef unsigned long long u64;
DEVI void flag_set(u64* f, u64 tag) { __hip_atomic_store(f, tag, __ATOMIC_RELAXED, __HIP_MEMORY_SCOPE_AGENT); }
typedef __attribute__((ext_vector_type(16))) unsigned u32x16;
DEVI void flag_wait16(const u64* f, u64 tag, int lane) {
  const unsigned tl = (unsigned)tag, th = (unsigned)(tag >> 32);
  for (unsigned spin = 0; spin < 256; ++spin) {
    u32x16 a, b;
    asm volatile("s_load_dwordx16 %0, %2, 0x0 glc\n\ts_load_dwordx16 %1, %2, 0x40 glc\n\ts_waitcnt lgkmcnt(0)" : "=&s"(a), "=&s"(b) : "s"(f) : "memory");
    unsigned d = 0;
#pragma unroll
    for (int i = 0; i < 16; i += 2) d |= (a[i] ^ tl) | (a[i + 1] ^ th) | (b[i] ^ tl) | (b[i + 1] ^ th);
    if (d == 0) return;
    __builtin_amdgcn_s_sleep(1);
  }
  for (unsigned spin = 0; spin < (1u << 16); ++spin) {
    const u64 v = __hip_atomic_load(f + (lane & 15), __ATOMIC_RELAXED, __HIP_MEMORY_SCOPE_AGENT);
    if (__all(v == tag)) break;
    __builtin_amdgcn_s_sleep(2);
  }
  asm volatile("" ::: "memory");
}
template <int N> DEVI void waitvm_bar() { asm volatile("s_waitcnt vmcnt(%0)\n\ts_barrier" ::"i"(N) : "memory"); }
DEVI void bar() { asm volatile("s_barrier" ::: "memory"); }
DEVI void lg_bar() { asm volatile("s_waitcnt lgkmcnt(0)\n\ts_barrier" ::: "memory"); }
DEVI unsigned pack2(float a, float b) { f32x2 p = {a, b}; return __builtin_bit_cast(unsigned, __builtin_convertvector(p, bf16x2)); }
DEVI float bflo(unsigned v) { return __builtin_bit_cast(float, v << 16); }
DEVI float bfhi(unsigned v) { return __builtin_bit_cast(float, v & 0xffff0000u); }
template <int CTRL> DEVI float dppf(float v) {
  return __builtin_bit_cast(float, __builtin_amdgcn_update_dpp(0, __builtin_bit_cast(int, v), CTRL, 0xf, 0xf, true));
}
DEVI float red16(float v) { v += dppf<0xB1>(v); v += dppf<0x4E>(v); v += dppf<0x141>(v); v += dppf<0x140>(v); return v; }
DEVI void flag_wait1(const u64* f, u64 tag) {
  for (unsigned spin = 0; spin < 256; ++spin) {
    u64 a;
    asm volatile("s_load_dwordx2 %0, %1, 0x0 glc\n\ts_waitcnt lgkmcnt(0)" : "=s"(a) : "s"(f) : "memory");
    if (a == tag) return;
    __builtin_amdgcn_s_sleep(1);
  }
  for (unsigned spin = 0; spin < (1u << 16); ++spin) {
    if (__hip_atomic_load(f, __ATOMIC_RELAXED, __HIP_MEMORY_SCOPE_AGENT) == tag) break;
    __builtin_amdgcn_s_sleep(2);
  }
  asm volatile("" ::: "memory");
}

template <int TA_, int TB_, int NWA_, int NWB_, int KS_, int BK_, int NST_, int NK_, int PRE_> struct Cfg {
  static constexpr int TA = TA_, TB = TB_, NWA = NWA_, NWB = NWB_, KS = KS_, BK = BK_, NST = NST_, NK = NK_;
  static constexpr int NSUB = BK / 64, PA = TA / 8, PB = TB / 8, PPS = PA + PB, STG = NSUB * PPS * 1024, SUBB = PPS * 1024;
  static constexpr int WAF = TA / 16 / NWA, WBF = TB / 16 / NWB, KH = (BK / 32) / KS;
  static constexpr int NF = WAF + WBF, NMF = WAF * WBF;
  static constexpr int LDS = NST * STG;
  static constexpr int LASTSLOT = (NK - 1) % NST;
  static constexpr int issued(int k) {
    if (k < -1) return -1;
    int a = 2 * k + 1 + PRE_, b = k - 1 + NST, c = NK - 1;
    if (k == -1) b = NST - 1;
    int m = a < b ? a : b; return m < c ? m : c;
  }
  static constexpr int steady_begin() { int k = 0; while (!(issued(k) == k - 1 + NST && issued(k - 1) == k - 2 + NST) && k < NK) ++k; return k; }
  static constexpr int K0 = steady_begin() < NK - NST + 1 ? steady_begin() : NK - NST + 1;
  static constexpr int K1 = NK - NST + 1 > K0 ? NK - NST + 1 : K0;
};

template <class C, int A0, int GA, int B0, int GB> struct Pieces {
  static constexpr int G = GA + GB;
  unsigned voff[G];
  template <class SA, class SB_> DEVI Pieces(int lane, SA sA, unsigned vA, SB_ sB, unsigned vB) {
    const unsigned sw0 = (unsigned)(((lane & 7) ^ (lane >> 4)) * 16);
    const unsigned a_e = vA + sw0, a_o = vA + (sw0 ^ 64u), b_e = vB + sw0, b_o = vB + (sw0 ^ 64u);
    static_for<0, G>([&](auto I) {
      constexpr int i = decltype(I)::value;
      if constexpr (i < GA) { constexpr int p = A0 + i, sub = p / C::PA, pr = p % C::PA; voff[i] = ((pr & 1) ? a_o : a_e) + (sA(pr) + sub * 128u); }
      else { constexpr int p = B0 + i - GA, sub = p / C::PB, pr = p % C::PB; voff[i] = ((pr & 1) ? b_o : b_e) + (sB(pr) + sub * 128u); }
    });
  }
  template <int DEP, int B, int E> DEVI void issue(const char* pa, const char* pb, unsigned slot) const {
    static_for<B, E>([&](auto I) {
      constexpr int i = decltype(I)::value;
      constexpr int p = i < GA ? A0 + i : B0 + i - GA, np = i < GA ? C::PA : C::PB;
      constexpr unsigned lo = (p / np) * C::SUBB + ((i < GA ? 0 : C::PA) + p % np) * 1024;
      if constexpr (DEP == 2 || (DEP == 1 && i < GA)) glds16c(voff[i], i < GA ? pa : pb, lo + slot);
      else glds16(voff[i], i < GA ? pa : pb, lo + slot);
    });
  }
};
template <class C, int X, int NX> struct XSched {
  static constexpr int xdone(int k) { int n = (k + 1 - PFX0) * X; return n < 0 ? 0 : (n > NX ? NX : n); }
  static constexpr int it(int c) { int i = -1; while (C::issued(i) < c) ++i; return i; }
  static constexpr int younger(int k, int G) { return (C::issued(k) - (k + 1)) * G + xdone(k) - xdone(it(k + 1) - 1); }
};
template <class C, int A0, int GA, int B0, int GB, int X, int NX, class SA, class SB_, class Ptrs, class Extra>
DEVI void loader_x(int lane, SA sA, unsigned vA, SB_ sB, unsigned vB, Ptrs ptrs, Extra extra) {
  constexpr int G = GA + GB;
  using XS = XSched<C, X, NX>;
  const Pieces<C, A0, GA, B0, GB> pc(lane, sA, vA, sB, vB);
  static_for<-1, C::NK - 1>([&](auto K) {
    constexpr int k = decltype(K)::value;
    static_for<C::issued(k - 1) + 1, C::issued(k) + 1>([&](auto S) {
      constexpr int s = decltype(S)::value;
      const char *pa, *pb; ptrs(s, pa, pb);
      pc.template issue<0, 0, G>(pa, pb, (s % C::NST) * C::STG);
    });
    static_for<XS::xdone(k - 1), XS::xdone(k)>(extra);
    static_assert(XS::younger(k, G) <= 63, "vmcnt range");
    waitvm_bar<XS::younger(k, G)>();
  });
  static_for<XS::xdone(C::NK - 2), NX>(extra);
}
template <class C, int DEP, int A0, int GA, int B0, int GB, class SA, class SB_, class Ptrs, class Pre>
DEVI void loader_dep(int lane, SA sA, unsigned vA, SB_ sB, unsigned vB, Ptrs ptrs, Pre pre) {
  constexpr int G = GA + GB, NST = C::NST, GD = DEP == 1 ? GA : GB;
  constexpr int DB = DEP == 1 ? 0 : GA, DE = DEP == 1 ? GA : G, IB = DEP == 1 ? GA : 0, IE = DEP == 1 ? G : GA;
  const Pieces<C, A0, GA, B0, GB> pc(lane, sA, vA, sB, vB);
  static_for<0, NST>([&](auto S) { constexpr int s = decltype(S)::value; const char *pa, *pb; ptrs(s, pa, pb); pc.template issue<DEP, IB, IE>(pa, pb, s * C::STG); });
  pre();
  bar();
  constexpr int KS0a = C::K0 > NST - 2 ? C::K0 : NST - 2, KS0 = KS0a > 1 ? KS0a : 1;
  static_for<-1, KS0>([&](auto K) {
    constexpr int k = decltype(K)::value;
    static_for<C::issued(k - 1) + 1, C::issued(k) + 1>([&](auto S) {
      constexpr int s = decltype(S)::value;
      const char *pa, *pb; ptrs(s, pa, pb);
      if constexpr (s < NST) pc.template issue<DEP, DB, DE>(pa, pb, s * C::STG); else pc.template issue<DEP, 0, G>(pa, pb, (s % NST) * C::STG);
    });
    constexpr int hi = C::issued(k), lo = k + 2;
    constexpr int nd = (hi < NST - 1 ? hi : NST - 1) - lo + 1 > 0 ? (hi < NST - 1 ? hi : NST - 1) - lo + 1 : 0;
    constexpr int nf = hi - lo + 1 - nd;
    waitvm_bar<nd * GD + nf * G>();
  });
  unsigned slot = ((KS0 - 1 + NST) % NST) * C::STG;
  for (int k = KS0; k < C::K1; ++k) {
    const char *pa, *pb; ptrs(k - 1 + NST, pa, pb);
    pc.template issue<DEP, 0, G>(pa, pb, slot);
    slot += C::STG; if (slot == NST * C::STG) slot = 0;
    waitvm_bar<(NST - 2) * G>();
  }
  static_for<C::K1, C::NK - 1>([&](auto K) { constexpr int k = decltype(K)::value; waitvm_bar<(C::NK - 2 - k) * G>(); });
}

template <class C, int M1 = -1, int M2 = -1, class Extra, class Mid> DEVI void consumer(f32x4 (&acc)[C::WAF][C::WBF], char* shm, int wid, int lane, Extra extra, Mid mid) {
  constexpr int WAF = C::WAF, WBF = C::WBF, KH = C::KH, NF = C::NF, NMF = C::NMF, STG = C::STG, NST = C::NST, NK = C::NK;
  const int kg = wid / (C::NWA * C::NWB), wab = wid % (C::NWA * C::NWB), wa = wab / C::NWB, wb = wab % C::NWB;
#pragma unroll
  for (int j = 0; j < WAF; ++j)
#pragma unroll
    for (int b = 0; b < WBF; ++b) acc[j][b] = f32x4{0, 0, 0, 0};
  const int fr = lane & 15, fq = lane >> 4;
  const int c0 = fr * 128 + ((fq ^ (fr >> 1)) * 16);
  int rda[KH], rdb[KH];
#pragma unroll
  for (int h = 0; h < KH; ++h) {
    int ks = kg * KH + h, o = (c0 ^ ((ks & 1) * 64)) + (ks >> 1) * C::SUBB;
    rda[h] = o + wa * WAF * 2048; rdb[h] = o + C::TA * 128 + wb * WBF * 2048;
  }
  bf16x8 R[2][NF];
  auto rd1 = [&](bf16x8 (&Rn)[NF], unsigned slot, int h, int f) {
    if (f < WAF) Rn[f] = *(const bf16x8*)(shm + slot + rda[h] + f * 2048);
    else Rn[f] = *(const bf16x8*)(shm + slot + rdb[h] + (f - WAF) * 2048);
  };
  auto body = [&](bf16x8 (&Rc)[NF], bf16x8 (&Rn)[NF], unsigned slot, int h, bool dor) {
    int ri = 0;
#pragma unroll
    for (int m = 0; m < NMF; ++m) {
      { int n = m / WAF, j = m % WAF; acc[j][n] = MFMA(Rc[j], Rc[WAF + n], acc[j][n]); }
      int rt = ((m + 1) * NF * 4 + 3 * NMF - 1) / (3 * NMF); if (rt > NF) rt = NF;
#pragma unroll
      for (; ri < rt; ++ri) if (dor) rd1(Rn, slot, h, ri);
      SB();
    }
    extra(Rc);
    SB();
  };
  lg_bar();
#pragma unroll
  for (int f = 0; f < NF; ++f) rd1(R[0], 0, 0, f);
  unsigned slot = 0;
  auto stage = [&](auto P, bool last) {
    constexpr int par = decltype(P)::value;
    unsigned nslot = slot + STG; if (nslot == NST * STG) nslot = 0;
#pragma unroll
    for (int h = 0; h < KH; ++h) {
      const int c = (par + h) & 1;
      if (h == KH - 1) { if (!last) { lg_bar(); body(R[c], R[c ^ 1], nslot, 0, true); } else body(R[c], R[c ^ 1], 0, 0, false); }
      else body(R[c], R[c ^ 1], slot, h + 1, true);
    }
    slot = nslot;
  };
  auto run = [&](int b, int e) {
    if constexpr (KH % 2 == 0) { for (int k = b; k < e; ++k) stage(IC(0), false); }
    else { for (int k = b; k < e; k += 2) { stage(IC(0), false); stage(IC(1), false); } }
  };
  constexpr int END = KH % 2 == 0 ? NK - 1 : NK - 2;
  static_assert(KH % 2 == 0 || (NK % 2 == 0 && (M1 < 0 || M1 % 2 == 0) && (M2 < 0 || M2 % 2 == 0)), "");
  if constexpr (M1 < 0) run(0, END);
  else if constexpr (M2 < 0) { run(0, M1); mid(IC(0)); run(M1, END); }
  else { run(0, M1); mid(IC(0)); run(M1, M2); mid(IC(1)); run(M2, END); }
  if constexpr (KH % 2 == 0) stage(IC(0), true);
  else { stage(IC(0), false); stage(IC(1), true); }
}

struct TrUnit { u32x2 v[8]; };
DEVI void tr_load(TrUnit& t, const u16* W, int N, int k0, int n0, int lane) {
  const unsigned vo = (unsigned)(((8 * (lane >> 4)) * N + 4 * (lane & 15)) * 2);
  const char* b = (const char*)(W + (size_t)k0 * N + n0);
#pragma unroll
  for (int i = 0; i < 8; ++i) asm volatile("global_load_dwordx2 %0, %1, %2 nt" : "=v"(t.v[i]) : "v"(vo), "s"(b + (size_t)i * N * 2));
}
DEVI void tr_wait(TrUnit& t) {
  asm volatile("s_waitcnt vmcnt(0)" : "+v"(t.v[0]), "+v"(t.v[1]), "+v"(t.v[2]), "+v"(t.v[3]), "+v"(t.v[4]), "+v"(t.v[5]), "+v"(t.v[6]), "+v"(t.v[7])::"memory");
}
DEVI void tr_store(const TrUnit& t, u16* WT, int K, int k0, int n0, int lane) {
  const int src = (((lane & 3) << 4) | (lane >> 2)) << 2;
  u16* p = WT + (size_t)(n0 + 4 * (lane >> 2)) * K + k0 + 8 * (lane & 3);
  u32x4 o;
#define BP(v_) (unsigned)__builtin_amdgcn_ds_bpermute(src, (int)(v_))
#define TRP(W_, SEL) o.x = BP(__builtin_amdgcn_perm(t.v[1].W_, t.v[0].W_, SEL)); o.y = BP(__builtin_amdgcn_perm(t.v[3].W_, t.v[2].W_, SEL)); \
                     o.z = BP(__builtin_amdgcn_perm(t.v[5].W_, t.v[4].W_, SEL)); o.w = BP(__builtin_amdgcn_perm(t.v[7].W_, t.v[6].W_, SEL));
  TRP(x, 0x05040100u) gstore16c(p, o);
  TRP(x, 0x07060302u) gstore16c(p + K, o);
  TRP(y, 0x05040100u) gstore16c(p + 2 * K, o);
  TRP(y, 0x07060302u) gstore16c(p + 3 * K, o);
#undef TRP
#undef BP
}
DEVI float fadd_(float a, float b) { float r; asm("v_add_f32 %0, %1, %2" : "=v"(r) : "v"(a), "v"(b)); return r; }
template <int H> DEVI void tr_colsum(const TrUnit& t, float (&c)[4]) {
#pragma unroll
  for (int i = 4 * H; i < 4 * H + 4; ++i) {
    c[0] = fadd_(c[0], bflo(t.v[i].x)); c[1] = fadd_(c[1], bfhi(t.v[i].x)); c[2] = fadd_(c[2], bflo(t.v[i].y)); c[3] = fadd_(c[3], bfhi(t.v[i].y)); }
  if constexpr (H == 1) {
#pragma unroll
    for (int i = 0; i < 4; ++i) { c[i] = fadd_(c[i], __shfl_xor(c[i], 16)); c[i] = fadd_(c[i], __shfl_xor(c[i], 32)); }
  }
}
struct TrJob { const u16* src; u16* dst; int lds, ldd, k0, n0, cso; bool ok, cs; };
DEVI TrJob tr_job(int u, const u16* Wg, const u16* Wu, const u16* Wd, u16* WguT, u16* WdT) {
  TrJob j; j.ok = u < 1440; if (!j.ok) u = 1439;
  const int w = u / 480, r = u - w * 480;
  j.cs = w < 2;
  if (w < 2) { const int kb = r / 24; j.src = w ? Wu : Wg; j.dst = WguT + (size_t)w * FF * DM; j.lds = FF; j.ldd = DM; j.k0 = kb * 32; j.n0 = (r - kb * 24) * 64; j.cso = kb * 2 * FF + w * FF + j.n0; }
  else { const int kb = r / 10; j.src = Wd; j.dst = WdT; j.lds = DM; j.ldd = FF; j.k0 = kb * 32; j.n0 = (r - kb * 10) * 64; j.cso = 0; }
  return j;
}

using C1 = Cfg<128, 80, 4, 1, 1, 64, 6, 10, 3>;
using C2 = Cfg<80, 128, 1, 4, 1, 128, 3, 16, 2>;
using C3 = Cfg<192, 128, 2, 2, 1, 64, 4, 10, 2>;
using C4 = Cfg<80, 64, 1, 2, 2, 128, 4, 12, 4>;
constexpr int LDS_BYTES = 160 * 1024;
constexpr size_t WS_ABT = 0, WS_WGU = 6u << 20, WS_WD = 10u << 20, WS_ACT = 12u << 20, WS_XB = 18u << 20, WS_CSP = 24u << 20, WS_SYNC = 25u << 20,
                 WS_STATS = 26u << 20, WS_H = 27u << 20;

template <int W, class Mid> DEVI void k1_consumer(char* shm, float* wsum, char* stg, int lane, float sgn, Mid mid) {
  using C = C1;
  f32x4 acc[2][5];
  f32x4 sx[2] = {{0, 0, 0, 0}, {0, 0, 0, 0}}, sq[2] = {{0, 0, 0, 0}, {0, 0, 0, 0}}, sw = {0, 0, 0, 0}, sw4 = {0, 0, 0, 0};
  bf16x8 ones = {0x3f80, 0x3f80, 0x3f80, 0x3f80, 0x3f80, 0x3f80, 0x3f80, 0x3f80};
  asm volatile("" : "+v"(ones));
  consumer<C, SJ_AT>(acc, shm, W, lane, [&](bf16x8 (&R)[7]) {
    sx[0] = MFMA(R[0], ones, sx[0]); sx[1] = MFMA(R[1], ones, sx[1]);
    sq[0] = MFMA(R[0], R[0], sq[0]); sq[1] = MFMA(R[1], R[1], sq[1]);
    sw = MFMA(ones, R[2 + W], sw);
    if constexpr (W == 0) sw4 = MFMA(ones, R[6], sw4);
  }, mid);
  mid(IC(1));
  const int fr = lane & 15, fq = lane >> 4;
  if (lane < 16) { wsum[W * 16 + lane] = sw[0]; if constexpr (W == 0) wsum[64 + lane] = sw4[0]; }
  f32x4 rs[2], mr[2];
#pragma unroll
  for (int a = 0; a < 2; ++a)
#pragma unroll
    for (int r = 0; r < 4; ++r) {
      float s2 = __shfl(sq[a][r], 20 * fq + r);
      float mean = sx[a][r] * (1.0f / DM);
      float var = fmaxf(s2 * (1.0f / DM) - mean * mean, 0.0f);
      float rstd = sgn * __builtin_amdgcn_rsqf(var + 1e-5f);
      rs[a][r] = rstd; mr[a][r] = mean * rstd;
    }
  lg_bar();
#pragma unroll
  for (int b = 0; b < 5; ++b) {
    float w = wsum[b * 16 + fr];
    f32x4 v0 = acc[0][b] * rs[0] - w * mr[0], v1 = acc[1][b] * rs[1] - w * mr[1];
    u32x4 o; o.x = pack2(v0[0], v0[1]); o.y = pack2(v0[2], v0[3]); o.z = pack2(v1[0], v1[1]); o.w = pack2(v1[2], v1[3]);
    *(u32x4*)(stg + (b * 16 + fr) * 272 + W * 64 + fq * 16) = o;
  }
}
DEVI u32x4 add8(f32x4 a0, f32x4 a1, u32x4 r) {
  u32x4 o;
  o.x = pack2(a0[0] + bflo(r.x), a0[1] + bfhi(r.x)); o.y = pack2(a0[2] + bflo(r.y), a0[3] + bfhi(r.y));
  o.z = pack2(a1[0] + bflo(r.z), a1[1] + bfhi(r.z)); o.w = pack2(a1[2] + bflo(r.w), a1[3] + bfhi(r.w));
  return o;
}
DEVI void gload16c(u32x4& d, unsigned voff, const char* sbase) { asm volatile("global_load_dwordx4 %0, %1, %2 sc1" : "=v"(d) : "v"(voff), "s"(sbase)); }
template <int NP, bool STATS>
DEVI void tile64_finish(const char* red, int lane, u32x4 r0, u32x4 r1, u16* outp, f32x2* stats) {
  const int ch = lane & 15; const bool act = ch < 10; const int chc = act ? ch : 9;
#pragma unroll
  for (int i = 0; i < 2; ++i) {
    const int row = (threadIdx.x >> 4) + i * 32;
    const char* p = red + row * 336 + chc * 32;
    f32x4 a0 = *(const f32x4*)p, a1 = *(const f32x4*)(p + 16);
    if constexpr (NP == 2) { a0 += *(const f32x4*)(p + 64 * 336); a1 += *(const f32x4*)(p + 16 + 64 * 336); }
    const u32x4 o = add8(a0, a1, i == 0 ? r0 : r1);
    if (act) { if constexpr (STATS) gstore16c(outp + (size_t)row * DM + ch * 8, o); else *(u32x4*)(outp + (size_t)row * DM + ch * 8) = o; }
    if constexpr (STATS) {
      float v[8] = {bflo(o.x), bfhi(o.x), bflo(o.y), bfhi(o.y), bflo(o.z), bfhi(o.z), bflo(o.w), bfhi(o.w)};
      float s = ((v[0] + v[1]) + (v[2] + v[3])) + ((v[4] + v[5]) + (v[6] + v[7]));
      s = red16(act ? s : 0.0f);
      const float mu = s * (1.0f / 80);
      float m2 = 0;
#pragma unroll
      for (int e = 0; e < 8; ++e) { float d = v[e] - mu; m2 += d * d; }
      m2 = red16(act ? m2 : 0.0f);
      if (ch == 0) __hip_atomic_store((u64*)(stats + row * 8), __builtin_bit_cast(u64, f32x2{s, m2}), __ATOMIC_RELAXED, __HIP_MEMORY_SCOPE_AGENT);
    }
  }
}
template <int N> DEVI void flag_wait_all(const u64* f, u64 tag, int lane) {
  for (unsigned spin = 0; spin < (1u << 16); ++spin) {
    bool ok = true;
#pragma unroll
    for (int i = 0; i < N; ++i) ok &= __hip_atomic_load(f + i * 64 + lane, __ATOMIC_RELAXED, __HIP_MEMORY_SCOPE_AGENT) == tag;
    if (__all(ok)) break;
    __builtin_amdgcn_s_sleep(2);
  }
  asm volatile("" ::: "memory");
}
DEVI void drain_bar() { asm volatile("s_waitcnt vmcnt(0) lgkmcnt(0)\n\ts_barrier" ::: "memory"); }

extern "C" __global__ __launch_bounds__(512, 2) void fnet_geglu_fused(const u16* __restrict__ x, const u16* __restrict__ Cr, const u16* __restrict__ Ci,
                                                           const u16* __restrict__ Wre, const u16* __restrict__ Wim, const u16* __restrict__ Wg,
                                                           const u16* __restrict__ Wu, const u16* __restrict__ Wd, char* ws, u16* __restrict__ out) {
  extern __shared__ __attribute__((aligned(16))) char shm[];
  const int bid = blockIdx.x, xcd = bid & 7, idx = bid >> 3;
  const int wid = __builtin_amdgcn_readfirstlane(threadIdx.x >> 6), lane = threadIdx.x & 63;
  u16* abT = (u16*)(ws + WS_ABT); u16* WguT = (u16*)(ws + WS_WGU); u16* WdT = (u16*)(ws + WS_WD); u16* act = (u16*)(ws + WS_ACT);
  char* xb_ = ws + WS_XB; float* csp = (float*)(ws + WS_CSP); u16* h = (u16*)(ws + WS_H); f32x2* stats = (f32x2*)(ws + WS_STATS);
  u64* gen = (u64*)(ws + WS_SYNC); u64* flags1 = gen + 64; u64* flags2 = flags1 + 256; u64* flags3 = flags2 + 256; u64* flagsS = flags3 + 256; u64* flagsT = flagsS + 1024; u64* flagsX = flagsT + 1024;
  u64 tag = *(const __attribute__((address_space(4))) u64*)(size_t)gen;

  TrUnit tu[2];
  {
    using C = C1;
    const int ts = (xcd >> 1) * 4 + (idx >> 3), te = (xcd & 1) * 8 + (idx & 7), half = te >> 3;
    constexpr int FREE = ((C::LASTSLOT + 1) % C::NST) * C::STG;
    float* wsum = (float*)(shm + FREE);
    char* stg = shm + FREE + 1024;
    if (__builtin_expect(wid >= 4, 1)) {
      const int lw = wid - 4, j = lane >> 3;
      const char* Wb = (const char*)(half ? Wim : Wre) + (size_t)(te & 7) * 80 * DM * 2;
      const char* xb = (const char*)x + (size_t)ts * 128 * DM * 2;
      auto sA = [](int p) { return (unsigned)((32 * (p >> 2) + 16 * (p & 1) + 4 * ((p >> 1) & 1)) * DM * 2); };
      auto sB = [](int p) { return (unsigned)(p * 8 * DM * 2); };
      auto ptrs = [&](int k, const char*& pa, const char*& pb) { pa = xb + k * 128; pb = Wb + k * 128; };
      const unsigned vA = (unsigned)((8 * (j >> 2) + (j & 3)) * DM * 2), vB = (unsigned)(j * DM * 2);
      const char* cp = (const char*)((xcd & 1) ? Ci : Cr) + (size_t)(((xcd >> 1) * 4 + (idx >> 3)) * 128 + (idx & 7) * 16 + lw * 4) * 4096;
      auto extra = [&](auto I) {
        constexpr int i = decltype(I)::value;
        glds16(lane * 16 + (i & 3) * 4096 + (i >> 2) * 1024, cp, C::LDS);
      };
      if (wid == 4) loader_x<C, 0, 4, 0, 3, PFX, 16>(lane, sA, vA, sB, vB, ptrs, extra);
      else if (wid == 5) loader_x<C, 4, 4, 3, 3, PFX, 16>(lane, sA, vA, sB, vB, ptrs, extra);
      else if (wid == 6) loader_x<C, 8, 4, 6, 2, PFX, 16>(lane, sA, vA, sB, vB, ptrs, extra);
      else loader_x<C, 12, 4, 8, 2, PFX, 16>(lane, sA, vA, sB, vB, ptrs, extra);
      lg_bar();
    } else {
      unsigned csink = 0;
      if (wid == 0) {
        unsigned long long t0, t1; int d0, d1;
        asm volatile("s_getpc_b64 %0\n\ts_mov_b32 %1, fnet_geglu_fused@rel32@lo+4" : "=s"(t0), "=s"(d0));
        asm volatile("s_getpc_b64 %0\n\ts_mov_b32 %1, fnet_geglu_end@rel32@lo+4" : "=s"(t1), "=s"(d1));
        t0 += (long long)d0; t1 += (long long)d1;
        const long long len = (long long)(t1 - t0);
        const unsigned off = (unsigned)((idx * 64 + lane) * 64);
        if ((long long)off < len) csink = *(const volatile unsigned*)((const char*)t0 + off);
        asm volatile("" ::"v"(csink));
      }
      const float sgn = half ? -1.0f : 1.0f;
      auto sjload = [&](auto M) {
        if constexpr (decltype(M)::value == 1) { tr_wait(tu[0]); tr_wait(tu[1]); return; }
        const TrJob j0 = tr_job(bid * 4 + wid, Wg, Wu, Wd, WguT, WdT), j1 = tr_job(1024 + bid * 4 + wid, Wg, Wu, Wd, WguT, WdT);
        tr_load(tu[0], j0.src, j0.lds, j0.k0, j0.n0, lane);
        tr_load(tu[1], j1.src, j1.lds, j1.k0, j1.n0, lane);
      };
      if (wid == 0) k1_consumer<0>(shm, wsum, stg, lane, sgn, sjload);
      else if (wid == 1) k1_consumer<1>(shm, wsum, stg, lane, sgn, sjload);
      else if (wid == 2) k1_consumer<2>(shm, wsum, stg, lane, sgn, sjload);
      else k1_consumer<3>(shm, wsum, stg, lane, sgn, sjload);
    }
    lg_bar();
    u16* ob = abT + (size_t)(te & 7) * 80 * 4096 + half * 2048 + ts * 128;
#pragma unroll
    for (int i = 0; i < 3; ++i) {
      int c = threadIdx.x + i * 512;
      if (i < 2 || c < 1280) { int row = c >> 4, ch = c & 15; gstore16c(ob + (size_t)row * 4096 + ch * 8, *(const u32x4*)(stg + row * 272 + ch * 16)); }
    }
    asm volatile("s_waitcnt vmcnt(0) lgkmcnt(0)\n\ts_barrier" : "+s"(tag)::"memory");
    tag += 1;
    if (threadIdx.x == 0) flag_set(flags1 + te * 16 + ts, tag);
  }
  {
    using C = C2;
    const int half = xcd & 1, tt = (xcd >> 1) * 4 + (idx >> 3), te = idx & 7;
    static_assert((C::NST - 1 - C::LASTSLOT) * C::STG >= 64 * 336, "");
    char* red = shm + (C::LASTSLOT + 1) * C::STG;
    u32x4 r0, r1;
    auto xload = [&]() {
      int ch = threadIdx.x & 15; if (ch > 9) ch = 9;
      const u16* rp = x + (size_t)(tt * 128 + half * 64 + (threadIdx.x >> 4)) * DM + te * 80 + ch * 8;
      r0 = *(const u32x4*)rp; r1 = *(const u32x4*)(rp + 32 * DM);
    };
    if (wid >= 4) {
      const int j = lane >> 3;
      const char* ab = (const char*)abT + (size_t)te * 80 * 8192 + half * 4096;
      const char* cc = (const char*)(half ? Ci : Cr) + (size_t)tt * 128 * 4096;
      auto sA = [](int p) { return (unsigned)(p * 8 * 8192); };
      auto sB = [](int p) { return (unsigned)(p * 8 * 4096); };
      auto ptrs = [&](int k, const char*& pa, const char*& pb) { pa = ab + k * (C::BK * 2); pb = cc + k * (C::BK * 2); };
      const unsigned vA = (unsigned)(j * 8192), vB = (unsigned)(j * 4096);
      auto pre = [&]() { if (wid == 4) flag_wait16(flags1 + (half * 8 + te) * 16, tag, lane); };
      if (wid == 4) loader_dep<C, 1, 0, 5, 0, 8>(lane, sA, vA, sB, vB, ptrs, pre);
      else if (wid == 5) loader_dep<C, 1, 10, 5, 16, 8>(lane, sA, vA, sB, vB, ptrs, pre);
      else if (wid == 6) loader_dep<C, 1, 5, 5, 8, 8>(lane, sA, vA, sB, vB, ptrs, pre);
      else loader_dep<C, 1, 15, 5, 24, 8>(lane, sA, vA, sB, vB, ptrs, pre);
      xload();
      if (wid == 4) flag_wait_all<16>(flagsS, tag, lane);
    } else {
      { const TrJob jb = tr_job(bid * 4 + wid, Wg, Wu, Wd, WguT, WdT);
        tr_store(tu[0], jb.dst, jb.ldd, jb.k0, jb.n0, lane);
        bar();
        if (jb.cs) {
          float c[4] = {0.f, 0.f, 0.f, 0.f}; tr_colsum<0>(tu[0], c); tr_colsum<1>(tu[0], c);
          if (lane < 16) gstore16c(csp + jb.cso + 4 * lane, __builtin_bit_cast(u32x4, f32x4{c[0], c[1], c[2], c[3]}));
        } }
      f32x4 acc[5][2];
      consumer<C, C::NK / 2>(acc, shm, wid, lane, [](auto&) {}, [&](auto) {
        asm volatile("s_waitcnt vmcnt(0)" ::: "memory");
        if (lane == 0) flag_set(flagsS + bid * 4 + wid, tag);
        xload();
      });
      const int slot = (tt * 8 + te) * 4 + wid;
      char* xp = xb_ + (size_t)slot * 10240;
      if ((wid >> 1) != half) {
        asm volatile("s_nop 15\n\ts_nop 7" : "+v"(acc[0][0]), "+v"(acc[1][0]), "+v"(acc[2][0]), "+v"(acc[3][0]), "+v"(acc[4][0]),
                     "+v"(acc[0][1]), "+v"(acc[1][1]), "+v"(acc[2][1]), "+v"(acc[3][1]), "+v"(acc[4][1]));
#pragma unroll
        for (int n = 0; n < 2; ++n)
#pragma unroll
          for (int j = 0; j < 5; ++j) gstore16c(xp + (n * 5 + j) * 1024 + lane * 16, __builtin_bit_cast(u32x4, acc[j][n]));
        asm volatile("s_waitcnt vmcnt(0)" ::: "memory");
        if (lane == 0) flag_set(flagsX + slot, tag);
      } else {
        flag_wait1(flagsX + slot, tag);
        u32x4 o[10];
#pragma unroll
        for (int i = 0; i < 10; ++i) gload16c(o[i], (unsigned)(i * 1024 + lane * 16), xp);
        asm volatile("s_waitcnt vmcnt(0)" : "+v"(o[0]), "+v"(o[1]), "+v"(o[2]), "+v"(o[3]), "+v"(o[4]), "+v"(o[5]), "+v"(o[6]), "+v"(o[7]), "+v"(o[8]), "+v"(o[9])::"memory");
        const int fr = lane & 15, fq = lane >> 4;
#pragma unroll
        for (int n = 0; n < 2; ++n)
#pragma unroll
          for (int j = 0; j < 5; ++j)
            *(f32x4*)(red + (((wid & 1) * 2 + n) * 16 + fr) * 336 + j * 64 + fq * 16) = acc[j][n] + __builtin_bit_cast(f32x4, o[n * 5 + j]);
      }
    }
    lg_bar();
    tile64_finish<1, true>(red, lane, r0, r1, h + (size_t)(tt * 128 + half * 64) * DM + te * 80, stats + (tt * 128 + half * 64) * 8 + te);
    drain_bar();
    if (threadIdx.x == 0) flag_set(flags2 + tt * 16 + half * 8 + te, tag);
  }
  {
    using C = C3;
    const int tm = (xcd >> 2) * 8 + (idx >> 2), tn = (xcd & 3) * 4 + (idx & 3);
    constexpr int FREE = ((C::LASTSLOT + 1) % C::NST) * C::STG;
    f32x2* rowst = (f32x2*)(shm + FREE);
    float* lcs = (float*)(shm + FREE + 1024);
    char* stg = shm + FREE + 4096;
    auto geglu2 = [&](f32x4 (&a)[6][2], int w, int n0) {
      const int fr = lane & 15, fq = lane >> 4, wa = w >> 1, wb = w & 1;
      f32x4 cs[6];
#pragma unroll
      for (int j = 0; j < 6; ++j) cs[j] = *(const f32x4*)(lcs + wa * 96 + j * 16 + 4 * fq);
#pragma unroll
      for (int n = 0; n < 2; ++n) {
        const int ml = (wb * 4 + n0 + n) * 16 + fr;
        const f32x2 st = rowst[ml];
        const float nmean = st[0], ve = st[1];
        const float rstd = __builtin_amdgcn_rsqf(ve);
        const float k1 = -2.3022081985f * rstd;
        const float k2 = k1 * 0.044715f * rstd * rstd;
        f32x4 G[3], U[3], z[3];
#pragma unroll
        for (int p = 0; p < 3; ++p) G[p] = cs[2 * p] * nmean + a[2 * p][n];
#pragma unroll
        for (int p = 0; p < 3; ++p) z[p] = G[p] * G[p];
#pragma unroll
        for (int p = 0; p < 3; ++p) U[p] = cs[2 * p + 1] * nmean + a[2 * p + 1][n];
#pragma unroll
        for (int p = 0; p < 3; ++p) z[p] = z[p] * k2 + k1;
#pragma unroll
        for (int p = 0; p < 3; ++p) z[p] = z[p] * G[p];
#pragma unroll
        for (int p = 0; p < 3; ++p) { z[p][0] = __builtin_amdgcn_exp2f(z[p][0]); z[p][1] = __builtin_amdgcn_exp2f(z[p][1]); z[p][2] = __builtin_amdgcn_exp2f(z[p][2]); z[p][3] = __builtin_amdgcn_exp2f(z[p][3]); U[p] = U[p] * G[p]; }
#pragma unroll
        for (int p = 0; p < 3; ++p) z[p] = z[p] * ve + ve;
#pragma unroll
        for (int p = 0; p < 3; ++p) { z[p][0] = __builtin_amdgcn_rcpf(z[p][0]); z[p][1] = __builtin_amdgcn_rcpf(z[p][1]); z[p][2] = __builtin_amdgcn_rcpf(z[p][2]); z[p][3] = __builtin_amdgcn_rcpf(z[p][3]); }
#pragma unroll
        for (int p = 0; p < 3; ++p) {
          f32x4 v = U[p] * z[p];
          u32x2 o; o.x = pack2(v[0], v[1]); o.y = pack2(v[2], v[3]);
          *(u32x2*)(stg + ml * 208 + (wa * 48 + p * 16 + fq * 4) * 2) = o;
        }
      }
    };
    static_assert(C::LASTSLOT == 1 && C::NST == 4, "");
    char* hand = shm + (((wid >> 1) & 1) ? 3 : 0) * C::STG + (wid & 1) * 12288 + lane * 16;
    f32x4 ah[6][2];
    if (wid >= 4) {
      const char* wbp = (const char*)WguT + (size_t)tn * 96 * DM * 2;
      const char* hb = (const char*)h + (size_t)tm * 128 * DM * 2;
      const unsigned v = (unsigned)((lane >> 3) * DM * 2);
      auto sA = [](int p) { return (unsigned)((((p >> 1) & 1) * FF + 16 * (p >> 2) + 8 * (p & 1)) * DM * 2); };
      auto sB = [](int p) { return (unsigned)(p * 8 * DM * 2); };
      auto ptrs = [&](int k, const char*& pa, const char*& pb) { pa = wbp + k * 128; pb = hb + k * 128; };
      if (wid == 4) loader_dep<C, 2, 0, 6, 0, 4>(lane, sA, v, sB, v, ptrs, []() {});
      else if (wid == 5) loader_dep<C, 2, 6, 6, 4, 4>(lane, sA, v, sB, v, ptrs, []() {});
      else if (wid == 6) loader_dep<C, 2, 12, 6, 8, 4>(lane, sA, v, sB, v, ptrs, []() {});
      else loader_dep<C, 2, 18, 6, 12, 4>(lane, sA, v, sB, v, ptrs, []() {});
      if (wid == 4) flag_wait_all<7>(flagsT, tag, lane);
      lg_bar();
#pragma unroll
      for (int n = 0; n < 2; ++n)
#pragma unroll
        for (int j = 0; j < 6; ++j) ah[j][n] = *(const f32x4*)(hand + (n * 6 + j) * 1024);
      geglu2(ah, wid - 4, 2);
    } else {
      float cpart = 0;
      { const TrJob jb = tr_job(1024 + bid * 4 + wid, Wg, Wu, Wd, WguT, WdT);
        if (jb.ok) tr_store(tu[1], jb.dst, jb.ldd, jb.k0, jb.n0, lane); }
      if (wid < 3) {
        const int r = wid * 64 + lane;
        const float* cq = csp + ((r >> 4) & 1) * FF + tn * 96 + 16 * (r >> 5) + (r & 15);
        float t[20];
#pragma unroll
        for (int i = 0; i < 20; ++i) t[i] = __hip_atomic_load(cq + i * 2 * FF, __ATOMIC_RELAXED, __HIP_MEMORY_SCOPE_AGENT);
#pragma unroll
        for (int i = 0; i < 20; ++i) cpart += t[i];
        asm volatile("" : "+v"(cpart));
      } else {
        flag_wait16(flags2 + tm * 16, tag, lane);
      }
      bar();
      if (bid == 0 && threadIdx.x == 0) *(volatile u64*)gen = tag;
      u32x4 sp[2];
      gload16c(sp[0], (unsigned)(lane * 16), (const char*)(stats + (size_t)(tm * 128 + wid * 32) * 8));
      gload16c(sp[1], (unsigned)(lane * 16 + 1024), (const char*)(stats + (size_t)(tm * 128 + wid * 32) * 8));
      f32x4 acc[6][4];
      consumer<C, 4>(acc, shm, wid, lane, [](auto&) {}, [&](auto) {
        asm volatile("s_waitcnt vmcnt(0)" : "+v"(sp[0]), "+v"(sp[1])::"memory");
        if (lane == 0) flag_set(flagsT + bid * 4 + wid, tag);
      });
      asm volatile("s_waitcnt vmcnt(0)" : "+v"(sp[0]), "+v"(sp[1])::"memory");
#pragma unroll
      for (int i = 0; i < 2; ++i) {
        const f32x4 s4 = __builtin_bit_cast(f32x4, sp[i]);
        float sm = s4[0] + s4[2]; sm += dppf<0xB1>(sm); sm += dppf<0x4E>(sm);
        const float mean = sm * (1.0f / DM);
        const float d0 = s4[0] * (1.0f / 80) - mean, d1 = s4[2] * (1.0f / 80) - mean;
        float m2 = s4[1] + s4[3] + 80.0f * (d0 * d0 + d1 * d1);
        m2 += dppf<0xB1>(m2); m2 += dppf<0x4E>(m2);
        if ((lane & 3) == 0) rowst[wid * 32 + i * 16 + (lane >> 2)] = f32x2{-mean, m2 * (1.0f / DM) + 1e-5f};
      }
      if (wid < 3) lcs[wid * 64 + lane] = cpart;
#pragma unroll
      for (int n = 0; n < 2; ++n)
#pragma unroll
        for (int j = 0; j < 6; ++j) *(f32x4*)(hand + (n * 6 + j) * 1024) = acc[j][2 + n];
      lg_bar();
#pragma unroll
      for (int n = 0; n < 2; ++n)
#pragma unroll
        for (int j = 0; j < 6; ++j) ah[j][n] = acc[j][n];
      geglu2(ah, wid, 0);
    }
    lg_bar();
    u16* ob = act + (size_t)tm * 128 * FF + tn * 96;
#pragma unroll
    for (int i = 0; i < 3; ++i) {
      int c = threadIdx.x + i * 512, row = c / 12, ch = c % 12;
      gstore16c(ob + (size_t)row * FF + ch * 8, *(const u32x4*)(stg + row * 208 + ch * 16));
    }
    drain_bar();
    if (threadIdx.x == 0) flag_set(flags3 + tm * 16 + tn, tag);
  }
  {
    using C = C4;
    const int tm4 = (xcd >> 1) * 8 + (idx >> 2), td = (xcd & 1) * 4 + (idx & 3);
    static_assert(C::LASTSLOT == C::NST - 1 && (C::NST - 1) * C::STG >= 2 * 64 * 336, "");
    u32x4 r0, r1;
    auto rload = [&]() {
      int ch = threadIdx.x & 15; if (ch > 9) ch = 9;
      const unsigned o = (unsigned)(((tm4 * 64 + (threadIdx.x >> 4)) * DM + td * 80 + ch * 8) * 2);
      gload16c(r0, o, (const char*)h); gload16c(r1, o + 32 * DM * 2, (const char*)h);
    };
    if (wid >= 4) {
      const char* wp = (const char*)WdT + (size_t)td * 80 * FF * 2;
      const char* ap = (const char*)act + (size_t)tm4 * 64 * FF * 2;
      const unsigned v = (unsigned)((lane >> 3) * FF * 2);
      auto sA = [](int p) { return (unsigned)(p * 8 * FF * 2); };
      auto ptrs = [&](int k, const char*& pa, const char*& pb) { pa = wp + k * 256; pb = ap + k * 256; };
      if (wid == 4) loader_dep<C, 2, 0, 5, 0, 4>(lane, sA, v, sA, v, ptrs, []() {});
      else if (wid == 5) loader_dep<C, 2, 5, 5, 4, 4>(lane, sA, v, sA, v, ptrs, []() {});
      else if (wid == 6) loader_dep<C, 2, 10, 5, 8, 4>(lane, sA, v, sA, v, ptrs, []() {});
      else loader_dep<C, 2, 15, 5, 12, 4>(lane, sA, v, sA, v, ptrs, []() {});
      rload();
    } else {
      if (wid == 0) flag_wait16(flags3 + (tm4 >> 1) * 16, tag, lane);
      bar();
      rload();
      f32x4 acc[5][2];
      consumer<C>(acc, shm, wid, lane, [](auto&) {}, [](auto) {});
      const int fr = lane & 15, fq = lane >> 4, kg = wid >> 1, wb = wid & 1;
#pragma unroll
      for (int n = 0; n < 2; ++n)
#pragma unroll
        for (int j = 0; j < 5; ++j) *(f32x4*)(shm + kg * (64 * 336) + ((wb * 2 + n) * 16 + fr) * 336 + j * 64 + fq * 16) = acc[j][n];
    }
    asm volatile("s_waitcnt vmcnt(0) lgkmcnt(0)\n\ts_barrier" : "+v"(r0), "+v"(r1)::"memory");
    tile64_finish<2, false>(shm, lane, r0, r1, out + (size_t)tm4 * 64 * DM + td * 80, nullptr);
  }
}

extern "C" __global__ void fnet_geglu_end() {}

extern "C" void kernel_launch(void* const* d_in, const int* in_sizes, int n_in, void* d_out, int out_size, void* d_ws, size_t ws_size,
                              hipStream_t stream) {
  (void)hipFuncSetAttribute((const void*)fnet_geglu_fused, hipFuncAttributeMaxDynamicSharedMemorySize, LDS_BYTES);
  fnet_geglu_fused<<<256, 512, LDS_BYTES, stream>>>((const u16*)d_in[0], (const u16*)d_in[1], (const u16*)d_in[2], (const u16*)d_in[3],
                                                    (const u16*)d_in[4], (const u16*)d_in[6], (const u16*)d_in[7], (const u16*)d_in[8],
                                                    (char*)d_ws, (u16*)d_out);
}
```
